```python
import torch

def _softmax(x, axis=-1):
    return torch.softmax(x, dim=axis)
def _gelu(x):
    return torch.nn.functional.gelu(x, approximate="tanh")
def _silu(x):
    return torch.nn.functional.silu(x)
def _sigmoid(x):
    return torch.sigmoid(x)
def _layernorm(x, axis=-1, eps=1e-5):
    m = x.mean(dim=axis, keepdim=True)
    v = x.var(dim=axis, keepdim=True, unbiased=False)
    return (x - m) / torch.sqrt(v + eps)
def _scatter_add(tgt, idx, src, axis=0):
    return tgt.clone().index_add_(axis, idx, src)
def _segment_sum(src, seg, n):
    out = torch.zeros((n,) + src.shape[1:], dtype=src.dtype)
    return out.index_add_(0, seg, src)
def _pad(x, pw):
    flat = tuple(v for pair in reversed(pw) for v in pair)
    return torch.nn.functional.pad(x, flat)

kernel_name = 'concat_heads__gqa_block_cached__mha_block_cached__near_ridge'

def setup_inputs():

    _w = (lambda a: (lambda t: t.to(torch.bfloat16) if t.is_floating_point() else t)(torch.as_tensor(a)))
    def _A_setup():
        B, Sq, D = 16, 48, 512
        Hq, Hkv, Sk, Dh = 32, 8, 512, 128
        assert Hq % Hkv == 0, (Hq, Hkv)
        return {
            "x": _w((lambda *s: torch.randn(*s, dtype=torch.bfloat16) * 0.8)(B, Sq, D)),
            "k_cache": _w((lambda *s: torch.randn(*s, dtype=torch.bfloat16) * 0.8)(B, Hkv, Sk, Dh)),
            "v_cache": _w((lambda *s: torch.randn(*s, dtype=torch.bfloat16) * 0.05)(B, Hkv, Sk, Dh)),
            "wq": _w((lambda *s: torch.randn(*s, dtype=torch.bfloat16) * 0.8)(D, Hq * Dh) * (D ** -0.5)),
            "wo": _w((lambda *s: torch.randn(*s, dtype=torch.bfloat16) * 0.05)(Hq * Dh, D) * ((Hq * Dh) ** -0.5)),
        }
    _A_in = _A_setup()

    def _B_setup():
        B, Sq, D = 16, 48, 512
        H, Sk, Dh = 32, 512, 128
        return {
            "x": _w((lambda *s: torch.randn(*s, dtype=torch.bfloat16) * 0.8)(B, Sq, D)),
            "k_cache": _w((lambda *s: torch.randn(*s, dtype=torch.bfloat16) * 0.8)(B, H, Sk, Dh)),
            "v_cache": _w((lambda *s: torch.randn(*s, dtype=torch.bfloat16) * 0.05)(B, H, Sk, Dh)),
            "wq": _w((lambda *s: torch.randn(*s, dtype=torch.bfloat16) * 0.8)(D, H * Dh) * (D ** -0.5)),
            "wo": _w((lambda *s: torch.randn(*s, dtype=torch.bfloat16) * 0.05)(H * Dh, D) * ((H * Dh) ** -0.5)),
        }
    _B_in = _B_setup()

    return {
        "A_x": _A_in["x"],
        "A_k_cache": _A_in["k_cache"],
        "A_v_cache": _A_in["v_cache"],
        "A_wq": _A_in["wq"],
        "A_wo": _A_in["wo"],
        "B_x": _B_in["x"],
        "B_k_cache": _B_in["k_cache"],
        "B_v_cache": _B_in["v_cache"],
        "B_wq": _B_in["wq"],
        "B_wo": _B_in["wo"],
    }

def reference(A_x, A_k_cache, A_v_cache, A_wq, A_wo, B_x, B_k_cache, B_v_cache, B_wq, B_wo):
    def _A_ref(x, k_cache, v_cache, wq, wo):
        B, Sq, D = x.shape
        Hq, Dh = 32, 128
        Hkv = k_cache.shape[1]
        rep = Hq // Hkv
        q = (lambda x, *d: x.permute(*d))((x @ wq).reshape(B, Sq, Hq, Dh), 0, 2, 1, 3)
        k = (lambda x, n, axis: torch.repeat_interleave(x, n, dim=axis))(k_cache, rep, axis=1)
        v = (lambda x, n, axis: torch.repeat_interleave(x, n, dim=axis))(v_cache, rep, axis=1)
        scores = torch.einsum("bhqd,bhkd->bhqk", q, k) * (Dh ** -0.5)
        p = _softmax(scores, axis=-1)
        o = torch.einsum("bhqk,bhkd->bhqd", p, v)
        return (lambda x, *d: x.permute(*d))(o, 0, 2, 1, 3).reshape(B, Sq, Hq * Dh) @ wo

    def _B_ref(x, k_cache, v_cache, wq, wo):
        B, Sq, D = x.shape
        H, Dh = 32, 128
        q = (lambda x, *d: x.permute(*d))((x @ wq).reshape(B, Sq, H, Dh), 0, 2, 1, 3)
        scores = torch.einsum("bhqd,bhkd->bhqk", q, k_cache) * (Dh ** -0.5)
        p = _softmax(scores, axis=-1)
        o = torch.einsum("bhqk,bhkd->bhqd", p, v_cache)
        return (lambda x, *d: x.permute(*d))(o, 0, 2, 1, 3).reshape(B, Sq, H * Dh) @ wo

    a = _A_ref(A_x, A_k_cache, A_v_cache, A_wq, A_wo)
    b = _B_ref(B_x, B_k_cache, B_v_cache, B_wq, B_wo)
    return torch.concatenate([a, b], axis=-1)
```

```cpp
#include <hip/hip_runtime.h>
#include <hip/hip_bf16.h>
typedef __hip_bfloat16 bf16;
__device__ inline float b2f(bf16 v){return __bfloat162float(v);}
__global__ void k_q(const bf16* x,const bf16* w,bf16* q){
  int n=blockIdx.x*256+threadIdx.x; int m=blockIdx.y;
  float a=0; for(int k=0;k<512;k++) a+=b2f(x[m*512+k])*b2f(w[(size_t)k*4096+n]);
  q[(size_t)m*4096+n]=__float2bfloat16(a);
}
__global__ void k_attn(const bf16* q,const bf16* kc,const bf16* vc,bf16* o,int hkv){
  int s=blockIdx.x,h=blockIdx.y,b=blockIdx.z; int t=threadIdx.x;
  int kh=h/(32/hkv);
  __shared__ float qs[128]; __shared__ float p[512]; __shared__ float red[128];
  qs[t]=b2f(q[(size_t)(b*48+s)*4096+h*128+t]); __syncthreads();
  const bf16* K=kc+(size_t)(b*hkv+kh)*512*128; const bf16* V=vc+(size_t)(b*hkv+kh)*512*128;
  float mx=-1e30f;
  for(int j=t;j<512;j+=128){float a=0;for(int d=0;d<128;d++)a+=qs[d]*b2f(K[j*128+d]); a*=0.08838834764831845f; p[j]=a; mx=fmaxf(mx,a);}
  red[t]=mx;__syncthreads();
  for(int i=64;i>0;i>>=1){if(t<i)red[t]=fmaxf(red[t],red[t+i]);__syncthreads();}
  mx=red[0];__syncthreads();
  float sm=0; for(int j=t;j<512;j+=128){float e=__expf(p[j]-mx);p[j]=e;sm+=e;}
  red[t]=sm;__syncthreads();
  for(int i=64;i>0;i>>=1){if(t<i)red[t]+=red[t+i];__syncthreads();}
  sm=red[0];
  float a=0; for(int j=0;j<512;j++)a+=p[j]*b2f(V[j*128+t]);
  o[(size_t)(b*48+s)*4096+h*128+t]=__float2bfloat16(a/sm);
}
__global__ void k_o(const bf16* o,const bf16* w,bf16* out,int off){
  int n=blockIdx.x*256+threadIdx.x; int m=blockIdx.y;
  float a=0; for(int k=0;k<4096;k++) a+=b2f(o[(size_t)m*4096+k])*b2f(w[(size_t)k*512+n]);
  out[(size_t)m*1024+off+n]=__float2bfloat16(a);
}
extern "C" void kernel_launch(void* const* d_in, const int* in_sizes, int n_in, void* d_out, int out_size, void* d_ws, size_t ws_size, hipStream_t stream){
  bf16* q=(bf16*)d_ws; bf16* o=q+768*4096;
  for(int p=0;p<2;p++){
    const bf16* x=(const bf16*)d_in[p*5+0];const bf16* k=(const bf16*)d_in[p*5+1];const bf16* v=(const bf16*)d_in[p*5+2];
    const bf16* wq=(const bf16*)d_in[p*5+3];const bf16* wo=(const bf16*)d_in[p*5+4];
    k_q<<<dim3(16,768),256,0,stream>>>(x,wq,q);
    k_attn<<<dim3(48,32,16),128,0,stream>>>(q,k,v,o,p==0?8:32);
    k_o<<<dim3(2,768),256,0,stream>>>(o,wo,(bf16*)d_out,p*512);
  }
}
```

```cpp
#include <hip/hip_runtime.h>
#include <hip/hip_bf16.h>
#include <stdint.h>

typedef __bf16 bf16x8 __attribute__((ext_vector_type(8)));
typedef __bf16 bf16x4 __attribute__((ext_vector_type(4)));
typedef float f32x4 __attribute__((ext_vector_type(4)));
typedef float f32x2 __attribute__((ext_vector_type(2)));
typedef unsigned u32x4 __attribute__((ext_vector_type(4)));
typedef unsigned u32x2 __attribute__((ext_vector_type(2)));
#define LAS __attribute__((address_space(3)))
#define DI __device__ __forceinline__

#define KV_NT 1
#define WARM 3
#define ORDER 2

constexpr unsigned SLOT = 16384, NSLOT = 7, RING = SLOT * NSLOT, SCR = RING, LDS_BYTES = 163840;
constexpr float CEXP = 0.08838834764831845f * 1.4426950408889634f;
constexpr size_t WS_PART = 4096;

DI f32x4 mfma(bf16x8 a, bf16x8 b, f32x4 c) { return __builtin_amdgcn_mfma_f32_16x16x32_bf16(a, b, c, 0, 0, 0); }
DI float fmx(float a, float b) { return __builtin_elementwise_maximum(a, b); }
DI bf16x8 lds_b128(unsigned a) { return *(const LAS bf16x8*)(uintptr_t)a; }
DI bf16x4 lds_tr(unsigned a) { return __builtin_amdgcn_ds_read_tr16_b64_v4bf16((LAS bf16x4*)(uintptr_t)a); }
DI bf16x8 cat(bf16x4 a, bf16x4 b) { return __builtin_shufflevector(a, b, 0, 1, 2, 3, 4, 5, 6, 7); }
DI bf16x4 cvt4(f32x4 v) { return __builtin_convertvector(v, bf16x4); }
DI unsigned fswz(unsigned r) { return (((r & 7) ^ ((r & 8) >> 1)) << 1) | (r >> 3); }
template <int N> DI void vmwait() { asm volatile("s_waitcnt vmcnt(%0)" ::"i"(N) : "memory"); }
DI void vmwait_dyn(int n) {
  if (n >= 20) { if (n >= 32) vmwait<32>(); else if (n >= 24) vmwait<24>(); else vmwait<20>(); }
  else if (n >= 12) { if (n >= 16) vmwait<16>(); else vmwait<12>(); }
  else if (n >= 4) { if (n >= 8) vmwait<8>(); else vmwait<4>(); }
  else vmwait<0>();
}
DI void bar() { asm volatile("s_waitcnt lgkmcnt(0)\n\ts_barrier" ::: "memory"); }
template <bool NT> DI void dma4(const char* sb, unsigned m0v, unsigned v0, unsigned v1, unsigned v2, unsigned v3) {
  if constexpr (NT)
    asm volatile("s_mov_b32 m0, %1\n\ts_nop 3\n\tglobal_load_lds_dwordx4 %2, %0 nt\n\tglobal_load_lds_dwordx4 %3, %0 offset:1024 nt\n\t"
                 "global_load_lds_dwordx4 %4, %0 offset:2048 nt\n\tglobal_load_lds_dwordx4 %5, %0 offset:3072 nt"
                 ::"s"(sb), "s"(m0v), "v"(v0), "v"(v1), "v"(v2), "v"(v3) : "memory");
  else
    asm volatile("s_mov_b32 m0, %1\n\ts_nop 3\n\tglobal_load_lds_dwordx4 %2, %0\n\tglobal_load_lds_dwordx4 %3, %0 offset:1024\n\t"
                 "global_load_lds_dwordx4 %4, %0 offset:2048\n\tglobal_load_lds_dwordx4 %5, %0 offset:3072"
                 ::"s"(sb), "s"(m0v), "v"(v0), "v"(v1), "v"(v2), "v"(v3) : "memory");
}
template <class T> DI void keep(const T& v) { asm volatile("" ::"v"(v) : "memory"); }
#define STAMP() do {} while (0)
DI unsigned ring_next(unsigned p) { p += SLOT; return p == RING ? 0u : p; }

__global__ __launch_bounds__(256, 1) void fused_block(
    const char* __restrict__ xA, const char* __restrict__ kA, const char* __restrict__ vA, const char* __restrict__ wqA, const char* __restrict__ woA,
    const char* __restrict__ xB, const char* __restrict__ kB, const char* __restrict__ vB, const char* __restrict__ wqB, const char* __restrict__ woB,
    char* __restrict__ out, char* __restrict__ ws) {
  const int wg = blockIdx.x, tid = threadIdx.x;
  const int w = __builtin_amdgcn_readfirstlane(tid >> 6);
  const unsigned l = tid & 63, i = l & 15, g = l >> 4, q = i >> 2, pp = l & 3;
  const int j = wg & 7, b = wg >> 4, hf = (wg >> 3) & 1, g0 = 4 * j + 2 * hf, u = w >> 1, kh = w & 1;
  const int first = ORDER == 0 ? 1 : ORDER == 1 ? 0 : (b & 1);

  STAMP();
  unsigned vKV[4], vWo[4], vWq[4];
#pragma unroll
  for (int a = 0; a < 4; ++a) {
    const unsigned ch = (i ^ fswz(4 * a + g)) << 4;
    vKV[a] = g * 256 + ch;
    vWo[a] = g * 1024 + ch + 3072 * a;
    const unsigned r = 8 * a + (l >> 3), c = l & 7, f = ((r >> 1) & 1) | (((r >> 3) & 1) << 1);
    vWq[a] = (l >> 3) * 8192 + (((((c >> 1) ^ f) << 1) | (c & 1)) << 4) + (65536 - 1024) * a;
  }
  const unsigned pK0 = i * 256 + ((g ^ fswz(i)) << 4);
  const unsigned pV0 = (4 * g + q) * 256 + (((pp >> 1) ^ fswz(4 * g + q)) << 4) + 8 * (pp & 1);
  const unsigned pW0 = (8 * g + q) * 128 + ((((q >> 1) & 1) | ((g & 1) << 1)) << 5) + 8 * pp;
  const unsigned pX0 = SCR + i * 1024 + ((g ^ i) << 4);

  const size_t wqoff = (size_t)(g0 * 128 + 64 * w) * 2, wooff = (size_t)g0 * 131072 + 256 * w;
  const size_t kvoffA = ((size_t)b * 8 + j) * 131072 + w * 4096, kvoffB = ((size_t)b * 32 + g0 + u) * 131072 + kh * 4096;

  auto load_x = [&](const char* x) {
#pragma unroll
    for (int m = 0; m < 3; ++m) {
      const unsigned r0 = 12 * w + 4 * m;
      dma4<false>(x + (size_t)b * 49152 + r0 * 1024, SCR + r0 * 1024, (l ^ (r0 & 15)) << 4, (l ^ ((r0 + 1) & 15)) << 4, (l ^ ((r0 + 2) & 15)) << 4, (l ^ ((r0 + 3) & 15)) << 4);
    }
  };

  unsigned wr = 0, rd = 0;
  int I = 0, C = 0;
  int xat = -100, xn = 0;
  const unsigned myq = w * 4096;

  load_x(first ? xB : xA);
  if (WARM & 1) {
    const unsigned r = (wg >> 3) * 16 + w * 4, v = l * 16;
    dma4<true>(wqA + (size_t)r * 8192 + j * 1024, 6 * SLOT + myq, v, v + 7168, v + 14336, v + 21504);
    dma4<true>(wqB + (size_t)r * 8192 + j * 1024, 6 * SLOT + myq, v, v + 7168, v + 14336, v + 21504);
  }
  if (WARM & 2) {
    const unsigned r = j * 512 + (wg >> 3) * 16 + w * 4, v = l * 16;
    dma4<true>(woA + (size_t)r * 1024, 6 * SLOT + myq, v, v, v, v);
    dma4<true>(woB + (size_t)r * 1024, 6 * SLOT + myq, v, v, v, v);
  }
  {
    const char* wq0 = (first ? wqB : wqA) + wqoff;
#pragma unroll 1
    for (; I < 5; ++I) { dma4<false>(wq0 + (size_t)I * 262144, wr + myq, vWq[0], vWq[1], vWq[2], vWq[3]); wr = ring_next(wr); }
  }

#pragma unroll 1
  for (int su = 0; su < 2; ++su) {
    const int pr = su ? 1 - first : first;
    const bool isB = pr != 0, last = su != 0;
    const char* wq = (isB ? wqB : wqA) + wqoff;
    const char* wqn = (isB ? wqA : wqB) + wqoff;
    const char* wo = (isB ? woB : woA) + wooff;
    const char* kp = isB ? kB + kvoffB : kA + kvoffA;
    const char* vp = isB ? vB + kvoffB : vA + kvoffA;
    const int lnk = isB ? 3 : 2, nkv = 4 << lnk, E1 = 16 + nkv, E2 = E1 + 16;
    const int rot = isB ? b + 2 * u : b, rmask = isB ? 15 : 7, lstr = isB ? 13 : 14;
    const int lim = last ? E2 : E2 + 7;

    auto is_wq = [&](const char* base, int n) { dma4<false>(base + (size_t)n * 262144, wr + myq, vWq[0], vWq[1], vWq[2], vWq[3]); };
    auto is_wo = [&](int n) { dma4<false>(wo + (size_t)n * 16384, wr + myq, vWo[0], vWo[1], vWo[2], vWo[3]); };
    auto is_kv = [&](int m) {
      const int cm = m >> lnk, n = ((cm >> 1) << lnk) + (m & ((1 << lnk) - 1));
      const char* p = ((cm & 1) ? vp : kp) + ((size_t)((n + rot) & rmask) << lstr);
      dma4<KV_NT != 0>(p, wr + myq, vKV[0], vKV[1], vKV[2], vKV[3]);
    };
    auto wait_slots = [&](int k) { vmwait_dyn(4 * (I - C - k) + (C + k <= xat ? xn : 0)); };
#define FILL(PH)                                                                      \
  do {                                                                                \
    _Pragma("unroll 1") while (I < C + 7 && I < lim) {                                \
      if (PH == 0) { if (I < 16) is_wq(wq, I); else is_kv(I - 16); }                  \
      else if (PH == 1) { if (I < E1) is_kv(I - 16); else is_wo(I - E1); }            \
      else { if (I < E2) is_wo(I - E1); else is_wq(wqn, I - E2); }                    \
      wr = ring_next(wr); ++I;                                                        \
    }                                                                                 \
  } while (0)

    f32x4 qa[4][3];
#pragma unroll
    for (int c = 0; c < 4; ++c)
#pragma unroll
      for (int rb = 0; rb < 3; ++rb) qa[c][rb] = f32x4{0.f, 0.f, 0.f, 0.f};
#pragma unroll 1
    for (int un = 0; un < 8; ++un) {
      wait_slots(2);
      if (un == 0) bar();
#pragma unroll
      for (int h = 0; h < 2; ++h) {
        const unsigned xa = pX0 ^ (unsigned)((2 * un + h) << 6), base = rd + myq;
        rd = ring_next(rd);
        bf16x8 xf[3], wf[4];
#pragma unroll
        for (int rb = 0; rb < 3; ++rb) xf[rb] = lds_b128(xa + rb * 16384);
#pragma unroll
        for (int c = 0; c < 4; ++c) { const unsigned a = (pW0 ^ (c << 5)) + base; wf[c] = cat(lds_tr(a), lds_tr(a + 512)); }
        if (h == 0) { keep(xf[0]); FILL(0); }
#pragma unroll
        for (int c = 0; c < 4; ++c)
#pragma unroll
          for (int rb = 0; rb < 3; ++rb) qa[c][rb] = mfma(wf[c], xf[rb], qa[c][rb]);
      }
      C += 2;
    }
    STAMP();
    bar();
#pragma unroll
    for (int c = 0; c < 4; ++c)
#pragma unroll
      for (int rb = 0; rb < 3; ++rb) {
        const unsigned ch = 8 * kh + 2 * c + (g >> 1);
        *(LAS bf16x4*)(uintptr_t)(SCR + u * 12288 + rb * 4096 + i * 256 + ((ch ^ fswz(i)) << 4) + 8 * (g & 1)) = cvt4(qa[c][rb]);
      }
    bar();
    bf16x8 qf[4][3];
#pragma unroll
    for (int s = 0; s < 4; ++s)
#pragma unroll
      for (int rb = 0; rb < 3; ++rb) qf[s][rb] = lds_b128((pK0 ^ (s << 6)) + SCR + u * 12288 + rb * 4096);

    f32x4 oa[8][3], la[3];
    float mrun[3];
#pragma unroll
    for (int rb = 0; rb < 3; ++rb) {
      mrun[rb] = -__builtin_inff(); la[rb] = f32x4{0.f, 0.f, 0.f, 0.f};
#pragma unroll
      for (int t = 0; t < 8; ++t) oa[t][rb] = f32x4{0.f, 0.f, 0.f, 0.f};
    }
    const bf16x8 ones = {(__bf16)1.f, (__bf16)1.f, (__bf16)1.f, (__bf16)1.f, (__bf16)1.f, (__bf16)1.f, (__bf16)1.f, (__bf16)1.f};
    const int kslots = isB ? 2 : 1;
#define KV_BEGIN(B0, B1)                                                              \
  wait_slots(kslots);                                                                 \
  if (!isB) bar();                                                                    \
  unsigned B0, B1;                                                                    \
  if (isB) { B0 = rd + myq; rd = ring_next(rd); B1 = rd + myq; } else { B0 = rd + kh * 4096; B1 = B0 + 8192; } \
  rd = ring_next(rd);
#pragma unroll 1
    for (int ck = 0; ck < 2; ++ck) {
      f32x4 sa[8][3];
#pragma unroll
      for (int bl = 0; bl < 4; ++bl) {
        KV_BEGIN(b0, b1)
        bf16x8 kf[2][4];
#pragma unroll
        for (int s = 0; s < 4; ++s) kf[0][s] = lds_b128((pK0 ^ (s << 6)) + b0);
#pragma unroll
        for (int s = 0; s < 4; ++s) kf[1][s] = lds_b128((pK0 ^ (s << 6)) + b1);
        keep(kf[0][0]); FILL(1);
#pragma unroll
        for (int T = 0; T < 2; ++T)
#pragma unroll
          for (int rb = 0; rb < 3; ++rb) {
            f32x4 acc = {0.f, 0.f, 0.f, 0.f};
#pragma unroll
            for (int s = 0; s < 4; ++s) acc = mfma(kf[T][s], qf[s][rb], acc);
            sa[2 * bl + T][rb] = acc;
          }
        C += kslots;
      }
      STAMP();
      if (ck == 0) bar();
      bf16x8 pf[4][3];
#pragma unroll
      for (int rb = 0; rb < 3; ++rb) {
        float m = mrun[rb];
#pragma unroll
        for (int t = 0; t < 8; ++t) { m = fmx(fmx(m, sa[t][rb][0]), sa[t][rb][1]); m = fmx(fmx(m, sa[t][rb][2]), sa[t][rb][3]); }
        auto r1 = __builtin_amdgcn_permlane16_swap(__float_as_uint(m), __float_as_uint(m), false, false);
        m = fmx(__uint_as_float(r1[0]), __uint_as_float(r1[1]));
        auto r2 = __builtin_amdgcn_permlane32_swap(__float_as_uint(m), __float_as_uint(m), false, false);
        m = fmx(__uint_as_float(r2[0]), __uint_as_float(r2[1]));
        const float al = __builtin_amdgcn_exp2f((mrun[rb] - m) * CEXP), mc = -m * CEXP;
        mrun[rb] = m;
#pragma unroll
        for (int t = 0; t < 8; ++t) oa[t][rb] *= al;
        la[rb] *= al;
#pragma unroll
        for (int bl = 0; bl < 4; ++bl) {
          f32x4 p0, p1;
#pragma unroll
          for (int r = 0; r < 4; ++r) {
            p0[r] = __builtin_amdgcn_exp2f(__builtin_fmaf(sa[2 * bl][rb][r], CEXP, mc));
            p1[r] = __builtin_amdgcn_exp2f(__builtin_fmaf(sa[2 * bl + 1][rb][r], CEXP, mc));
          }
          pf[bl][rb] = cat(cvt4(p0), cvt4(p1));
        }
      }
      STAMP();
#pragma unroll
      for (int bl = 0; bl < 4; ++bl) {
        KV_BEGIN(b0, b1)
        bf16x8 vf[8];
#pragma unroll
        for (int t = 0; t < 8; ++t) { const unsigned a = pV0 ^ (t << 5); vf[t] = cat(lds_tr(a + b0), lds_tr(a + b1)); }
        keep(vf[0]); FILL(1);
#pragma unroll
        for (int rb = 0; rb < 3; ++rb) la[rb] = mfma(ones, pf[bl][rb], la[rb]);
#pragma unroll
        for (int t = 0; t < 8; ++t)
#pragma unroll
          for (int rb = 0; rb < 3; ++rb) oa[t][rb] = mfma(vf[t], pf[bl][rb], oa[t][rb]);
        C += kslots;
      }
    }

    STAMP();
    f32x4 own[4][3];
#pragma unroll
    for (int rb = 0; rb < 3; ++rb) {
#pragma unroll
      for (int s = 0; s < 2; ++s) {
        bf16x8 snd;
        if (kh) { snd = cat(cvt4(oa[2 * s][rb]), cvt4(oa[2 * s + 1][rb])); own[2 * s][rb] = oa[4 + 2 * s][rb]; own[2 * s + 1][rb] = oa[5 + 2 * s][rb]; }
        else { snd = cat(cvt4(oa[4 + 2 * s][rb]), cvt4(oa[5 + 2 * s][rb])); own[2 * s][rb] = oa[2 * s][rb]; own[2 * s + 1][rb] = oa[2 * s + 1][rb]; }
        *(LAS bf16x8*)(uintptr_t)(SCR + w * 6144 + (s * 3 + rb) * 1024 + l * 16) = snd;
      }
      *(LAS f32x2*)(uintptr_t)(SCR + 24576 + w * 1536 + rb * 512 + l * 8) = f32x2{mrun[rb], la[rb][0]};
    }
    bar();
#pragma unroll
    for (int rb = 0; rb < 3; ++rb) {
      const f32x2 st = *(const LAS f32x2*)(uintptr_t)(SCR + 24576 + (w ^ 1) * 1536 + rb * 512 + l * 8);
      const float m = fmx(mrun[rb], st[0]);
      float a0 = __builtin_amdgcn_exp2f((mrun[rb] - m) * CEXP), a1 = __builtin_amdgcn_exp2f((st[0] - m) * CEXP);
      const float inv = 1.0f / (a0 * la[rb][0] + a1 * st[1]);
      a0 *= inv; a1 *= inv;
#pragma unroll
      for (int s = 0; s < 2; ++s) {
        const unsigned a = SCR + (w ^ 1) * 6144 + (s * 3 + rb) * 1024 + l * 16;
        const u32x4 rv = *(const LAS u32x4*)(uintptr_t)a;
        f32x4 x0, x1;
        x0[0] = __uint_as_float(rv[0] << 16); x0[1] = __uint_as_float(rv[0] & 0xffff0000u); x0[2] = __uint_as_float(rv[1] << 16); x0[3] = __uint_as_float(rv[1] & 0xffff0000u);
        x1[0] = __uint_as_float(rv[2] << 16); x1[1] = __uint_as_float(rv[2] & 0xffff0000u); x1[2] = __uint_as_float(rv[3] << 16); x1[3] = __uint_as_float(rv[3] & 0xffff0000u);
        *(LAS bf16x8*)(uintptr_t)a = cat(cvt4(own[2 * s][rb] * a0 + x0 * a1), cvt4(own[2 * s + 1][rb] * a0 + x1 * a1));
      }
    }
    bar();
    bf16x8 of[8][3];
#pragma unroll
    for (int ks = 0; ks < 8; ++ks)
#pragma unroll
      for (int rb = 0; rb < 3; ++rb) of[ks][rb] = lds_b128(SCR + ((ks >> 1) ^ 1) * 6144 + ((ks & 1) * 3 + rb) * 1024 + l * 16);
    if (!last) { bar(); load_x(isB ? xA : xB); xat = I; xn = 12; }

    STAMP();
    f32x4 ya[8][3];
#pragma unroll
    for (int t = 0; t < 8; ++t)
#pragma unroll
      for (int rb = 0; rb < 3; ++rb) ya[t][rb] = f32x4{0.f, 0.f, 0.f, 0.f};
#pragma unroll
    for (int ks = 0; ks < 8; ++ks) {
      wait_slots(2);
      const unsigned b0 = rd + myq; rd = ring_next(rd);
      const unsigned b1 = rd + myq; rd = ring_next(rd);
      bf16x8 vf[8];
#pragma unroll
      for (int t = 0; t < 8; ++t) { const unsigned a = pV0 ^ (t << 5); vf[t] = cat(lds_tr(a + b0), lds_tr(a + b1)); }
      keep(vf[0]); FILL(2);
#pragma unroll
      for (int t = 0; t < 8; ++t)
#pragma unroll
        for (int rb = 0; rb < 3; ++rb) ya[t][rb] = mfma(vf[t], of[ks][rb], ya[t][rb]);
      C += 2;
    }
    {
      char* pb = ws + WS_PART + ((size_t)(pr * 16 + b) * 256 + (size_t)(4 * w) * 16 + (2 * j + hf)) * 3072 + l * 16;
#pragma unroll
      for (int tp = 0; tp < 4; ++tp)
#pragma unroll
        for (int rb = 0; rb < 3; ++rb) {
          const bf16x8 v = cat(cvt4(ya[2 * tp][rb]), cvt4(ya[2 * tp + 1][rb]));
          asm volatile("global_store_dwordx4 %0, %1, off sc1\n\ts_nop 1" ::"v"(pb + tp * 49152 + rb * 1024), "v"(v) : "memory");
        }
    }
    STAMP();
    I -= E2; C = 0; xat = I; xn = 12;
    if (last) {
      vmwait<0>();
      bar();
      if (tid == 0) __hip_atomic_fetch_add((unsigned*)(ws + (pr * 16 + b) * 128), 1u, __ATOMIC_RELAXED, __HIP_MEMORY_SCOPE_AGENT);
    }
  }
  if (tid == 0) __hip_atomic_fetch_add((unsigned*)(ws + (first * 16 + b) * 128), 1u, __ATOMIC_RELAXED, __HIP_MEMORY_SCOPE_AGENT);

  STAMP();
  if (w < 3) {
#pragma unroll 1
    for (int pi = 0; pi < 2; ++pi) {
      const int pr = pi ? 1 - first : first;
      const unsigned* cnt = (const unsigned*)(ws + (pr * 16 + b) * 128);
      for (unsigned spin = 0; spin < (1u << 22); ++spin) {
        if (__builtin_amdgcn_readfirstlane(__hip_atomic_load(cnt, __ATOMIC_RELAXED, __HIP_MEMORY_SCOPE_AGENT)) >= 16u) break;
        __builtin_amdgcn_s_sleep(1);
      }
      const char* pb = ws + WS_PART + ((size_t)(pr * 16 + b) * 256 + (size_t)(2 * j + hf) * 16) * 3072 + w * 1024 + l * 16;
      u32x4 v[16];
      asm volatile(
          "global_load_dwordx4 %0, %16, off sc1\n\tglobal_load_dwordx4 %1, %16, off offset:3072 sc1\n\t"
          "global_load_dwordx4 %2, %17, off sc1\n\tglobal_load_dwordx4 %3, %17, off offset:3072 sc1\n\t"
          "global_load_dwordx4 %4, %18, off sc1\n\tglobal_load_dwordx4 %5, %18, off offset:3072 sc1\n\t"
          "global_load_dwordx4 %6, %19, off sc1\n\tglobal_load_dwordx4 %7, %19, off offset:3072 sc1\n\t"
          "global_load_dwordx4 %8, %20, off sc1\n\tglobal_load_dwordx4 %9, %20, off offset:3072 sc1\n\t"
          "global_load_dwordx4 %10, %21, off sc1\n\tglobal_load_dwordx4 %11, %21, off offset:3072 sc1\n\t"
          "global_load_dwordx4 %12, %22, off sc1\n\tglobal_load_dwordx4 %13, %22, off offset:3072 sc1\n\t"
          "global_load_dwordx4 %14, %23, off sc1\n\tglobal_load_dwordx4 %15, %23, off offset:3072 sc1\n\t"
          "s_waitcnt vmcnt(0)"
          : "=&v"(v[0]), "=&v"(v[1]), "=&v"(v[2]), "=&v"(v[3]), "=&v"(v[4]), "=&v"(v[5]), "=&v"(v[6]), "=&v"(v[7]),
            "=&v"(v[8]), "=&v"(v[9]), "=&v"(v[10]), "=&v"(v[11]), "=&v"(v[12]), "=&v"(v[13]), "=&v"(v[14]), "=&v"(v[15])
          : "v"(pb), "v"(pb + 6144), "v"(pb + 12288), "v"(pb + 18432), "v"(pb + 24576), "v"(pb + 30720), "v"(pb + 36864), "v"(pb + 43008)
          : "memory");
      f32x4 s0 = {0.f, 0.f, 0.f, 0.f}, s1 = s0;
#pragma unroll
      for (int n = 0; n < 16; ++n) {
        s0[0] += __uint_as_float(v[n][0] << 16); s0[1] += __uint_as_float(v[n][0] & 0xffff0000u); s0[2] += __uint_as_float(v[n][1] << 16); s0[3] += __uint_as_float(v[n][1] & 0xffff0000u);
        s1[0] += __uint_as_float(v[n][2] << 16); s1[1] += __uint_as_float(v[n][2] & 0xffff0000u); s1[2] += __uint_as_float(v[n][3] << 16); s1[3] += __uint_as_float(v[n][3] & 0xffff0000u);
      }
      char* op = out + ((size_t)(b * 48 + w * 16 + i) * 1024 + pr * 512 + (2 * j + hf) * 32 + 4 * g) * 2;
      *(bf16x4*)op = cvt4(s0);
      *(bf16x4*)(op + 32) = cvt4(s1);
      STAMP();
    }
  }
}

extern "C" void kernel_launch(void* const* d_in, const int* in_sizes, int n_in, void* d_out, int out_size, void* d_ws, size_t ws_size, hipStream_t stream) {
  (void)in_sizes; (void)n_in; (void)out_size; (void)ws_size;
  (void)hipFuncSetAttribute((const void*)fused_block, hipFuncAttributeMaxDynamicSharedMemorySize, (int)LDS_BYTES);
  (void)hipMemsetAsync(d_ws, 0, 4096, stream);
  fused_block<<<256, 256, LDS_BYTES, stream>>>((const char*)d_in[0], (const char*)d_in[1], (const char*)d_in[2], (const char*)d_in[3], (const char*)d_in[4],
                                               (const char*)d_in[5], (const char*)d_in[6], (const char*)d_in[7], (const char*)d_in[8], (const char*)d_in[9],
                                               (char*)d_out, (char*)d_ws);
}
```

```cpp
#include <hip/hip_runtime.h>
#include <hip/hip_bf16.h>

__global__ __launch_bounds__(256, 1) void fused_block(const void* xA, const void* kA, const void* vA, const void* wqA, const void* woA,
                                                      const void* xB, const void* kB, const void* vB, const void* wqB, const void* woB,
                                                      void* out, void* ws) {
  const int w = __builtin_amdgcn_readfirstlane(threadIdx.x >> 6);
  asm volatile(
      "s_memrealtime s[62:63]\n\t"
      "s_bfe_u32 s12, %1, 0x10003\n\t"
      "s_mul_i32 s19, s12, 40\n\t"
      "s_add_u32 s22, s19, 32\n\t"
      "s_load_dwordx8 s[68:75], %0, s19\n\t"
      "s_load_dwordx4 s[76:79], %0, 0x50\n\t"
      "s_load_dwordx2 s[80:81], %0, s22\n\t"
      "s_load_dwordx8 s[52:59], %0, 0x28\n\t"
      "s_load_dwordx2 s[98:99], %0, 0x48\n\t"
      "s_mov_b32 s6, %2\n\t"
      "s_mul_i32 s23, s12, 0\n\t"
      "s_xor_b32 s10, %1, s23\n\t"
      "s_and_b32 s10, s10, 7\n\t"
      "s_lshr_b32 s9, %1, 4\n\t"
      "s_lshl_b32 s11, s12, 4\n\t"
      "s_add_u32 s11, s11, s9\n\t"
      "s_and_b32 s27, s9, 7\n\t"
      "s_and_b32 s16, s9, 3\n\t"
      "s_lshr_b32 s7, s6, 1\n\t"
      "s_and_b32 s8, s6, 1\n\t"
      "s_mov_b32 s13, 0\n\t"
      "s_lshl_b32 s14, s6, 15\n\t"
      "s_mul_i32 s19, s6, 3072\n\t"
      "s_add_u32 s15, s19, 0x20000\n\t"
      "s_lshl_b32 s17, s16, 20\n\t"
      "s_add_u32 s90, s14, 0x0\n\t"
      "s_add_u32 s91, s14, 0x1000\n\t"
      "s_add_u32 s92, s14, 0x2000\n\t"
      "s_add_u32 s93, s14, 0x3000\n\t"
      "s_add_u32 s94, s14, 0x4000\n\t"
      "s_add_u32 s95, s14, 0x5000\n\t"
      "s_add_u32 s96, s14, 0x6000\n\t"
      "s_add_u32 s97, s14, 0x7000\n\t"
      "v_mbcnt_lo_u32_b32 v246, -1, 0\n\t"
      "v_mbcnt_hi_u32_b32 v246, -1, v246\n\t"
      "v_and_b32 v247, 15, v246\n\t"
      "v_lshrrev_b32 v248, 4, v246\n\t"
      "v_lshlrev_b32 v216, 4, v246\n\t"
      "v_lshlrev_b32 v249, 1, v248\n\t"
      "v_xor_b32 v249, v247, v249\n\t"
      "v_lshlrev_b32 v249, 4, v249\n\t"
      "v_lshl_add_u32 v232, v248, 13, v249\n\t"
      "v_xor_b32 v212, 0, v232\n\t"
      "v_xor_b32 v213, 128, v232\n\t"
      "v_add_u32 v213, 0x7c00, v213\n\t"
      "v_xor_b32 v214, 144, v232\n\t"
      "v_add_u32 v214, 0xf800, v214\n\t"
      "v_xor_b32 v215, 16, v232\n\t"
      "v_add_u32 v215, 0x17400, v215\n\t"
      "v_lshl_add_u32 v232, v248, 8, v249\n\t"
      "v_xor_b32 v204, 0, v232\n\t"
      "v_xor_b32 v205, 128, v232\n\t"
      "v_xor_b32 v206, 144, v232\n\t"
      "v_xor_b32 v207, 16, v232\n\t"
      "v_lshl_add_u32 v232, v248, 10, v249\n\t"
      "s_mul_i32 s19, s6, 3\n\t"
      "s_add_u32 s19, s19, 0\n\t"
      "s_bfe_u32 s22, s19, 0x10001\n\t"
      "s_xor_b32 s23, s19, s22\n\t"
      "s_and_b32 s23, s23, 1\n\t"
      "s_lshl_b32 s23, s23, 3\n\t"
      "s_or_b32 s23, s23, s22\n\t"
      "s_lshl_b32 s23, s23, 4\n\t"
      "s_lshl_b32 s19, s19, 12\n\t"
      "s_sub_u32 s19, s19, 0\n\t"
      "v_xor_b32 v245, s23, v232\n\t"
      "v_add_u32 v245, s19, v245\n\t"
      "s_mul_i32 s19, s6, 3\n\t"
      "s_add_u32 s19, s19, 1\n\t"
      "s_bfe_u32 s22, s19, 0x10001\n\t"
      "s_xor_b32 s23, s19, s22\n\t"
      "s_and_b32 s23, s23, 1\n\t"
      "s_lshl_b32 s23, s23, 3\n\t"
      "s_or_b32 s23, s23, s22\n\t"
      "s_lshl_b32 s23, s23, 4\n\t"
      "s_lshl_b32 s19, s19, 12\n\t"
      "s_sub_u32 s19, s19, 1024\n\t"
      "v_xor_b32 v250, s23, v232\n\t"
      "v_add_u32 v250, s19, v250\n\t"
      "s_mul_i32 s19, s6, 3\n\t"
      "s_add_u32 s19, s19, 2\n\t"
      "s_bfe_u32 s22, s19, 0x10001\n\t"
      "s_xor_b32 s23, s19, s22\n\t"
      "s_and_b32 s23, s23, 1\n\t"
      "s_lshl_b32 s23, s23, 3\n\t"
      "s_or_b32 s23, s23, s22\n\t"
      "s_lshl_b32 s23, s23, 4\n\t"
      "s_lshl_b32 s19, s19, 12\n\t"
      "s_sub_u32 s19, s19, 2048\n\t"
      "v_xor_b32 v251, s23, v232\n\t"
      "v_add_u32 v251, s19, v251\n\t"
      "s_lshl_b32 s19, s10, 10\n\t"
      "s_lshl_b32 s22, s6, 8\n\t"
      "s_add_u32 s19, s19, s22\n\t"
      "s_mul_i32 s23, s9, 0xc000\n\t"
      "s_waitcnt lgkmcnt(0)\n\t"
      "s_cmp_lg_u32 s12, 0\n\t"
      "s_cbranch_scc1 L_nohelp_%=\n\t"
      "s_mov_b32 m0, 0x27000\n\t"
      "v_lshlrev_b32 v220, 3, v216\n\t"
      "s_getpc_b64 s[28:29]\n\t"
      "L_pc_%=:\n\t"
      "s_mov_b32 s30, L_last_%=-L_pc_%=\n\t"
      "s_mov_b32 s31, 0x20000\n\t"
      "s_lshl_b32 s20, s9, 2\n\t"
      "s_add_u32 s20, s20, s6\n\t"
      "s_lshl_b32 s20, s20, 10\n\t"
      "v_and_b32 v221, 0x380, v220\n\t"
      "buffer_load_dword v221, s[28:31], s20 offen lds\n\t"
      "v_lshrrev_b32 v221, 3, v246\n\t"
      "v_and_b32 v222, 0x380, v220\n\t"
      "v_lshl_add_u32 v221, v221, 13, v222\n\t"
      "s_lshl_b32 s20, s9, 2\n\t"
      "s_add_u32 s20, s20, s6\n\t"
      "s_lshl_b32 s20, s20, 16\n\t"
      "s_xor_b32 s21, s10, 0\n\t"
      "s_lshl_b32 s21, s21, 10\n\t"
      "s_add_u32 s20, s20, s21\n\t"
      "s_add_u32 s28, s58, s20\n\t"
      "s_addc_u32 s29, s59, 0\n\t"
      "s_sub_u32 s30, 0x400000, s20\n\t"
      "s_mov_b32 s31, 0x20000\n\t"
      "buffer_load_dword v221, s[28:31], 0 offen lds\n\t"
      "s_mul_i32 s20, s6, 12288\n\t"
      "s_add_u32 s20, s20, s23\n\t"
      "s_add_u32 s28, s52, s20\n\t"
      "s_addc_u32 s29, s53, 0\n\t"
      "s_sub_u32 s30, 0xc0000, s20\n\t"
      "s_mov_b32 s31, 0x20000\n\t"
      "s_mov_b32 s21, 4096\n\t"
      "buffer_load_dword v220, s[28:31], 0 offen lds\n\t"
      "buffer_load_dword v220, s[28:31], s21 offen lds\n\t"
      "s_lshl_b32 s20, s9, 3\n\t"
      "s_add_u32 s20, s20, s10\n\t"
      "s_xor_b32 s20, s20, 0\n\t"
      "s_lshl_b32 s20, s20, 2\n\t"
      "s_add_u32 s20, s20, s7\n\t"
      "s_lshl_b32 s20, s20, 17\n\t"
      "s_add_u32 s28, s54, s20\n\t"
      "s_addc_u32 s29, s55, 0\n\t"
      "s_sub_u32 s30, 0x4000000, s20\n\t"
      "s_mov_b32 s31, 0x20000\n\t"
      "s_lshl_b32 s20, s7, 1\n\t"
      "s_add_u32 s20, s20, s9\n\t"
      "s_add_u32 s20, s20, s8\n\t"
      "s_add_u32 s21, s20, 0\n\t"
      "s_and_b32 s21, s21, 15\n\t"
      "s_lshl_b32 s21, s21, 13\n\t"
      "buffer_load_dword v220, s[28:31], s21 offen lds\n\t"
      "s_add_u32 s21, s20, 2\n\t"
      "s_and_b32 s21, s21, 15\n\t"
      "s_lshl_b32 s21, s21, 13\n\t"
      "buffer_load_dword v220, s[28:31], s21 offen lds\n\t"
      "L_cap_%=:\n\t"
      "s_lshl_b32 s20, s9, 3\n\t"
      "s_add_u32 s20, s20, s10\n\t"
      "s_lshl_b32 s20, s20, 5\n\t"
      "s_add_u32 s20, s20, s6\n\t"
      "s_lshl_b32 s20, s20, 12\n\t"
      "s_add_u32 s28, s70, s20\n\t"
      "s_addc_u32 s29, s71, 0\n\t"
      "s_sub_u32 s30, 0x1000000, s20\n\t"
      "s_mov_b32 s31, 0x20000\n\t"
      "s_add_u32 s32, s72, s20\n\t"
      "s_addc_u32 s33, s73, 0\n\t"
      "s_sub_u32 s34, 0x1000000, s20\n\t"
      "s_mov_b32 s35, 0x20000\n\t"
      "s_add_u32 s21, s9, 0\n\t"
      "s_and_b32 s21, s21, 7\n\t"
      "s_lshl_b32 s21, s21, 14\n\t"
      "buffer_load_dwordx4 v[96:99], v204, s[28:31], s21 offen nt\n\t"
      "buffer_load_dwordx4 v[100:103], v205, s[28:31], s21 offen offset:1024 nt\n\t"
      "buffer_load_dwordx4 v[104:107], v206, s[28:31], s21 offen offset:2048 nt\n\t"
      "buffer_load_dwordx4 v[108:111], v207, s[28:31], s21 offen offset:3072 nt\n\t"
      "s_add_u32 s21, s9, 1\n\t"
      "s_and_b32 s21, s21, 7\n\t"
      "s_lshl_b32 s21, s21, 14\n\t"
      "buffer_load_dwordx4 v[112:115], v204, s[28:31], s21 offen nt\n\t"
      "buffer_load_dwordx4 v[116:119], v205, s[28:31], s21 offen offset:1024 nt\n\t"
      "buffer_load_dwordx4 v[120:123], v206, s[28:31], s21 offen offset:2048 nt\n\t"
      "buffer_load_dwordx4 v[124:127], v207, s[28:31], s21 offen offset:3072 nt\n\t"
      "s_add_u32 s21, s9, 0\n\t"
      "s_and_b32 s21, s21, 7\n\t"
      "s_lshl_b32 s21, s21, 14\n\t"
      "buffer_load_dwordx4 v[128:131], v204, s[32:35], s21 offen nt\n\t"
      "buffer_load_dwordx4 v[132:135], v205, s[32:35], s21 offen offset:1024 nt\n\t"
      "buffer_load_dwordx4 v[136:139], v206, s[32:35], s21 offen offset:2048 nt\n\t"
      "buffer_load_dwordx4 v[140:143], v207, s[32:35], s21 offen offset:3072 nt\n\t"
      "s_add_u32 s21, s9, 1\n\t"
      "s_and_b32 s21, s21, 7\n\t"
      "s_lshl_b32 s21, s21, 14\n\t"
      "buffer_load_dwordx4 v[144:147], v204, s[32:35], s21 offen nt\n\t"
      "buffer_load_dwordx4 v[148:151], v205, s[32:35], s21 offen offset:1024 nt\n\t"
      "buffer_load_dwordx4 v[152:155], v206, s[32:35], s21 offen offset:2048 nt\n\t"
      "buffer_load_dwordx4 v[156:159], v207, s[32:35], s21 offen offset:3072 nt\n\t"
      "s_add_u32 s21, s9, 2\n\t"
      "s_and_b32 s21, s21, 7\n\t"
      "s_lshl_b32 s21, s21, 14\n\t"
      "buffer_load_dwordx4 v[160:163], v204, s[28:31], s21 offen nt\n\t"
      "buffer_load_dwordx4 v[164:167], v205, s[28:31], s21 offen offset:1024 nt\n\t"
      "buffer_load_dwordx4 v[168:171], v206, s[28:31], s21 offen offset:2048 nt\n\t"
      "buffer_load_dwordx4 v[172:175], v207, s[28:31], s21 offen offset:3072 nt\n\t"
      "s_add_u32 s21, s9, 3\n\t"
      "s_and_b32 s21, s21, 7\n\t"
      "s_lshl_b32 s21, s21, 14\n\t"
      "buffer_load_dwordx4 v[176:179], v204, s[28:31], s21 offen nt\n\t"
      "buffer_load_dwordx4 v[180:183], v205, s[28:31], s21 offen offset:1024 nt\n\t"
      "buffer_load_dwordx4 v[184:187], v206, s[28:31], s21 offen offset:2048 nt\n\t"
      "buffer_load_dwordx4 v[188:191], v207, s[28:31], s21 offen offset:3072 nt\n\t"
      "s_add_u32 s21, s9, 2\n\t"
      "s_and_b32 s21, s21, 7\n\t"
      "s_lshl_b32 s21, s21, 14\n\t"
      "buffer_load_dwordx4 a[112:115], v204, s[32:35], s21 offen nt\n\t"
      "buffer_load_dwordx4 a[116:119], v205, s[32:35], s21 offen offset:1024 nt\n\t"
      "buffer_load_dwordx4 a[120:123], v206, s[32:35], s21 offen offset:2048 nt\n\t"
      "buffer_load_dwordx4 a[124:127], v207, s[32:35], s21 offen offset:3072 nt\n\t"
      "s_add_u32 s21, s9, 3\n\t"
      "s_and_b32 s21, s21, 7\n\t"
      "s_lshl_b32 s21, s21, 14\n\t"
      "buffer_load_dwordx4 a[128:131], v204, s[32:35], s21 offen nt\n\t"
      "buffer_load_dwordx4 a[132:135], v205, s[32:35], s21 offen offset:1024 nt\n\t"
      "buffer_load_dwordx4 a[136:139], v206, s[32:35], s21 offen offset:2048 nt\n\t"
      "buffer_load_dwordx4 a[140:143], v207, s[32:35], s21 offen offset:3072 nt\n\t"
      "s_waitcnt vmcnt(24)\n\t"
      "s_add_u32 s62, s62, 580\n\t"
      "L_ad_%=:\n\t"
      "s_sleep 2\n\t"
      "s_memrealtime s[64:65]\n\t"
      "s_waitcnt lgkmcnt(0)\n\t"
      "s_sub_u32 s24, s64, s62\n\t"
      "s_cmp_lt_i32 s24, 0\n\t"
      "s_cbranch_scc1 L_ad_%=\n\t"
      "s_mov_b64 s[20:21], s[54:55]\n\t"
      "L_nohelp_%=:\n\t"
      "s_add_u32 s44, s74, s19\n\t"
      "s_addc_u32 s45, s75, 0\n\t"
      "s_sub_u32 s46, 0x400000, s19\n\t"
      "s_mov_b32 s47, 0x20000\n\t"
      "s_mov_b32 m0, s90\n\t"
      "s_nop 0\n\t"
      "buffer_load_dwordx4 v212, s[44:47], s17 offen lds\n\t"
      "buffer_load_dwordx4 v213, s[44:47], s17 offen offset:1024 lds\n\t"
      "buffer_load_dwordx4 v214, s[44:47], s17 offen offset:2048 lds\n\t"
      "buffer_load_dwordx4 v215, s[44:47], s17 offen offset:3072 lds\n\t"
      "s_add_u32 s17, s17, 0x20000\n\t"
      "s_and_b32 s17, s17, 0x3fffff\n\t"
      "s_add_u32 s48, s68, s23\n\t"
      "s_addc_u32 s49, s69, 0\n\t"
      "s_sub_u32 s50, 0xc0000, s23\n\t"
      "s_mov_b32 s51, 0x20000\n\t"
      "s_add_u32 s24, s16, 0\n\t"
      "s_and_b32 s24, s24, 3\n\t"
      "s_lshl_b32 s24, s24, 8\n\t"
      "s_mov_b32 m0, s15\n\t"
      "s_nop 0\n\t"
      "buffer_load_dwordx4 v245, s[48:51], s24 offen lds\n\t"
      "buffer_load_dwordx4 v250, s[48:51], s24 offen offset:1024 lds\n\t"
      "buffer_load_dwordx4 v251, s[48:51], s24 offen offset:2048 lds\n\t"
      "s_mov_b32 m0, s91\n\t"
      "s_nop 0\n\t"
      "buffer_load_dwordx4 v212, s[44:47], s17 offen lds\n\t"
      "buffer_load_dwordx4 v213, s[44:47], s17 offen offset:1024 lds\n\t"
      "buffer_load_dwordx4 v214, s[44:47], s17 offen offset:2048 lds\n\t"
      "buffer_load_dwordx4 v215, s[44:47], s17 offen offset:3072 lds\n\t"
      "s_add_u32 s17, s17, 0x20000\n\t"
      "s_and_b32 s17, s17, 0x3fffff\n\t"
      "s_lshl_b32 s19, s11, 7\n\t"
      "s_load_dwordx2 s[4:5], s[78:79], s19\n\t"
      "v_xor_b32 v208, 0, v232\n\t"
      "v_xor_b32 v209, 128, v232\n\t"
      "v_add_u32 v209, 0xc00, v209\n\t"
      "v_xor_b32 v210, 144, v232\n\t"
      "v_add_u32 v210, 0x1800, v210\n\t"
      "v_xor_b32 v211, 16, v232\n\t"
      "v_add_u32 v211, 0x2400, v211\n\t"
      "v_and_b32 v232, 8, v247\n\t"
      "v_lshrrev_b32 v232, 1, v232\n\t"
      "v_and_b32 v233, 7, v247\n\t"
      "v_xor_b32 v232, v232, v233\n\t"
      "v_lshrrev_b32 v233, 3, v247\n\t"
      "v_lshl_add_u32 v232, v232, 1, v233\n\t"
      "v_xor_b32 v233, v248, v232\n\t"
      "v_lshlrev_b32 v233, 4, v233\n\t"
      "v_lshl_add_u32 v217, v247, 8, v233\n\t"
      "v_lshrrev_b32 v233, 1, v248\n\t"
      "v_xor_b32 v252, v233, v232\n\t"
      "v_lshlrev_b32 v252, 4, v252\n\t"
      "v_lshl_add_u32 v252, v247, 8, v252\n\t"
      "v_and_b32 v253, 1, v248\n\t"
      "v_lshl_add_u32 v239, v253, 3, v252\n\t"
      "v_lshrrev_b32 v254, 2, v247\n\t"
      "v_bfe_u32 v232, v246, 1, 1\n\t"
      "v_and_b32 v234, 1, v246\n\t"
      "v_lshlrev_b32 v234, 3, v234\n\t"
      "v_xor_b32 v252, v253, v233\n\t"
      "v_lshl_add_u32 v252, v252, 2, v254\n\t"
      "v_lshl_add_u32 v252, v252, 1, v233\n\t"
      "v_xor_b32 v252, v252, v232\n\t"
      "v_lshl_add_u32 v252, v252, 4, v234\n\t"
      "v_lshl_add_u32 v235, v248, 2, v254\n\t"
      "v_lshl_add_u32 v218, v235, 8, v252\n\t"
      "v_lshl_add_u32 v252, v253, 2, v254\n\t"
      "v_lshl_add_u32 v252, v252, 1, v253\n\t"
      "v_xor_b32 v252, v252, v232\n\t"
      "v_lshl_add_u32 v252, v252, 4, v234\n\t"
      "v_lshl_add_u32 v235, v253, 3, v254\n\t"
      "v_lshl_add_u32 v252, v235, 8, v252\n\t"
      "v_lshl_add_u32 v219, v233, 12, v252\n\t"
      "v_xad_u32 v220, v219, 0, s14\n\t"
      "v_xad_u32 v221, v219, 32, s14\n\t"
      "v_xad_u32 v222, v219, 64, s14\n\t"
      "v_xor_b32 v223, 96, v219\n\t"
      "v_add_u32 v223, s14, v223\n\t"
      "v_xor_b32 v224, 128, v219\n\t"
      "v_add_u32 v224, s14, v224\n\t"
      "v_xor_b32 v225, 160, v219\n\t"
      "v_add_u32 v225, s14, v225\n\t"
      "v_xor_b32 v226, 192, v219\n\t"
      "v_add_u32 v226, s14, v226\n\t"
      "v_xor_b32 v227, 224, v219\n\t"
      "v_add_u32 v227, s14, v227\n\t"
      "v_xor_b32 v228, 0, v217\n\t"
      "v_add_u32 v228, 0x20000, v228\n\t"
      "v_xor_b32 v229, 64, v217\n\t"
      "v_add_u32 v229, 0x20000, v229\n\t"
      "v_xor_b32 v230, 128, v217\n\t"
      "v_add_u32 v230, 0x20000, v230\n\t"
      "v_xor_b32 v231, 192, v217\n\t"
      "v_add_u32 v231, 0x20000, v231\n\t"
      "s_mov_b32 m0, s92\n\t"
      "s_nop 0\n\t"
      "buffer_load_dwordx4 v212, s[44:47], s17 offen lds\n\t"
      "buffer_load_dwordx4 v213, s[44:47], s17 offen offset:1024 lds\n\t"
      "buffer_load_dwordx4 v214, s[44:47], s17 offen offset:2048 lds\n\t"
      "buffer_load_dwordx4 v215, s[44:47], s17 offen offset:3072 lds\n\t"
      "s_add_u32 s17, s17, 0x20000\n\t"
      "s_and_b32 s17, s17, 0x3fffff\n\t"
      "s_mov_b32 m0, s93\n\t"
      "s_nop 0\n\t"
      "buffer_load_dwordx4 v212, s[44:47], s17 offen lds\n\t"
      "buffer_load_dwordx4 v213, s[44:47], s17 offen offset:1024 lds\n\t"
      "buffer_load_dwordx4 v214, s[44:47], s17 offen offset:2048 lds\n\t"
      "buffer_load_dwordx4 v215, s[44:47], s17 offen offset:3072 lds\n\t"
      "s_add_u32 s17, s17, 0x20000\n\t"
      "s_and_b32 s17, s17, 0x3fffff\n\t"
      "v_mov_b32 v232, 0x3f803f80\n\t"
      "v_accvgpr_write_b32 a208, v232\n\t"
      "v_accvgpr_write_b32 a209, v232\n\t"
      "v_accvgpr_write_b32 a210, v232\n\t"
      "v_accvgpr_write_b32 a211, v232\n\t"
      "s_mov_b32 s26, 0x3e0293ee\n\t"
      "v_lshlrev_b32 v233, 3, v247\n\t"
      "s_xor_b32 s19, s6, 0\n\t"
      "s_mul_i32 s19, s19, 6144\n\t"
      "s_add_u32 s19, s19, 0x20000\n\t"
      "v_add_u32 v232, s19, v216\n\t"
      "v_accvgpr_write_b32 a212, v232\n\t"
      "s_xor_b32 s19, s6, 1\n\t"
      "s_mul_i32 s19, s19, 6144\n\t"
      "s_add_u32 s19, s19, 0x20000\n\t"
      "v_add_u32 v232, s19, v216\n\t"
      "v_accvgpr_write_b32 a213, v232\n\t"
      "s_xor_b32 s19, s6, 0\n\t"
      "s_mul_i32 s19, s19, 384\n\t"
      "s_add_u32 s19, s19, 0x26000\n\t"
      "v_add_u32 v232, s19, v233\n\t"
      "v_accvgpr_write_b32 a214, v232\n\t"
      "s_xor_b32 s19, s6, 1\n\t"
      "s_mul_i32 s19, s19, 384\n\t"
      "s_add_u32 s19, s19, 0x26000\n\t"
      "v_add_u32 v232, s19, v233\n\t"
      "v_accvgpr_write_b32 a215, v232\n\t"
      "s_lshl_b32 s23, s12, 1\n\t"
      "s_add_u32 s19, s27, 0\n\t"
      "s_and_b32 s19, s19, 7\n\t"
      "s_xor_b32 s19, s19, s23\n\t"
      "s_mul_i32 s19, s19, 3072\n\t"
      "s_add_u32 s19, s19, 0x20000\n\t"
      "v_add_u32 v232, s19, v216\n\t"
      "v_accvgpr_write_b32 a216, v232\n\t"
      "s_add_u32 s19, s27, 1\n\t"
      "s_and_b32 s19, s19, 7\n\t"
      "s_xor_b32 s19, s19, s23\n\t"
      "s_mul_i32 s19, s19, 3072\n\t"
      "s_add_u32 s19, s19, 0x20000\n\t"
      "v_add_u32 v232, s19, v216\n\t"
      "v_accvgpr_write_b32 a217, v232\n\t"
      "s_add_u32 s19, s27, 2\n\t"
      "s_and_b32 s19, s19, 7\n\t"
      "s_xor_b32 s19, s19, s23\n\t"
      "s_mul_i32 s19, s19, 3072\n\t"
      "s_add_u32 s19, s19, 0x20000\n\t"
      "v_add_u32 v232, s19, v216\n\t"
      "v_accvgpr_write_b32 a218, v232\n\t"
      "s_add_u32 s19, s27, 3\n\t"
      "s_and_b32 s19, s19, 7\n\t"
      "s_xor_b32 s19, s19, s23\n\t"
      "s_mul_i32 s19, s19, 3072\n\t"
      "s_add_u32 s19, s19, 0x20000\n\t"
      "v_add_u32 v232, s19, v216\n\t"
      "v_accvgpr_write_b32 a219, v232\n\t"
      "s_add_u32 s19, s27, 4\n\t"
      "s_and_b32 s19, s19, 7\n\t"
      "s_xor_b32 s19, s19, s23\n\t"
      "s_mul_i32 s19, s19, 3072\n\t"
      "s_add_u32 s19, s19, 0x20000\n\t"
      "v_add_u32 v232, s19, v216\n\t"
      "v_accvgpr_write_b32 a220, v232\n\t"
      "s_add_u32 s19, s27, 5\n\t"
      "s_and_b32 s19, s19, 7\n\t"
      "s_xor_b32 s19, s19, s23\n\t"
      "s_mul_i32 s19, s19, 3072\n\t"
      "s_add_u32 s19, s19, 0x20000\n\t"
      "v_add_u32 v232, s19, v216\n\t"
      "v_accvgpr_write_b32 a221, v232\n\t"
      "s_add_u32 s19, s27, 6\n\t"
      "s_and_b32 s19, s19, 7\n\t"
      "s_xor_b32 s19, s19, s23\n\t"
      "s_mul_i32 s19, s19, 3072\n\t"
      "s_add_u32 s19, s19, 0x20000\n\t"
      "v_add_u32 v232, s19, v216\n\t"
      "v_accvgpr_write_b32 a222, v232\n\t"
      "s_add_u32 s19, s27, 7\n\t"
      "s_and_b32 s19, s19, 7\n\t"
      "s_xor_b32 s19, s19, s23\n\t"
      "s_mul_i32 s19, s19, 3072\n\t"
      "s_add_u32 s19, s19, 0x20000\n\t"
      "v_add_u32 v232, s19, v216\n\t"
      "v_accvgpr_write_b32 a223, v232\n\t"
      "s_lshl_b32 s19, s9, 3\n\t"
      "s_add_u32 s19, s19, s10\n\t"
      "s_lshl_b32 s22, s19, 2\n\t"
      "s_add_u32 s22, s22, s7\n\t"
      "s_lshl_b32 s19, s19, 5\n\t"
      "s_add_u32 s19, s19, s6\n\t"
      "s_lshl_b32 s22, s22, 5\n\t"
      "s_add_u32 s22, s22, s8\n\t"
      "s_cmp_lg_u32 s12, 0\n\t"
      "s_cselect_b32 s19, s22, s19\n\t"
      "s_cselect_b32 s22, 26, 24\n\t"
      "s_lshl_b32 s22, 1, s22\n\t"
      "s_lshl_b32 s19, s19, 12\n\t"
      "s_add_u32 s28, s70, s19\n\t"
      "s_addc_u32 s29, s71, 0\n\t"
      "s_sub_u32 s30, s22, s19\n\t"
      "s_mov_b32 s31, 0x20000\n\t"
      "s_add_u32 s32, s72, s19\n\t"
      "s_addc_u32 s33, s73, 0\n\t"
      "s_sub_u32 s34, s22, s19\n\t"
      "s_mov_b32 s35, 0x20000\n\t"
      "s_cmp_lg_u32 s12, 0\n\t"
      "s_cselect_b64 s[36:37], s[28:29], s[28:29]\n\t"
      "s_cselect_b64 s[38:39], s[30:31], s[30:31]\n\t"
      "s_cselect_b64 s[40:41], s[28:29], s[32:33]\n\t"
      "s_cselect_b64 s[42:43], s[30:31], s[34:35]\n\t"
      "s_lshl_b32 s19, s7, 1\n\t"
      "s_add_u32 s19, s19, s9\n\t"
      "s_cmp_lg_u32 s12, 0\n\t"
      "s_cselect_b32 s25, s19, s9\n\t"
      "s_cselect_b32 s22, 15, 7\n\t"
      "s_cselect_b32 s23, 13, 14\n\t"
      "s_add_u32 s52, s25, 0\n\t"
      "s_and_b32 s52, s52, s22\n\t"
      "s_lshl_b32 s52, s52, s23\n\t"
      "s_add_u32 s53, s25, 1\n\t"
      "s_and_b32 s53, s53, s22\n\t"
      "s_lshl_b32 s53, s53, s23\n\t"
      "s_add_u32 s54, s25, 2\n\t"
      "s_and_b32 s54, s54, s22\n\t"
      "s_lshl_b32 s54, s54, s23\n\t"
      "s_add_u32 s55, s25, 3\n\t"
      "s_and_b32 s55, s55, s22\n\t"
      "s_lshl_b32 s55, s55, s23\n\t"
      "s_add_u32 s56, s25, 4\n\t"
      "s_and_b32 s56, s56, s22\n\t"
      "s_lshl_b32 s56, s56, s23\n\t"
      "s_add_u32 s57, s25, 5\n\t"
      "s_and_b32 s57, s57, s22\n\t"
      "s_lshl_b32 s57, s57, s23\n\t"
      "s_add_u32 s58, s25, 6\n\t"
      "s_and_b32 s58, s58, s22\n\t"
      "s_lshl_b32 s58, s58, s23\n\t"
      "s_add_u32 s59, s25, 7\n\t"
      "s_and_b32 s59, s59, s22\n\t"
      "s_lshl_b32 s59, s59, s23\n\t"
      "s_add_u32 s60, s25, 8\n\t"
      "s_and_b32 s60, s60, s22\n\t"
      "s_lshl_b32 s60, s60, s23\n\t"
      "s_add_u32 s61, s25, 9\n\t"
      "s_and_b32 s61, s61, s22\n\t"
      "s_lshl_b32 s61, s61, s23\n\t"
      "s_add_u32 s62, s25, 10\n\t"
      "s_and_b32 s62, s62, s22\n\t"
      "s_lshl_b32 s62, s62, s23\n\t"
      "s_add_u32 s63, s25, 11\n\t"
      "s_and_b32 s63, s63, s22\n\t"
      "s_lshl_b32 s63, s63, s23\n\t"
      "s_add_u32 s64, s25, 12\n\t"
      "s_and_b32 s64, s64, s22\n\t"
      "s_lshl_b32 s64, s64, s23\n\t"
      "s_add_u32 s65, s25, 13\n\t"
      "s_and_b32 s65, s65, s22\n\t"
      "s_lshl_b32 s65, s65, s23\n\t"
      "s_add_u32 s66, s25, 14\n\t"
      "s_and_b32 s66, s66, s22\n\t"
      "s_lshl_b32 s66, s66, s23\n\t"
      "s_add_u32 s67, s25, 15\n\t"
      "s_and_b32 s67, s67, s22\n\t"
      "s_lshl_b32 s67, s67, s23\n\t"
      "s_cmp_lg_u32 s12, 0\n\t"
      "s_cselect_b32 s82, s52, s52\n\t"
      "s_cselect_b32 s83, s53, s53\n\t"
      "s_cselect_b32 s84, s54, s52\n\t"
      "s_cselect_b32 s85, s55, s53\n\t"
      "s_cmp_lg_u32 s12, 0\n\t"
      "s_cselect_b32 s86, s56, s54\n\t"
      "s_cselect_b32 s87, s57, s55\n\t"
      "s_cselect_b32 s88, s58, s54\n\t"
      "s_cselect_b32 s89, s59, s55\n\t"
      "s_waitcnt vmcnt(8)\n\t"
      "s_barrier\n\t"
      "ds_read_b128 a[64:67], v228\n\t"
      "ds_read_b128 a[68:71], v228 offset:4096\n\t"
      "ds_read_b128 a[72:75], v228 offset:8192\n\t"
      "ds_read_b64_tr_b16 a[0:1], v220\n\t"
      "ds_read_b64_tr_b16 a[2:3], v224 offset:1024\n\t"
      "ds_read_b64_tr_b16 a[4:5], v221\n\t"
      "ds_read_b64_tr_b16 a[6:7], v225 offset:1024\n\t"
      "ds_read_b64_tr_b16 a[8:9], v222\n\t"
      "ds_read_b64_tr_b16 a[10:11], v226 offset:1024\n\t"
      "ds_read_b64_tr_b16 a[12:13], v223\n\t"
      "ds_read_b64_tr_b16 a[14:15], v227 offset:1024\n\t"
      "ds_read_b64_tr_b16 a[16:17], v224\n\t"
      "ds_read_b64_tr_b16 a[18:19], v220 offset:1024\n\t"
      "ds_read_b64_tr_b16 a[20:21], v225\n\t"
      "ds_read_b64_tr_b16 a[22:23], v221 offset:1024\n\t"
      "ds_read_b64_tr_b16 a[24:25], v226\n\t"
      "ds_read_b64_tr_b16 a[26:27], v222 offset:1024\n\t"
      "ds_read_b64_tr_b16 a[28:29], v227\n\t"
      "ds_read_b64_tr_b16 a[30:31], v223 offset:1024\n\t"
      "s_mov_b32 m0, s94\n\t"
      "s_nop 0\n\t"
      "buffer_load_dwordx4 v212, s[44:47], s17 offen lds\n\t"
      "buffer_load_dwordx4 v213, s[44:47], s17 offen offset:1024 lds\n\t"
      "buffer_load_dwordx4 v214, s[44:47], s17 offen offset:2048 lds\n\t"
      "buffer_load_dwordx4 v215, s[44:47], s17 offen offset:3072 lds\n\t"
      "s_add_u32 s17, s17, 0x20000\n\t"
      "s_and_b32 s17, s17, 0x3fffff\n\t"
      "s_add_u32 s24, s16, 1\n\t"
      "s_and_b32 s24, s24, 3\n\t"
      "s_lshl_b32 s24, s24, 8\n\t"
      "s_add_u32 m0, s15, 12288\n\t"
      "s_nop 0\n\t"
      "buffer_load_dwordx4 v245, s[48:51], s24 offen lds\n\t"
      "buffer_load_dwordx4 v250, s[48:51], s24 offen offset:1024 lds\n\t"
      "buffer_load_dwordx4 v251, s[48:51], s24 offen offset:2048 lds\n\t"
      "s_mov_b32 m0, s95\n\t"
      "s_nop 0\n\t"
      "buffer_load_dwordx4 v212, s[44:47], s17 offen lds\n\t"
      "buffer_load_dwordx4 v213, s[44:47], s17 offen offset:1024 lds\n\t"
      "buffer_load_dwordx4 v214, s[44:47], s17 offen offset:2048 lds\n\t"
      "buffer_load_dwordx4 v215, s[44:47], s17 offen offset:3072 lds\n\t"
      "s_add_u32 s17, s17, 0x20000\n\t"
      "s_and_b32 s17, s17, 0x3fffff\n\t"
      "s_waitcnt lgkmcnt(0)\n\t"
      "s_add_u32 s4, s4, 0x7f4a7c15\n\t"
      "s_addc_u32 s5, s5, 0x9e3779b9\n\t"
      "s_waitcnt vmcnt(11)\n\t"
      "s_mov_b32 m0, s96\n\t"
      "v_mfma_f32_16x16x32_bf16 v[0:3], a[0:3], a[64:67], 0\n\t"
      "ds_read_b128 a[76:79], v229\n\t"
      "buffer_load_dwordx4 v212, s[44:47], s17 offen lds\n\t"
      "v_mfma_f32_16x16x32_bf16 v[4:7], a[0:3], a[68:71], 0\n\t"
      "ds_read_b128 a[80:83], v229 offset:4096\n\t"
      "v_mfma_f32_16x16x32_bf16 v[8:11], a[0:3], a[72:75], 0\n\t"
      "ds_read_b128 a[84:87], v229 offset:8192\n\t"
      "v_mfma_f32_16x16x32_bf16 v[12:15], a[4:7], a[64:67], 0\n\t"
      "ds_read_b64_tr_b16 a[32:33], v220 offset:8192\n\t"
      "buffer_load_dwordx4 v213, s[44:47], s17 offen offset:1024 lds\n\t"
      "v_mfma_f32_16x16x32_bf16 v[16:19], a[4:7], a[68:71], 0\n\t"
      "ds_read_b64_tr_b16 a[34:35], v224 offset:9216\n\t"
      "v_mfma_f32_16x16x32_bf16 v[20:23], a[4:7], a[72:75], 0\n\t"
      "ds_read_b64_tr_b16 a[36:37], v221 offset:8192\n\t"
      "v_mfma_f32_16x16x32_bf16 v[24:27], a[8:11], a[64:67], 0\n\t"
      "ds_read_b64_tr_b16 a[38:39], v225 offset:9216\n\t"
      "buffer_load_dwordx4 v214, s[44:47], s17 offen offset:2048 lds\n\t"
      "v_mfma_f32_16x16x32_bf16 v[28:31], a[8:11], a[68:71], 0\n\t"
      "ds_read_b64_tr_b16 a[40:41], v222 offset:8192\n\t"
      "v_mfma_f32_16x16x32_bf16 v[32:35], a[8:11], a[72:75], 0\n\t"
      "ds_read_b64_tr_b16 a[42:43], v226 offset:9216\n\t"
      "v_mfma_f32_16x16x32_bf16 v[36:39], a[12:15], a[64:67], 0\n\t"
      "ds_read_b64_tr_b16 a[44:45], v223 offset:8192\n\t"
      "v_mfma_f32_16x16x32_bf16 v[40:43], a[12:15], a[68:71], 0\n\t"
      "buffer_load_dwordx4 v215, s[44:47], s17 offen offset:3072 lds\n\t"
      "s_add_u32 s17, s17, 0x20000\n\t"
      "s_and_b32 s17, s17, 0x3fffff\n\t"
      "s_mov_b32 m0, s97\n\t"
      "s_nop 0\n\t"
      "buffer_load_dwordx4 v212, s[44:47], s17 offen lds\n\t"
      "v_mfma_f32_16x16x32_bf16 v[44:47], a[12:15], a[72:75], 0\n\t"
      "ds_read_b64_tr_b16 a[46:47], v227 offset:9216\n\t"
      "v_mfma_f32_16x16x32_bf16 v[48:51], a[16:19], a[64:67], 0\n\t"
      "ds_read_b64_tr_b16 a[48:49], v224 offset:8192\n\t"
      "v_mfma_f32_16x16x32_bf16 v[52:55], a[16:19], a[68:71], 0\n\t"
      "ds_read_b64_tr_b16 a[50:51], v220 offset:9216\n\t"
      "buffer_load_dwordx4 v213, s[44:47], s17 offen offset:1024 lds\n\t"
      "v_mfma_f32_16x16x32_bf16 v[56:59], a[16:19], a[72:75], 0\n\t"
      "ds_read_b64_tr_b16 a[52:53], v225 offset:8192\n\t"
      "v_mfma_f32_16x16x32_bf16 v[60:63], a[20:23], a[64:67], 0\n\t"
      "ds_read_b64_tr_b16 a[54:55], v221 offset:9216\n\t"
      "v_mfma_f32_16x16x32_bf16 v[64:67], a[20:23], a[68:71], 0\n\t"
      "ds_read_b64_tr_b16 a[56:57], v226 offset:8192\n\t"
      "v_mfma_f32_16x16x32_bf16 v[68:71], a[20:23], a[72:75], 0\n\t"
      "ds_read_b64_tr_b16 a[58:59], v222 offset:9216\n\t"
      "buffer_load_dwordx4 v214, s[44:47], s17 offen offset:2048 lds\n\t"
      "v_mfma_f32_16x16x32_bf16 v[72:75], a[24:27], a[64:67], 0\n\t"
      "ds_read_b64_tr_b16 a[60:61], v227 offset:8192\n\t"
      "v_mfma_f32_16x16x32_bf16 v[76:79], a[24:27], a[68:71], 0\n\t"
      "ds_read_b64_tr_b16 a[62:63], v223 offset:9216\n\t"
      "v_mfma_f32_16x16x32_bf16 v[80:83], a[24:27], a[72:75], 0\n\t"
      "buffer_load_dwordx4 v215, s[44:47], s17 offen offset:3072 lds\n\t"
      "s_add_u32 s17, s17, 0x20000\n\t"
      "s_and_b32 s17, s17, 0x3fffff\n\t"
      "s_mov_b32 m0, s90\n\t"
      "s_nop 0\n\t"
      "buffer_load_dwordx4 v212, s[44:47], s17 offen lds\n\t"
      "v_mfma_f32_16x16x32_bf16 v[84:87], a[28:31], a[64:67], 0\n\t"
      "buffer_load_dwordx4 v213, s[44:47], s17 offen offset:1024 lds\n\t"
      "v_mfma_f32_16x16x32_bf16 v[88:91], a[28:31], a[68:71], 0\n\t"
      "buffer_load_dwordx4 v214, s[44:47], s17 offen offset:2048 lds\n\t"
      "v_mfma_f32_16x16x32_bf16 v[92:95], a[28:31], a[72:75], 0\n\t"
      "buffer_load_dwordx4 v215, s[44:47], s17 offen offset:3072 lds\n\t"
      "s_add_u32 s17, s17, 0x20000\n\t"
      "s_and_b32 s17, s17, 0x3fffff\n\t"
      "s_waitcnt lgkmcnt(0)\n\t"
      "s_waitcnt vmcnt(12)\n\t"
      "s_mov_b32 m0, s91\n\t"
      "v_mfma_f32_16x16x32_bf16 v[0:3], a[32:35], a[76:79], v[0:3]\n\t"
      "ds_read_b128 a[64:67], v230\n\t"
      "buffer_load_dwordx4 v212, s[44:47], s17 offen lds\n\t"
      "v_mfma_f32_16x16x32_bf16 v[4:7], a[32:35], a[80:83], v[4:7]\n\t"
      "ds_read_b128 a[68:71], v230 offset:4096\n\t"
      "v_mfma_f32_16x16x32_bf16 v[8:11], a[32:35], a[84:87], v[8:11]\n\t"
      "ds_read_b128 a[72:75], v230 offset:8192\n\t"
      "v_mfma_f32_16x16x32_bf16 v[12:15], a[36:39], a[76:79], v[12:15]\n\t"
      "ds_read_b64_tr_b16 a[0:1], v220 offset:16384\n\t"
      "buffer_load_dwordx4 v213, s[44:47], s17 offen offset:1024 lds\n\t"
      "v_mfma_f32_16x16x32_bf16 v[16:19], a[36:39], a[80:83], v[16:19]\n\t"
      "ds_read_b64_tr_b16 a[2:3], v224 offset:17408\n\t"
      "v_mfma_f32_16x16x32_bf16 v[20:23], a[36:39], a[84:87], v[20:23]\n\t"
      "ds_read_b64_tr_b16 a[4:5], v221 offset:16384\n\t"
      "v_mfma_f32_16x16x32_bf16 v[24:27], a[40:43], a[76:79], v[24:27]\n\t"
      "ds_read_b64_tr_b16 a[6:7], v225 offset:17408\n\t"
      "buffer_load_dwordx4 v214, s[44:47], s17 offen offset:2048 lds\n\t"
      "v_mfma_f32_16x16x32_bf16 v[28:31], a[40:43], a[80:83], v[28:31]\n\t"
      "ds_read_b64_tr_b16 a[8:9], v222 offset:16384\n\t"
      "v_mfma_f32_16x16x32_bf16 v[32:35], a[40:43], a[84:87], v[32:35]\n\t"
      "ds_read_b64_tr_b16 a[10:11], v226 offset:17408\n\t"
      "v_mfma_f32_16x16x32_bf16 v[36:39], a[44:47], a[76:79], v[36:39]\n\t"
      "ds_read_b64_tr_b16 a[12:13], v223 offset:16384\n\t"
      "v_mfma_f32_16x16x32_bf16 v[40:43], a[44:47], a[80:83], v[40:43]\n\t"
      "buffer_load_dwordx4 v215, s[44:47], s17 offen offset:3072 lds\n\t"
      "s_add_u32 s17, s17, 0x20000\n\t"
      "s_and_b32 s17, s17, 0x3fffff\n\t"
      "s_mov_b32 m0, s92\n\t"
      "s_nop 0\n\t"
      "buffer_load_dwordx4 v212, s[44:47], s17 offen lds\n\t"
      "v_mfma_f32_16x16x32_bf16 v[44:47], a[44:47], a[84:87], v[44:47]\n\t"
      "ds_read_b64_tr_b16 a[14:15], v227 offset:17408\n\t"
      "v_mfma_f32_16x16x32_bf16 v[48:51], a[48:51], a[76:79], v[48:51]\n\t"
      "ds_read_b64_tr_b16 a[16:17], v224 offset:16384\n\t"
      "v_mfma_f32_16x16x32_bf16 v[52:55], a[48:51], a[80:83], v[52:55]\n\t"
      "ds_read_b64_tr_b16 a[18:19], v220 offset:17408\n\t"
      "buffer_load_dwordx4 v213, s[44:47], s17 offen offset:1024 lds\n\t"
      "v_mfma_f32_16x16x32_bf16 v[56:59], a[48:51], a[84:87], v[56:59]\n\t"
      "ds_read_b64_tr_b16 a[20:21], v225 offset:16384\n\t"
      "v_mfma_f32_16x16x32_bf16 v[60:63], a[52:55], a[76:79], v[60:63]\n\t"
      "ds_read_b64_tr_b16 a[22:23], v221 offset:17408\n\t"
      "v_mfma_f32_16x16x32_bf16 v[64:67], a[52:55], a[80:83], v[64:67]\n\t"
      "ds_read_b64_tr_b16 a[24:25], v226 offset:16384\n\t"
      "v_mfma_f32_16x16x32_bf16 v[68:71], a[52:55], a[84:87], v[68:71]\n\t"
      "ds_read_b64_tr_b16 a[26:27], v222 offset:17408\n\t"
      "buffer_load_dwordx4 v214, s[44:47], s17 offen offset:2048 lds\n\t"
      "v_mfma_f32_16x16x32_bf16 v[72:75], a[56:59], a[76:79], v[72:75]\n\t"
      "ds_read_b64_tr_b16 a[28:29], v227 offset:16384\n\t"
      "v_mfma_f32_16x16x32_bf16 v[76:79], a[56:59], a[80:83], v[76:79]\n\t"
      "ds_read_b64_tr_b16 a[30:31], v223 offset:17408\n\t"
      "v_mfma_f32_16x16x32_bf16 v[80:83], a[56:59], a[84:87], v[80:83]\n\t"
      "buffer_load_dwordx4 v215, s[44:47], s17 offen offset:3072 lds\n\t"
      "s_add_u32 s17, s17, 0x20000\n\t"
      "s_and_b32 s17, s17, 0x3fffff\n\t"
      "s_mov_b32 m0, s93\n\t"
      "s_nop 0\n\t"
      "buffer_load_dwordx4 v212, s[44:47], s17 offen lds\n\t"
      "v_mfma_f32_16x16x32_bf16 v[84:87], a[60:63], a[76:79], v[84:87]\n\t"
      "buffer_load_dwordx4 v213, s[44:47], s17 offen offset:1024 lds\n\t"
      "v_mfma_f32_16x16x32_bf16 v[88:91], a[60:63], a[80:83], v[88:91]\n\t"
      "buffer_load_dwordx4 v214, s[44:47], s17 offen offset:2048 lds\n\t"
      "v_mfma_f32_16x16x32_bf16 v[92:95], a[60:63], a[84:87], v[92:95]\n\t"
      "buffer_load_dwordx4 v215, s[44:47], s17 offen offset:3072 lds\n\t"
      "s_add_u32 s17, s17, 0x20000\n\t"
      "s_and_b32 s17, s17, 0x3fffff\n\t"
      "s_waitcnt lgkmcnt(0)\n\t"
      "s_waitcnt vmcnt(16)\n\t"
      "s_mov_b32 m0, s94\n\t"
      "v_mfma_f32_16x16x32_bf16 v[0:3], a[0:3], a[64:67], v[0:3]\n\t"
      "ds_read_b128 a[76:79], v231\n\t"
      "buffer_load_dwordx4 v212, s[44:47], s17 offen lds\n\t"
      "v_mfma_f32_16x16x32_bf16 v[4:7], a[0:3], a[68:71], v[4:7]\n\t"
      "ds_read_b128 a[80:83], v231 offset:4096\n\t"
      "v_mfma_f32_16x16x32_bf16 v[8:11], a[0:3], a[72:75], v[8:11]\n\t"
      "ds_read_b128 a[84:87], v231 offset:8192\n\t"
      "v_mfma_f32_16x16x32_bf16 v[12:15], a[4:7], a[64:67], v[12:15]\n\t"
      "ds_read_b64_tr_b16 a[32:33], v220 offset:24576\n\t"
      "v_mfma_f32_16x16x32_bf16 v[16:19], a[4:7], a[68:71], v[16:19]\n\t"
      "ds_read_b64_tr_b16 a[34:35], v224 offset:25600\n\t"
      "v_mfma_f32_16x16x32_bf16 v[20:23], a[4:7], a[72:75], v[20:23]\n\t"
      "ds_read_b64_tr_b16 a[36:37], v221 offset:24576\n\t"
      "v_mfma_f32_16x16x32_bf16 v[24:27], a[8:11], a[64:67], v[24:27]\n\t"
      "ds_read_b64_tr_b16 a[38:39], v225 offset:25600\n\t"
      "v_mfma_f32_16x16x32_bf16 v[28:31], a[8:11], a[68:71], v[28:31]\n\t"
      "ds_read_b64_tr_b16 a[40:41], v222 offset:24576\n\t"
      "v_mfma_f32_16x16x32_bf16 v[32:35], a[8:11], a[72:75], v[32:35]\n\t"
      "ds_read_b64_tr_b16 a[42:43], v226 offset:25600\n\t"
      "buffer_load_dwordx4 v213, s[44:47], s17 offen offset:1024 lds\n\t"
      "v_mfma_f32_16x16x32_bf16 v[36:39], a[12:15], a[64:67], v[36:39]\n\t"
      "ds_read_b64_tr_b16 a[44:45], v223 offset:24576\n\t"
      "v_mfma_f32_16x16x32_bf16 v[40:43], a[12:15], a[68:71], v[40:43]\n\t"
      "buffer_load_dwordx4 v214, s[44:47], s17 offen offset:2048 lds\n\t"
      "v_mfma_f32_16x16x32_bf16 v[44:47], a[12:15], a[72:75], v[44:47]\n\t"
      "ds_read_b64_tr_b16 a[46:47], v227 offset:25600\n\t"
      "v_mfma_f32_16x16x32_bf16 v[48:51], a[16:19], a[64:67], v[48:51]\n\t"
      "ds_read_b64_tr_b16 a[48:49], v224 offset:24576\n\t"
      "v_mfma_f32_16x16x32_bf16 v[52:55], a[16:19], a[68:71], v[52:55]\n\t"
      "ds_read_b64_tr_b16 a[50:51], v220 offset:25600\n\t"
      "v_mfma_f32_16x16x32_bf16 v[56:59], a[16:19], a[72:75], v[56:59]\n\t"
      "ds_read_b64_tr_b16 a[52:53], v225 offset:24576\n\t"
      "v_mfma_f32_16x16x32_bf16 v[60:63], a[20:23], a[64:67], v[60:63]\n\t"
      "ds_read_b64_tr_b16 a[54:55], v221 offset:25600\n\t"
      "v_mfma_f32_16x16x32_bf16 v[64:67], a[20:23], a[68:71], v[64:67]\n\t"
      "ds_read_b64_tr_b16 a[56:57], v226 offset:24576\n\t"
      "buffer_load_dwordx4 v215, s[44:47], s17 offen offset:3072 lds\n\t"
      "v_mfma_f32_16x16x32_bf16 v[68:71], a[20:23], a[72:75], v[68:71]\n\t"
      "ds_read_b64_tr_b16 a[58:59], v222 offset:25600\n\t"
      "v_mfma_f32_16x16x32_bf16 v[72:75], a[24:27], a[64:67], v[72:75]\n\t"
      "ds_read_b64_tr_b16 a[60:61], v227 offset:24576\n\t"
      "v_mfma_f32_16x16x32_bf16 v[76:79], a[24:27], a[68:71], v[76:79]\n\t"
      "ds_read_b64_tr_b16 a[62:63], v223 offset:25600\n\t"
      "s_add_u32 s17, s17, 0x20000\n\t"
      "s_and_b32 s17, s17, 0x3fffff\n\t"
      "s_mov_b32 m0, s95\n\t"
      "v_mfma_f32_16x16x32_bf16 v[80:83], a[24:27], a[72:75], v[80:83]\n\t"
      "buffer_load_dwordx4 v212, s[44:47], s17 offen lds\n\t"
      "v_mfma_f32_16x16x32_bf16 v[84:87], a[28:31], a[64:67], v[84:87]\n\t"
      "buffer_load_dwordx4 v213, s[44:47], s17 offen offset:1024 lds\n\t"
      "v_mfma_f32_16x16x32_bf16 v[88:91], a[28:31], a[68:71], v[88:91]\n\t"
      "buffer_load_dwordx4 v214, s[44:47], s17 offen offset:2048 lds\n\t"
      "v_mfma_f32_16x16x32_bf16 v[92:95], a[28:31], a[72:75], v[92:95]\n\t"
      "buffer_load_dwordx4 v215, s[44:47], s17 offen offset:3072 lds\n\t"
      "s_add_u32 s17, s17, 0x20000\n\t"
      "s_and_b32 s17, s17, 0x3fffff\n\t"
      "s_waitcnt lgkmcnt(0)\n\t"
      "s_waitcnt vmcnt(16)\n\t"
      "s_barrier\n\t"
      "s_add_u32 s24, s16, 2\n\t"
      "s_and_b32 s24, s24, 3\n\t"
      "s_lshl_b32 s24, s24, 8\n\t"
      "s_mov_b32 m0, s15\n\t"
      "v_mfma_f32_16x16x32_bf16 v[0:3], a[32:35], a[76:79], v[0:3]\n\t"
      "ds_read_b128 a[64:67], v228 offset:12288\n\t"
      "buffer_load_dwordx4 v245, s[48:51], s24 offen lds\n\t"
      "v_mfma_f32_16x16x32_bf16 v[4:7], a[32:35], a[80:83], v[4:7]\n\t"
      "ds_read_b128 a[68:71], v228 offset:16384\n\t"
      "v_mfma_f32_16x16x32_bf16 v[8:11], a[32:35], a[84:87], v[8:11]\n\t"
      "ds_read_b128 a[72:75], v228 offset:20480\n\t"
      "v_mfma_f32_16x16x32_bf16 v[12:15], a[36:39], a[76:79], v[12:15]\n\t"
      "ds_read_b64_tr_b16 a[0:1], v220\n\t"
      "v_mfma_f32_16x16x32_bf16 v[16:19], a[36:39], a[80:83], v[16:19]\n\t"
      "ds_read_b64_tr_b16 a[2:3], v224 offset:1024\n\t"
      "buffer_load_dwordx4 v250, s[48:51], s24 offen offset:1024 lds\n\t"
      "v_mfma_f32_16x16x32_bf16 v[20:23], a[36:39], a[84:87], v[20:23]\n\t"
      "ds_read_b64_tr_b16 a[4:5], v221\n\t"
      "v_mfma_f32_16x16x32_bf16 v[24:27], a[40:43], a[76:79], v[24:27]\n\t"
      "ds_read_b64_tr_b16 a[6:7], v225 offset:1024\n\t"
      "v_mfma_f32_16x16x32_bf16 v[28:31], a[40:43], a[80:83], v[28:31]\n\t"
      "ds_read_b64_tr_b16 a[8:9], v222\n\t"
      "v_mfma_f32_16x16x32_bf16 v[32:35], a[40:43], a[84:87], v[32:35]\n\t"
      "ds_read_b64_tr_b16 a[10:11], v226 offset:1024\n\t"
      "buffer_load_dwordx4 v251, s[48:51], s24 offen offset:2048 lds\n\t"
      "v_mfma_f32_16x16x32_bf16 v[36:39], a[44:47], a[76:79], v[36:39]\n\t"
      "ds_read_b64_tr_b16 a[12:13], v223\n\t"
      "s_mov_b32 m0, s96\n\t"
      "v_mfma_f32_16x16x32_bf16 v[40:43], a[44:47], a[80:83], v[40:43]\n\t"
      "buffer_load_dwordx4 v212, s[44:47], s17 offen lds\n\t"
      "v_mfma_f32_16x16x32_bf16 v[44:47], a[44:47], a[84:87], v[44:47]\n\t"
      "ds_read_b64_tr_b16 a[14:15], v227 offset:1024\n\t"
      "v_mfma_f32_16x16x32_bf16 v[48:51], a[48:51], a[76:79], v[48:51]\n\t"
      "ds_read_b64_tr_b16 a[16:17], v224\n\t"
      "buffer_load_dwordx4 v213, s[44:47], s17 offen offset:1024 lds\n\t"
      "v_mfma_f32_16x16x32_bf16 v[52:55], a[48:51], a[80:83], v[52:55]\n\t"
      "ds_read_b64_tr_b16 a[18:19], v220 offset:1024\n\t"
      "v_mfma_f32_16x16x32_bf16 v[56:59], a[48:51], a[84:87], v[56:59]\n\t"
      "ds_read_b64_tr_b16 a[20:21], v225\n\t"
      "v_mfma_f32_16x16x32_bf16 v[60:63], a[52:55], a[76:79], v[60:63]\n\t"
      "ds_read_b64_tr_b16 a[22:23], v221 offset:1024\n\t"
      "v_mfma_f32_16x16x32_bf16 v[64:67], a[52:55], a[80:83], v[64:67]\n\t"
      "ds_read_b64_tr_b16 a[24:25], v226\n\t"
      "buffer_load_dwordx4 v214, s[44:47], s17 offen offset:2048 lds\n\t"
      "v_mfma_f32_16x16x32_bf16 v[68:71], a[52:55], a[84:87], v[68:71]\n\t"
      "ds_read_b64_tr_b16 a[26:27], v222 offset:1024\n\t"
      "v_mfma_f32_16x16x32_bf16 v[72:75], a[56:59], a[76:79], v[72:75]\n\t"
      "ds_read_b64_tr_b16 a[28:29], v227\n\t"
      "v_mfma_f32_16x16x32_bf16 v[76:79], a[56:59], a[80:83], v[76:79]\n\t"
      "ds_read_b64_tr_b16 a[30:31], v223 offset:1024\n\t"
      "v_mfma_f32_16x16x32_bf16 v[80:83], a[56:59], a[84:87], v[80:83]\n\t"
      "buffer_load_dwordx4 v215, s[44:47], s17 offen offset:3072 lds\n\t"
      "s_add_u32 s17, s17, 0x20000\n\t"
      "s_and_b32 s17, s17, 0x3fffff\n\t"
      "s_mov_b32 m0, s97\n\t"
      "s_nop 0\n\t"
      "buffer_load_dwordx4 v212, s[44:47], s17 offen lds\n\t"
      "v_mfma_f32_16x16x32_bf16 v[84:87], a[60:63], a[76:79], v[84:87]\n\t"
      "buffer_load_dwordx4 v213, s[44:47], s17 offen offset:1024 lds\n\t"
      "v_mfma_f32_16x16x32_bf16 v[88:91], a[60:63], a[80:83], v[88:91]\n\t"
      "buffer_load_dwordx4 v214, s[44:47], s17 offen offset:2048 lds\n\t"
      "v_mfma_f32_16x16x32_bf16 v[92:95], a[60:63], a[84:87], v[92:95]\n\t"
      "buffer_load_dwordx4 v215, s[44:47], s17 offen offset:3072 lds\n\t"
      "s_add_u32 s17, s17, 0x20000\n\t"
      "s_and_b32 s17, s17, 0x3fffff\n\t"
      "s_waitcnt lgkmcnt(0)\n\t"
      "s_waitcnt vmcnt(19)\n\t"
      "s_mov_b32 m0, s90\n\t"
      "v_mfma_f32_16x16x32_bf16 v[0:3], a[0:3], a[64:67], v[0:3]\n\t"
      "ds_read_b128 a[76:79], v229 offset:12288\n\t"
      "buffer_load_dwordx4 v212, s[44:47], s17 offen lds\n\t"
      "v_mfma_f32_16x16x32_bf16 v[4:7], a[0:3], a[68:71], v[4:7]\n\t"
      "ds_read_b128 a[80:83], v229 offset:16384\n\t"
      "v_mfma_f32_16x16x32_bf16 v[8:11], a[0:3], a[72:75], v[8:11]\n\t"
      "ds_read_b128 a[84:87], v229 offset:20480\n\t"
      "v_mfma_f32_16x16x32_bf16 v[12:15], a[4:7], a[64:67], v[12:15]\n\t"
      "ds_read_b64_tr_b16 a[32:33], v220 offset:8192\n\t"
      "v_mfma_f32_16x16x32_bf16 v[16:19], a[4:7], a[68:71], v[16:19]\n\t"
      "ds_read_b64_tr_b16 a[34:35], v224 offset:9216\n\t"
      "v_mfma_f32_16x16x32_bf16 v[20:23], a[4:7], a[72:75], v[20:23]\n\t"
      "ds_read_b64_tr_b16 a[36:37], v221 offset:8192\n\t"
      "v_mfma_f32_16x16x32_bf16 v[24:27], a[8:11], a[64:67], v[24:27]\n\t"
      "ds_read_b64_tr_b16 a[38:39], v225 offset:9216\n\t"
      "v_mfma_f32_16x16x32_bf16 v[28:31], a[8:11], a[68:71], v[28:31]\n\t"
      "ds_read_b64_tr_b16 a[40:41], v222 offset:8192\n\t"
      "v_mfma_f32_16x16x32_bf16 v[32:35], a[8:11], a[72:75], v[32:35]\n\t"
      "ds_read_b64_tr_b16 a[42:43], v226 offset:9216\n\t"
      "buffer_load_dwordx4 v213, s[44:47], s17 offen offset:1024 lds\n\t"
      "v_mfma_f32_16x16x32_bf16 v[36:39], a[12:15], a[64:67], v[36:39]\n\t"
      "ds_read_b64_tr_b16 a[44:45], v223 offset:8192\n\t"
      "v_mfma_f32_16x16x32_bf16 v[40:43], a[12:15], a[68:71], v[40:43]\n\t"
      "buffer_load_dwordx4 v214, s[44:47], s17 offen offset:2048 lds\n\t"
      "v_mfma_f32_16x16x32_bf16 v[44:47], a[12:15], a[72:75], v[44:47]\n\t"
      "ds_read_b64_tr_b16 a[46:47], v227 offset:9216\n\t"
      "v_mfma_f32_16x16x32_bf16 v[48:51], a[16:19], a[64:67], v[48:51]\n\t"
      "ds_read_b64_tr_b16 a[48:49], v224 offset:8192\n\t"
      "v_mfma_f32_16x16x32_bf16 v[52:55], a[16:19], a[68:71], v[52:55]\n\t"
      "ds_read_b64_tr_b16 a[50:51], v220 offset:9216\n\t"
      "v_mfma_f32_16x16x32_bf16 v[56:59], a[16:19], a[72:75], v[56:59]\n\t"
      "ds_read_b64_tr_b16 a[52:53], v225 offset:8192\n\t"
      "v_mfma_f32_16x16x32_bf16 v[60:63], a[20:23], a[64:67], v[60:63]\n\t"
      "ds_read_b64_tr_b16 a[54:55], v221 offset:9216\n\t"
      "v_mfma_f32_16x16x32_bf16 v[64:67], a[20:23], a[68:71], v[64:67]\n\t"
      "ds_read_b64_tr_b16 a[56:57], v226 offset:8192\n\t"
      "buffer_load_dwordx4 v215, s[44:47], s17 offen offset:3072 lds\n\t"
      "v_mfma_f32_16x16x32_bf16 v[68:71], a[20:23], a[72:75], v[68:71]\n\t"
      "ds_read_b64_tr_b16 a[58:59], v222 offset:9216\n\t"
      "v_mfma_f32_16x16x32_bf16 v[72:75], a[24:27], a[64:67], v[72:75]\n\t"
      "ds_read_b64_tr_b16 a[60:61], v227 offset:8192\n\t"
      "v_mfma_f32_16x16x32_bf16 v[76:79], a[24:27], a[68:71], v[76:79]\n\t"
      "ds_read_b64_tr_b16 a[62:63], v223 offset:9216\n\t"
      "s_add_u32 s17, s17, 0x20000\n\t"
      "s_and_b32 s17, s17, 0x3fffff\n\t"
      "s_mov_b32 m0, s91\n\t"
      "v_mfma_f32_16x16x32_bf16 v[80:83], a[24:27], a[72:75], v[80:83]\n\t"
      "buffer_load_dwordx4 v212, s[44:47], s17 offen lds\n\t"
      "v_mfma_f32_16x16x32_bf16 v[84:87], a[28:31], a[64:67], v[84:87]\n\t"
      "buffer_load_dwordx4 v213, s[44:47], s17 offen offset:1024 lds\n\t"
      "v_mfma_f32_16x16x32_bf16 v[88:91], a[28:31], a[68:71], v[88:91]\n\t"
      "buffer_load_dwordx4 v214, s[44:47], s17 offen offset:2048 lds\n\t"
      "v_mfma_f32_16x16x32_bf16 v[92:95], a[28:31], a[72:75], v[92:95]\n\t"
      "buffer_load_dwordx4 v215, s[44:47], s17 offen offset:3072 lds\n\t"
      "s_add_u32 s17, s17, 0x20000\n\t"
      "s_and_b32 s17, s17, 0x3fffff\n\t"
      "s_waitcnt lgkmcnt(0)\n\t"
      "s_waitcnt vmcnt(19)\n\t"
      "s_mov_b32 m0, s92\n\t"
      "v_mfma_f32_16x16x32_bf16 v[0:3], a[32:35], a[76:79], v[0:3]\n\t"
      "ds_read_b128 a[64:67], v230 offset:12288\n\t"
      "buffer_load_dwordx4 v212, s[44:47], s17 offen lds\n\t"
      "v_mfma_f32_16x16x32_bf16 v[4:7], a[32:35], a[80:83], v[4:7]\n\t"
      "ds_read_b128 a[68:71], v230 offset:16384\n\t"
      "v_mfma_f32_16x16x32_bf16 v[8:11], a[32:35], a[84:87], v[8:11]\n\t"
      "ds_read_b128 a[72:75], v230 offset:20480\n\t"
      "v_mfma_f32_16x16x32_bf16 v[12:15], a[36:39], a[76:79], v[12:15]\n\t"
      "ds_read_b64_tr_b16 a[0:1], v220 offset:16384\n\t"
      "v_mfma_f32_16x16x32_bf16 v[16:19], a[36:39], a[80:83], v[16:19]\n\t"
      "ds_read_b64_tr_b16 a[2:3], v224 offset:17408\n\t"
      "v_mfma_f32_16x16x32_bf16 v[20:23], a[36:39], a[84:87], v[20:23]\n\t"
      "ds_read_b64_tr_b16 a[4:5], v221 offset:16384\n\t"
      "v_mfma_f32_16x16x32_bf16 v[24:27], a[40:43], a[76:79], v[24:27]\n\t"
      "ds_read_b64_tr_b16 a[6:7], v225 offset:17408\n\t"
      "v_mfma_f32_16x16x32_bf16 v[28:31], a[40:43], a[80:83], v[28:31]\n\t"
      "ds_read_b64_tr_b16 a[8:9], v222 offset:16384\n\t"
      "v_mfma_f32_16x16x32_bf16 v[32:35], a[40:43], a[84:87], v[32:35]\n\t"
      "ds_read_b64_tr_b16 a[10:11], v226 offset:17408\n\t"
      "buffer_load_dwordx4 v213, s[44:47], s17 offen offset:1024 lds\n\t"
      "v_mfma_f32_16x16x32_bf16 v[36:39], a[44:47], a[76:79], v[36:39]\n\t"
      "ds_read_b64_tr_b16 a[12:13], v223 offset:16384\n\t"
      "v_mfma_f32_16x16x32_bf16 v[40:43], a[44:47], a[80:83], v[40:43]\n\t"
      "buffer_load_dwordx4 v214, s[44:47], s17 offen offset:2048 lds\n\t"
      "v_mfma_f32_16x16x32_bf16 v[44:47], a[44:47], a[84:87], v[44:47]\n\t"
      "ds_read_b64_tr_b16 a[14:15], v227 offset:17408\n\t"
      "v_mfma_f32_16x16x32_bf16 v[48:51], a[48:51], a[76:79], v[48:51]\n\t"
      "ds_read_b64_tr_b16 a[16:17], v224 offset:16384\n\t"
      "v_mfma_f32_16x16x32_bf16 v[52:55], a[48:51], a[80:83], v[52:55]\n\t"
      "ds_read_b64_tr_b16 a[18:19], v220 offset:17408\n\t"
      "v_mfma_f32_16x16x32_bf16 v[56:59], a[48:51], a[84:87], v[56:59]\n\t"
      "ds_read_b64_tr_b16 a[20:21], v225 offset:16384\n\t"
      "v_mfma_f32_16x16x32_bf16 v[60:63], a[52:55], a[76:79], v[60:63]\n\t"
      "ds_read_b64_tr_b16 a[22:23], v221 offset:17408\n\t"
      "v_mfma_f32_16x16x32_bf16 v[64:67], a[52:55], a[80:83], v[64:67]\n\t"
      "ds_read_b64_tr_b16 a[24:25], v226 offset:16384\n\t"
      "buffer_load_dwordx4 v215, s[44:47], s17 offen offset:3072 lds\n\t"
      "v_mfma_f32_16x16x32_bf16 v[68:71], a[52:55], a[84:87], v[68:71]\n\t"
      "ds_read_b64_tr_b16 a[26:27], v222 offset:17408\n\t"
      "v_mfma_f32_16x16x32_bf16 v[72:75], a[56:59], a[76:79], v[72:75]\n\t"
      "ds_read_b64_tr_b16 a[28:29], v227 offset:16384\n\t"
      "v_mfma_f32_16x16x32_bf16 v[76:79], a[56:59], a[80:83], v[76:79]\n\t"
      "ds_read_b64_tr_b16 a[30:31], v223 offset:17408\n\t"
      "s_add_u32 s17, s17, 0x20000\n\t"
      "s_and_b32 s17, s17, 0x3fffff\n\t"
      "s_mov_b32 m0, s93\n\t"
      "v_mfma_f32_16x16x32_bf16 v[80:83], a[56:59], a[84:87], v[80:83]\n\t"
      "buffer_load_dwordx4 v212, s[44:47], s17 offen lds\n\t"
      "v_mfma_f32_16x16x32_bf16 v[84:87], a[60:63], a[76:79], v[84:87]\n\t"
      "buffer_load_dwordx4 v213, s[44:47], s17 offen offset:1024 lds\n\t"
      "v_mfma_f32_16x16x32_bf16 v[88:91], a[60:63], a[80:83], v[88:91]\n\t"
      "buffer_load_dwordx4 v214, s[44:47], s17 offen offset:2048 lds\n\t"
      "v_mfma_f32_16x16x32_bf16 v[92:95], a[60:63], a[84:87], v[92:95]\n\t"
      "buffer_load_dwordx4 v215, s[44:47], s17 offen offset:3072 lds\n\t"
      "s_add_u32 s17, s17, 0x20000\n\t"
      "s_and_b32 s17, s17, 0x3fffff\n\t"
      "s_waitcnt lgkmcnt(0)\n\t"
      "s_waitcnt vmcnt(16)\n\t"
      "s_mov_b32 m0, s94\n\t"
      "v_mfma_f32_16x16x32_bf16 v[0:3], a[0:3], a[64:67], v[0:3]\n\t"
      "ds_read_b128 a[76:79], v231 offset:12288\n\t"
      "buffer_load_dwordx4 v212, s[44:47], s17 offen lds\n\t"
      "v_mfma_f32_16x16x32_bf16 v[4:7], a[0:3], a[68:71], v[4:7]\n\t"
      "ds_read_b128 a[80:83], v231 offset:16384\n\t"
      "v_mfma_f32_16x16x32_bf16 v[8:11], a[0:3], a[72:75], v[8:11]\n\t"
      "ds_read_b128 a[84:87], v231 offset:20480\n\t"
      "v_mfma_f32_16x16x32_bf16 v[12:15], a[4:7], a[64:67], v[12:15]\n\t"
      "ds_read_b64_tr_b16 a[32:33], v220 offset:24576\n\t"
      "v_mfma_f32_16x16x32_bf16 v[16:19], a[4:7], a[68:71], v[16:19]\n\t"
      "ds_read_b64_tr_b16 a[34:35], v224 offset:25600\n\t"
      "v_mfma_f32_16x16x32_bf16 v[20:23], a[4:7], a[72:75], v[20:23]\n\t"
      "ds_read_b64_tr_b16 a[36:37], v221 offset:24576\n\t"
      "v_mfma_f32_16x16x32_bf16 v[24:27], a[8:11], a[64:67], v[24:27]\n\t"
      "ds_read_b64_tr_b16 a[38:39], v225 offset:25600\n\t"
      "v_mfma_f32_16x16x32_bf16 v[28:31], a[8:11], a[68:71], v[28:31]\n\t"
      "ds_read_b64_tr_b16 a[40:41], v222 offset:24576\n\t"
      "v_mfma_f32_16x16x32_bf16 v[32:35], a[8:11], a[72:75], v[32:35]\n\t"
      "ds_read_b64_tr_b16 a[42:43], v226 offset:25600\n\t"
      "buffer_load_dwordx4 v213, s[44:47], s17 offen offset:1024 lds\n\t"
      "v_mfma_f32_16x16x32_bf16 v[36:39], a[12:15], a[64:67], v[36:39]\n\t"
      "ds_read_b64_tr_b16 a[44:45], v223 offset:24576\n\t"
      "v_mfma_f32_16x16x32_bf16 v[40:43], a[12:15], a[68:71], v[40:43]\n\t"
      "buffer_load_dwordx4 v214, s[44:47], s17 offen offset:2048 lds\n\t"
      "v_mfma_f32_16x16x32_bf16 v[44:47], a[12:15], a[72:75], v[44:47]\n\t"
      "ds_read_b64_tr_b16 a[46:47], v227 offset:25600\n\t"
      "v_mfma_f32_16x16x32_bf16 v[48:51], a[16:19], a[64:67], v[48:51]\n\t"
      "ds_read_b64_tr_b16 a[48:49], v224 offset:24576\n\t"
      "v_mfma_f32_16x16x32_bf16 v[52:55], a[16:19], a[68:71], v[52:55]\n\t"
      "ds_read_b64_tr_b16 a[50:51], v220 offset:25600\n\t"
      "v_mfma_f32_16x16x32_bf16 v[56:59], a[16:19], a[72:75], v[56:59]\n\t"
      "ds_read_b64_tr_b16 a[52:53], v225 offset:24576\n\t"
      "v_mfma_f32_16x16x32_bf16 v[60:63], a[20:23], a[64:67], v[60:63]\n\t"
      "ds_read_b64_tr_b16 a[54:55], v221 offset:25600\n\t"
      "v_mfma_f32_16x16x32_bf16 v[64:67], a[20:23], a[68:71], v[64:67]\n\t"
      "ds_read_b64_tr_b16 a[56:57], v226 offset:24576\n\t"
      "buffer_load_dwordx4 v215, s[44:47], s17 offen offset:3072 lds\n\t"
      "v_mfma_f32_16x16x32_bf16 v[68:71], a[20:23], a[72:75], v[68:71]\n\t"
      "ds_read_b64_tr_b16 a[58:59], v222 offset:25600\n\t"
      "v_mfma_f32_16x16x32_bf16 v[72:75], a[24:27], a[64:67], v[72:75]\n\t"
      "ds_read_b64_tr_b16 a[60:61], v227 offset:24576\n\t"
      "v_mfma_f32_16x16x32_bf16 v[76:79], a[24:27], a[68:71], v[76:79]\n\t"
      "ds_read_b64_tr_b16 a[62:63], v223 offset:25600\n\t"
      "s_add_u32 s17, s17, 0x20000\n\t"
      "s_and_b32 s17, s17, 0x3fffff\n\t"
      "s_mov_b32 m0, s95\n\t"
      "v_mfma_f32_16x16x32_bf16 v[80:83], a[24:27], a[72:75], v[80:83]\n\t"
      "buffer_load_dwordx4 v212, s[44:47], s17 offen lds\n\t"
      "v_mfma_f32_16x16x32_bf16 v[84:87], a[28:31], a[64:67], v[84:87]\n\t"
      "buffer_load_dwordx4 v213, s[44:47], s17 offen offset:1024 lds\n\t"
      "v_mfma_f32_16x16x32_bf16 v[88:91], a[28:31], a[68:71], v[88:91]\n\t"
      "buffer_load_dwordx4 v214, s[44:47], s17 offen offset:2048 lds\n\t"
      "v_mfma_f32_16x16x32_bf16 v[92:95], a[28:31], a[72:75], v[92:95]\n\t"
      "buffer_load_dwordx4 v215, s[44:47], s17 offen offset:3072 lds\n\t"
      "s_add_u32 s17, s17, 0x20000\n\t"
      "s_and_b32 s17, s17, 0x3fffff\n\t"
      "s_waitcnt lgkmcnt(0)\n\t"
      "s_waitcnt vmcnt(16)\n\t"
      "s_barrier\n\t"
      "s_add_u32 s24, s16, 3\n\t"
      "s_and_b32 s24, s24, 3\n\t"
      "s_lshl_b32 s24, s24, 8\n\t"
      "s_add_u32 m0, s15, 12288\n\t"
      "v_mfma_f32_16x16x32_bf16 v[0:3], a[32:35], a[76:79], v[0:3]\n\t"
      "ds_read_b128 a[64:67], v228\n\t"
      "buffer_load_dwordx4 v245, s[48:51], s24 offen lds\n\t"
      "v_mfma_f32_16x16x32_bf16 v[4:7], a[32:35], a[80:83], v[4:7]\n\t"
      "ds_read_b128 a[68:71], v228 offset:4096\n\t"
      "v_mfma_f32_16x16x32_bf16 v[8:11], a[32:35], a[84:87], v[8:11]\n\t"
      "ds_read_b128 a[72:75], v228 offset:8192\n\t"
      "v_mfma_f32_16x16x32_bf16 v[12:15], a[36:39], a[76:79], v[12:15]\n\t"
      "ds_read_b64_tr_b16 a[0:1], v220\n\t"
      "v_mfma_f32_16x16x32_bf16 v[16:19], a[36:39], a[80:83], v[16:19]\n\t"
      "ds_read_b64_tr_b16 a[2:3], v224 offset:1024\n\t"
      "buffer_load_dwordx4 v250, s[48:51], s24 offen offset:1024 lds\n\t"
      "v_mfma_f32_16x16x32_bf16 v[20:23], a[36:39], a[84:87], v[20:23]\n\t"
      "ds_read_b64_tr_b16 a[4:5], v221\n\t"
      "v_mfma_f32_16x16x32_bf16 v[24:27], a[40:43], a[76:79], v[24:27]\n\t"
      "ds_read_b64_tr_b16 a[6:7], v225 offset:1024\n\t"
      "v_mfma_f32_16x16x32_bf16 v[28:31], a[40:43], a[80:83], v[28:31]\n\t"
      "ds_read_b64_tr_b16 a[8:9], v222\n\t"
      "v_mfma_f32_16x16x32_bf16 v[32:35], a[40:43], a[84:87], v[32:35]\n\t"
      "ds_read_b64_tr_b16 a[10:11], v226 offset:1024\n\t"
      "buffer_load_dwordx4 v251, s[48:51], s24 offen offset:2048 lds\n\t"
      "v_mfma_f32_16x16x32_bf16 v[36:39], a[44:47], a[76:79], v[36:39]\n\t"
      "ds_read_b64_tr_b16 a[12:13], v223\n\t"
      "s_mov_b32 m0, s96\n\t"
      "v_mfma_f32_16x16x32_bf16 v[40:43], a[44:47], a[80:83], v[40:43]\n\t"
      "buffer_load_dwordx4 v212, s[44:47], s17 offen lds\n\t"
      "v_mfma_f32_16x16x32_bf16 v[44:47], a[44:47], a[84:87], v[44:47]\n\t"
      "ds_read_b64_tr_b16 a[14:15], v227 offset:1024\n\t"
      "v_mfma_f32_16x16x32_bf16 v[48:51], a[48:51], a[76:79], v[48:51]\n\t"
      "ds_read_b64_tr_b16 a[16:17], v224\n\t"
      "buffer_load_dwordx4 v213, s[44:47], s17 offen offset:1024 lds\n\t"
      "v_mfma_f32_16x16x32_bf16 v[52:55], a[48:51], a[80:83], v[52:55]\n\t"
      "ds_read_b64_tr_b16 a[18:19], v220 offset:1024\n\t"
      "v_mfma_f32_16x16x32_bf16 v[56:59], a[48:51], a[84:87], v[56:59]\n\t"
      "ds_read_b64_tr_b16 a[20:21], v225\n\t"
      "v_mfma_f32_16x16x32_bf16 v[60:63], a[52:55], a[76:79], v[60:63]\n\t"
      "ds_read_b64_tr_b16 a[22:23], v221 offset:1024\n\t"
      "v_mfma_f32_16x16x32_bf16 v[64:67], a[52:55], a[80:83], v[64:67]\n\t"
      "ds_read_b64_tr_b16 a[24:25], v226\n\t"
      "buffer_load_dwordx4 v214, s[44:47], s17 offen offset:2048 lds\n\t"
      "v_mfma_f32_16x16x32_bf16 v[68:71], a[52:55], a[84:87], v[68:71]\n\t"
      "ds_read_b64_tr_b16 a[26:27], v222 offset:1024\n\t"
      "v_mfma_f32_16x16x32_bf16 v[72:75], a[56:59], a[76:79], v[72:75]\n\t"
      "ds_read_b64_tr_b16 a[28:29], v227\n\t"
      "v_mfma_f32_16x16x32_bf16 v[76:79], a[56:59], a[80:83], v[76:79]\n\t"
      "ds_read_b64_tr_b16 a[30:31], v223 offset:1024\n\t"
      "v_mfma_f32_16x16x32_bf16 v[80:83], a[56:59], a[84:87], v[80:83]\n\t"
      "buffer_load_dwordx4 v215, s[44:47], s17 offen offset:3072 lds\n\t"
      "s_add_u32 s17, s17, 0x20000\n\t"
      "s_and_b32 s17, s17, 0x3fffff\n\t"
      "s_mov_b32 m0, s97\n\t"
      "s_nop 0\n\t"
      "buffer_load_dwordx4 v212, s[44:47], s17 offen lds\n\t"
      "v_mfma_f32_16x16x32_bf16 v[84:87], a[60:63], a[76:79], v[84:87]\n\t"
      "buffer_load_dwordx4 v213, s[44:47], s17 offen offset:1024 lds\n\t"
      "v_mfma_f32_16x16x32_bf16 v[88:91], a[60:63], a[80:83], v[88:91]\n\t"
      "buffer_load_dwordx4 v214, s[44:47], s17 offen offset:2048 lds\n\t"
      "v_mfma_f32_16x16x32_bf16 v[92:95], a[60:63], a[84:87], v[92:95]\n\t"
      "buffer_load_dwordx4 v215, s[44:47], s17 offen offset:3072 lds\n\t"
      "s_add_u32 s17, s17, 0x20000\n\t"
      "s_and_b32 s17, s17, 0x3fffff\n\t"
      "s_waitcnt lgkmcnt(0)\n\t"
      "s_waitcnt vmcnt(19)\n\t"
      "s_mov_b32 m0, s90\n\t"
      "v_mfma_f32_16x16x32_bf16 v[0:3], a[0:3], a[64:67], v[0:3]\n\t"
      "ds_read_b128 a[76:79], v229\n\t"
      "buffer_load_dwordx4 v212, s[44:47], s17 offen lds\n\t"
      "v_mfma_f32_16x16x32_bf16 v[4:7], a[0:3], a[68:71], v[4:7]\n\t"
      "ds_read_b128 a[80:83], v229 offset:4096\n\t"
      "v_mfma_f32_16x16x32_bf16 v[8:11], a[0:3], a[72:75], v[8:11]\n\t"
      "ds_read_b128 a[84:87], v229 offset:8192\n\t"
      "v_mfma_f32_16x16x32_bf16 v[12:15], a[4:7], a[64:67], v[12:15]\n\t"
      "ds_read_b64_tr_b16 a[32:33], v220 offset:8192\n\t"
      "v_mfma_f32_16x16x32_bf16 v[16:19], a[4:7], a[68:71], v[16:19]\n\t"
      "ds_read_b64_tr_b16 a[34:35], v224 offset:9216\n\t"
      "v_mfma_f32_16x16x32_bf16 v[20:23], a[4:7], a[72:75], v[20:23]\n\t"
      "ds_read_b64_tr_b16 a[36:37], v221 offset:8192\n\t"
      "v_mfma_f32_16x16x32_bf16 v[24:27], a[8:11], a[64:67], v[24:27]\n\t"
      "ds_read_b64_tr_b16 a[38:39], v225 offset:9216\n\t"
      "v_mfma_f32_16x16x32_bf16 v[28:31], a[8:11], a[68:71], v[28:31]\n\t"
      "ds_read_b64_tr_b16 a[40:41], v222 offset:8192\n\t"
      "v_mfma_f32_16x16x32_bf16 v[32:35], a[8:11], a[72:75], v[32:35]\n\t"
      "ds_read_b64_tr_b16 a[42:43], v226 offset:9216\n\t"
      "buffer_load_dwordx4 v213, s[44:47], s17 offen offset:1024 lds\n\t"
      "v_mfma_f32_16x16x32_bf16 v[36:39], a[12:15], a[64:67], v[36:39]\n\t"
      "ds_read_b64_tr_b16 a[44:45], v223 offset:8192\n\t"
      "v_mfma_f32_16x16x32_bf16 v[40:43], a[12:15], a[68:71], v[40:43]\n\t"
      "buffer_load_dwordx4 v214, s[44:47], s17 offen offset:2048 lds\n\t"
      "v_mfma_f32_16x16x32_bf16 v[44:47], a[12:15], a[72:75], v[44:47]\n\t"
      "ds_read_b64_tr_b16 a[46:47], v227 offset:9216\n\t"
      "v_mfma_f32_16x16x32_bf16 v[48:51], a[16:19], a[64:67], v[48:51]\n\t"
      "ds_read_b64_tr_b16 a[48:49], v224 offset:8192\n\t"
      "v_mfma_f32_16x16x32_bf16 v[52:55], a[16:19], a[68:71], v[52:55]\n\t"
      "ds_read_b64_tr_b16 a[50:51], v220 offset:9216\n\t"
      "v_mfma_f32_16x16x32_bf16 v[56:59], a[16:19], a[72:75], v[56:59]\n\t"
      "ds_read_b64_tr_b16 a[52:53], v225 offset:8192\n\t"
      "v_mfma_f32_16x16x32_bf16 v[60:63], a[20:23], a[64:67], v[60:63]\n\t"
      "ds_read_b64_tr_b16 a[54:55], v221 offset:9216\n\t"
      "v_mfma_f32_16x16x32_bf16 v[64:67], a[20:23], a[68:71], v[64:67]\n\t"
      "ds_read_b64_tr_b16 a[56:57], v226 offset:8192\n\t"
      "buffer_load_dwordx4 v215, s[44:47], s17 offen offset:3072 lds\n\t"
      "v_mfma_f32_16x16x32_bf16 v[68:71], a[20:23], a[72:75], v[68:71]\n\t"
      "ds_read_b64_tr_b16 a[58:59], v222 offset:9216\n\t"
      "v_mfma_f32_16x16x32_bf16 v[72:75], a[24:27], a[64:67], v[72:75]\n\t"
      "ds_read_b64_tr_b16 a[60:61], v227 offset:8192\n\t"
      "v_mfma_f32_16x16x32_bf16 v[76:79], a[24:27], a[68:71], v[76:79]\n\t"
      "ds_read_b64_tr_b16 a[62:63], v223 offset:9216\n\t"
      "s_add_u32 s17, s17, 0x20000\n\t"
      "s_and_b32 s17, s17, 0x3fffff\n\t"
      "s_mov_b32 m0, s91\n\t"
      "v_mfma_f32_16x16x32_bf16 v[80:83], a[24:27], a[72:75], v[80:83]\n\t"
      "buffer_load_dwordx4 v212, s[44:47], s17 offen lds\n\t"
      "v_mfma_f32_16x16x32_bf16 v[84:87], a[28:31], a[64:67], v[84:87]\n\t"
      "buffer_load_dwordx4 v213, s[44:47], s17 offen offset:1024 lds\n\t"
      "v_mfma_f32_16x16x32_bf16 v[88:91], a[28:31], a[68:71], v[88:91]\n\t"
      "buffer_load_dwordx4 v214, s[44:47], s17 offen offset:2048 lds\n\t"
      "v_mfma_f32_16x16x32_bf16 v[92:95], a[28:31], a[72:75], v[92:95]\n\t"
      "buffer_load_dwordx4 v215, s[44:47], s17 offen offset:3072 lds\n\t"
      "s_add_u32 s17, s17, 0x20000\n\t"
      "s_and_b32 s17, s17, 0x3fffff\n\t"
      "s_waitcnt lgkmcnt(0)\n\t"
      "s_waitcnt vmcnt(19)\n\t"
      "s_mov_b32 m0, s92\n\t"
      "v_mfma_f32_16x16x32_bf16 v[0:3], a[32:35], a[76:79], v[0:3]\n\t"
      "ds_read_b128 a[64:67], v230\n\t"
      "buffer_load_dwordx4 v212, s[44:47], s17 offen lds\n\t"
      "v_mfma_f32_16x16x32_bf16 v[4:7], a[32:35], a[80:83], v[4:7]\n\t"
      "ds_read_b128 a[68:71], v230 offset:4096\n\t"
      "v_mfma_f32_16x16x32_bf16 v[8:11], a[32:35], a[84:87], v[8:11]\n\t"
      "ds_read_b128 a[72:75], v230 offset:8192\n\t"
      "v_mfma_f32_16x16x32_bf16 v[12:15], a[36:39], a[76:79], v[12:15]\n\t"
      "ds_read_b64_tr_b16 a[0:1], v220 offset:16384\n\t"
      "v_mfma_f32_16x16x32_bf16 v[16:19], a[36:39], a[80:83], v[16:19]\n\t"
      "ds_read_b64_tr_b16 a[2:3], v224 offset:17408\n\t"
      "v_mfma_f32_16x16x32_bf16 v[20:23], a[36:39], a[84:87], v[20:23]\n\t"
      "ds_read_b64_tr_b16 a[4:5], v221 offset:16384\n\t"
      "v_mfma_f32_16x16x32_bf16 v[24:27], a[40:43], a[76:79], v[24:27]\n\t"
      "ds_read_b64_tr_b16 a[6:7], v225 offset:17408\n\t"
      "v_mfma_f32_16x16x32_bf16 v[28:31], a[40:43], a[80:83], v[28:31]\n\t"
      "ds_read_b64_tr_b16 a[8:9], v222 offset:16384\n\t"
      "v_mfma_f32_16x16x32_bf16 v[32:35], a[40:43], a[84:87], v[32:35]\n\t"
      "ds_read_b64_tr_b16 a[10:11], v226 offset:17408\n\t"
      "buffer_load_dwordx4 v213, s[44:47], s17 offen offset:1024 lds\n\t"
      "v_mfma_f32_16x16x32_bf16 v[36:39], a[44:47], a[76:79], v[36:39]\n\t"
      "ds_read_b64_tr_b16 a[12:13], v223 offset:16384\n\t"
      "v_mfma_f32_16x16x32_bf16 v[40:43], a[44:47], a[80:83], v[40:43]\n\t"
      "buffer_load_dwordx4 v214, s[44:47], s17 offen offset:2048 lds\n\t"
      "v_mfma_f32_16x16x32_bf16 v[44:47], a[44:47], a[84:87], v[44:47]\n\t"
      "ds_read_b64_tr_b16 a[14:15], v227 offset:17408\n\t"
      "v_mfma_f32_16x16x32_bf16 v[48:51], a[48:51], a[76:79], v[48:51]\n\t"
      "ds_read_b64_tr_b16 a[16:17], v224 offset:16384\n\t"
      "v_mfma_f32_16x16x32_bf16 v[52:55], a[48:51], a[80:83], v[52:55]\n\t"
      "ds_read_b64_tr_b16 a[18:19], v220 offset:17408\n\t"
      "v_mfma_f32_16x16x32_bf16 v[56:59], a[48:51], a[84:87], v[56:59]\n\t"
      "ds_read_b64_tr_b16 a[20:21], v225 offset:16384\n\t"
      "v_mfma_f32_16x16x32_bf16 v[60:63], a[52:55], a[76:79], v[60:63]\n\t"
      "ds_read_b64_tr_b16 a[22:23], v221 offset:17408\n\t"
      "v_mfma_f32_16x16x32_bf16 v[64:67], a[52:55], a[80:83], v[64:67]\n\t"
      "ds_read_b64_tr_b16 a[24:25], v226 offset:16384\n\t"
      "buffer_load_dwordx4 v215, s[44:47], s17 offen offset:3072 lds\n\t"
      "v_mfma_f32_16x16x32_bf16 v[68:71], a[52:55], a[84:87], v[68:71]\n\t"
      "ds_read_b64_tr_b16 a[26:27], v222 offset:17408\n\t"
      "v_mfma_f32_16x16x32_bf16 v[72:75], a[56:59], a[76:79], v[72:75]\n\t"
      "ds_read_b64_tr_b16 a[28:29], v227 offset:16384\n\t"
      "v_mfma_f32_16x16x32_bf16 v[76:79], a[56:59], a[80:83], v[76:79]\n\t"
      "ds_read_b64_tr_b16 a[30:31], v223 offset:17408\n\t"
      "s_add_u32 s17, s17, 0x20000\n\t"
      "s_and_b32 s17, s17, 0x3fffff\n\t"
      "s_mov_b32 m0, s93\n\t"
      "v_mfma_f32_16x16x32_bf16 v[80:83], a[56:59], a[84:87], v[80:83]\n\t"
      "buffer_load_dwordx4 v212, s[44:47], s17 offen lds\n\t"
      "v_mfma_f32_16x16x32_bf16 v[84:87], a[60:63], a[76:79], v[84:87]\n\t"
      "buffer_load_dwordx4 v213, s[44:47], s17 offen offset:1024 lds\n\t"
      "v_mfma_f32_16x16x32_bf16 v[88:91], a[60:63], a[80:83], v[88:91]\n\t"
      "buffer_load_dwordx4 v214, s[44:47], s17 offen offset:2048 lds\n\t"
      "v_mfma_f32_16x16x32_bf16 v[92:95], a[60:63], a[84:87], v[92:95]\n\t"
      "buffer_load_dwordx4 v215, s[44:47], s17 offen offset:3072 lds\n\t"
      "s_add_u32 s17, s17, 0x20000\n\t"
      "s_and_b32 s17, s17, 0x3fffff\n\t"
      "s_waitcnt lgkmcnt(0)\n\t"
      "s_waitcnt vmcnt(16)\n\t"
      "s_mov_b32 m0, s94\n\t"
      "v_mfma_f32_16x16x32_bf16 v[0:3], a[0:3], a[64:67], v[0:3]\n\t"
      "ds_read_b128 a[76:79], v231\n\t"
      "buffer_load_dwordx4 v212, s[44:47], s17 offen lds\n\t"
      "v_mfma_f32_16x16x32_bf16 v[4:7], a[0:3], a[68:71], v[4:7]\n\t"
      "ds_read_b128 a[80:83], v231 offset:4096\n\t"
      "v_mfma_f32_16x16x32_bf16 v[8:11], a[0:3], a[72:75], v[8:11]\n\t"
      "ds_read_b128 a[84:87], v231 offset:8192\n\t"
      "v_mfma_f32_16x16x32_bf16 v[12:15], a[4:7], a[64:67], v[12:15]\n\t"
      "ds_read_b64_tr_b16 a[32:33], v220 offset:24576\n\t"
      "v_mfma_f32_16x16x32_bf16 v[16:19], a[4:7], a[68:71], v[16:19]\n\t"
      "ds_read_b64_tr_b16 a[34:35], v224 offset:25600\n\t"
      "v_mfma_f32_16x16x32_bf16 v[20:23], a[4:7], a[72:75], v[20:23]\n\t"
      "ds_read_b64_tr_b16 a[36:37], v221 offset:24576\n\t"
      "v_mfma_f32_16x16x32_bf16 v[24:27], a[8:11], a[64:67], v[24:27]\n\t"
      "ds_read_b64_tr_b16 a[38:39], v225 offset:25600\n\t"
      "v_mfma_f32_16x16x32_bf16 v[28:31], a[8:11], a[68:71], v[28:31]\n\t"
      "ds_read_b64_tr_b16 a[40:41], v222 offset:24576\n\t"
      "v_mfma_f32_16x16x32_bf16 v[32:35], a[8:11], a[72:75], v[32:35]\n\t"
      "ds_read_b64_tr_b16 a[42:43], v226 offset:25600\n\t"
      "buffer_load_dwordx4 v213, s[44:47], s17 offen offset:1024 lds\n\t"
      "v_mfma_f32_16x16x32_bf16 v[36:39], a[12:15], a[64:67], v[36:39]\n\t"
      "ds_read_b64_tr_b16 a[44:45], v223 offset:24576\n\t"
      "v_mfma_f32_16x16x32_bf16 v[40:43], a[12:15], a[68:71], v[40:43]\n\t"
      "buffer_load_dwordx4 v214, s[44:47], s17 offen offset:2048 lds\n\t"
      "v_mfma_f32_16x16x32_bf16 v[44:47], a[12:15], a[72:75], v[44:47]\n\t"
      "ds_read_b64_tr_b16 a[46:47], v227 offset:25600\n\t"
      "v_mfma_f32_16x16x32_bf16 v[48:51], a[16:19], a[64:67], v[48:51]\n\t"
      "ds_read_b64_tr_b16 a[48:49], v224 offset:24576\n\t"
      "v_mfma_f32_16x16x32_bf16 v[52:55], a[16:19], a[68:71], v[52:55]\n\t"
      "ds_read_b64_tr_b16 a[50:51], v220 offset:25600\n\t"
      "v_mfma_f32_16x16x32_bf16 v[56:59], a[16:19], a[72:75], v[56:59]\n\t"
      "ds_read_b64_tr_b16 a[52:53], v225 offset:24576\n\t"
      "v_mfma_f32_16x16x32_bf16 v[60:63], a[20:23], a[64:67], v[60:63]\n\t"
      "ds_read_b64_tr_b16 a[54:55], v221 offset:25600\n\t"
      "v_mfma_f32_16x16x32_bf16 v[64:67], a[20:23], a[68:71], v[64:67]\n\t"
      "ds_read_b64_tr_b16 a[56:57], v226 offset:24576\n\t"
      "buffer_load_dwordx4 v215, s[44:47], s17 offen offset:3072 lds\n\t"
      "v_mfma_f32_16x16x32_bf16 v[68:71], a[20:23], a[72:75], v[68:71]\n\t"
      "ds_read_b64_tr_b16 a[58:59], v222 offset:25600\n\t"
      "v_mfma_f32_16x16x32_bf16 v[72:75], a[24:27], a[64:67], v[72:75]\n\t"
      "ds_read_b64_tr_b16 a[60:61], v227 offset:24576\n\t"
      "v_mfma_f32_16x16x32_bf16 v[76:79], a[24:27], a[68:71], v[76:79]\n\t"
      "ds_read_b64_tr_b16 a[62:63], v223 offset:25600\n\t"
      "s_add_u32 s17, s17, 0x20000\n\t"
      "s_and_b32 s17, s17, 0x3fffff\n\t"
      "s_mov_b32 m0, s95\n\t"
      "v_mfma_f32_16x16x32_bf16 v[80:83], a[24:27], a[72:75], v[80:83]\n\t"
      "buffer_load_dwordx4 v212, s[44:47], s17 offen lds\n\t"
      "v_mfma_f32_16x16x32_bf16 v[84:87], a[28:31], a[64:67], v[84:87]\n\t"
      "buffer_load_dwordx4 v213, s[44:47], s17 offen offset:1024 lds\n\t"
      "v_mfma_f32_16x16x32_bf16 v[88:91], a[28:31], a[68:71], v[88:91]\n\t"
      "buffer_load_dwordx4 v214, s[44:47], s17 offen offset:2048 lds\n\t"
      "v_mfma_f32_16x16x32_bf16 v[92:95], a[28:31], a[72:75], v[92:95]\n\t"
      "buffer_load_dwordx4 v215, s[44:47], s17 offen offset:3072 lds\n\t"
      "s_add_u32 s17, s17, 0x20000\n\t"
      "s_and_b32 s17, s17, 0x3fffff\n\t"
      "s_waitcnt lgkmcnt(0)\n\t"
      "s_waitcnt vmcnt(16)\n\t"
      "s_barrier\n\t"
      "s_mov_b32 m0, s96\n\t"
      "v_mfma_f32_16x16x32_bf16 v[0:3], a[32:35], a[76:79], v[0:3]\n\t"
      "ds_read_b128 a[64:67], v228 offset:12288\n\t"
      "buffer_load_dwordx4 v212, s[44:47], s17 offen lds\n\t"
      "v_mfma_f32_16x16x32_bf16 v[4:7], a[32:35], a[80:83], v[4:7]\n\t"
      "ds_read_b128 a[68:71], v228 offset:16384\n\t"
      "v_mfma_f32_16x16x32_bf16 v[8:11], a[32:35], a[84:87], v[8:11]\n\t"
      "ds_read_b128 a[72:75], v228 offset:20480\n\t"
      "v_mfma_f32_16x16x32_bf16 v[12:15], a[36:39], a[76:79], v[12:15]\n\t"
      "ds_read_b64_tr_b16 a[0:1], v220\n\t"
      "v_mfma_f32_16x16x32_bf16 v[16:19], a[36:39], a[80:83], v[16:19]\n\t"
      "ds_read_b64_tr_b16 a[2:3], v224 offset:1024\n\t"
      "v_mfma_f32_16x16x32_bf16 v[20:23], a[36:39], a[84:87], v[20:23]\n\t"
      "ds_read_b64_tr_b16 a[4:5], v221\n\t"
      "v_mfma_f32_16x16x32_bf16 v[24:27], a[40:43], a[76:79], v[24:27]\n\t"
      "ds_read_b64_tr_b16 a[6:7], v225 offset:1024\n\t"
      "v_mfma_f32_16x16x32_bf16 v[28:31], a[40:43], a[80:83], v[28:31]\n\t"
      "ds_read_b64_tr_b16 a[8:9], v222\n\t"
      "v_mfma_f32_16x16x32_bf16 v[32:35], a[40:43], a[84:87], v[32:35]\n\t"
      "ds_read_b64_tr_b16 a[10:11], v226 offset:1024\n\t"
      "buffer_load_dwordx4 v213, s[44:47], s17 offen offset:1024 lds\n\t"
      "v_mfma_f32_16x16x32_bf16 v[36:39], a[44:47], a[76:79], v[36:39]\n\t"
      "ds_read_b64_tr_b16 a[12:13], v223\n\t"
      "v_mfma_f32_16x16x32_bf16 v[40:43], a[44:47], a[80:83], v[40:43]\n\t"
      "buffer_load_dwordx4 v214, s[44:47], s17 offen offset:2048 lds\n\t"
      "v_mfma_f32_16x16x32_bf16 v[44:47], a[44:47], a[84:87], v[44:47]\n\t"
      "ds_read_b64_tr_b16 a[14:15], v227 offset:1024\n\t"
      "v_mfma_f32_16x16x32_bf16 v[48:51], a[48:51], a[76:79], v[48:51]\n\t"
      "ds_read_b64_tr_b16 a[16:17], v224\n\t"
      "v_mfma_f32_16x16x32_bf16 v[52:55], a[48:51], a[80:83], v[52:55]\n\t"
      "ds_read_b64_tr_b16 a[18:19], v220 offset:1024\n\t"
      "v_mfma_f32_16x16x32_bf16 v[56:59], a[48:51], a[84:87], v[56:59]\n\t"
      "ds_read_b64_tr_b16 a[20:21], v225\n\t"
      "v_mfma_f32_16x16x32_bf16 v[60:63], a[52:55], a[76:79], v[60:63]\n\t"
      "ds_read_b64_tr_b16 a[22:23], v221 offset:1024\n\t"
      "v_mfma_f32_16x16x32_bf16 v[64:67], a[52:55], a[80:83], v[64:67]\n\t"
      "ds_read_b64_tr_b16 a[24:25], v226\n\t"
      "buffer_load_dwordx4 v215, s[44:47], s17 offen offset:3072 lds\n\t"
      "v_mfma_f32_16x16x32_bf16 v[68:71], a[52:55], a[84:87], v[68:71]\n\t"
      "ds_read_b64_tr_b16 a[26:27], v222 offset:1024\n\t"
      "v_mfma_f32_16x16x32_bf16 v[72:75], a[56:59], a[76:79], v[72:75]\n\t"
      "ds_read_b64_tr_b16 a[28:29], v227\n\t"
      "v_mfma_f32_16x16x32_bf16 v[76:79], a[56:59], a[80:83], v[76:79]\n\t"
      "ds_read_b64_tr_b16 a[30:31], v223 offset:1024\n\t"
      "s_add_u32 s17, s17, 0x20000\n\t"
      "s_and_b32 s17, s17, 0x3fffff\n\t"
      "s_mov_b32 m0, s97\n\t"
      "v_mfma_f32_16x16x32_bf16 v[80:83], a[56:59], a[84:87], v[80:83]\n\t"
      "buffer_load_dwordx4 v212, s[44:47], s17 offen lds\n\t"
      "v_mfma_f32_16x16x32_bf16 v[84:87], a[60:63], a[76:79], v[84:87]\n\t"
      "buffer_load_dwordx4 v213, s[44:47], s17 offen offset:1024 lds\n\t"
      "v_mfma_f32_16x16x32_bf16 v[88:91], a[60:63], a[80:83], v[88:91]\n\t"
      "buffer_load_dwordx4 v214, s[44:47], s17 offen offset:2048 lds\n\t"
      "v_add_u32 v232, s14, v216\n\t"
      "v_mfma_f32_16x16x32_bf16 v[92:95], a[60:63], a[84:87], v[92:95]\n\t"
      "buffer_load_dwordx4 v215, s[44:47], s17 offen offset:3072 lds\n\t"
      "s_add_u32 s17, s17, 0x20000\n\t"
      "s_and_b32 s17, s17, 0x3fffff\n\t"
      "s_cmp_lg_u32 s12, 0\n\t"
      "s_cbranch_scc1 L_qB_%=\n\t"
      "s_waitcnt lgkmcnt(0)\n\t"
      "s_waitcnt vmcnt(16)\n\t"
      "v_mfma_f32_16x16x32_bf16 v[0:3], a[0:3], a[64:67], v[0:3]\n\t"
      "ds_read_b128 a[76:79], v229 offset:12288\n\t"
      "ds_write_b128 v232, v[96:99]\n\t"
      "v_mfma_f32_16x16x32_bf16 v[4:7], a[0:3], a[68:71], v[4:7]\n\t"
      "ds_read_b128 a[80:83], v229 offset:16384\n\t"
      "v_mfma_f32_16x16x32_bf16 v[8:11], a[0:3], a[72:75], v[8:11]\n\t"
      "ds_read_b128 a[84:87], v229 offset:20480\n\t"
      "v_mfma_f32_16x16x32_bf16 v[12:15], a[4:7], a[64:67], v[12:15]\n\t"
      "ds_read_b64_tr_b16 a[32:33], v220 offset:8192\n\t"
      "v_mfma_f32_16x16x32_bf16 v[16:19], a[4:7], a[68:71], v[16:19]\n\t"
      "ds_read_b64_tr_b16 a[34:35], v224 offset:9216\n\t"
      "v_mfma_f32_16x16x32_bf16 v[20:23], a[4:7], a[72:75], v[20:23]\n\t"
      "ds_read_b64_tr_b16 a[36:37], v221 offset:8192\n\t"
      "v_mfma_f32_16x16x32_bf16 v[24:27], a[8:11], a[64:67], v[24:27]\n\t"
      "ds_read_b64_tr_b16 a[38:39], v225 offset:9216\n\t"
      "v_mfma_f32_16x16x32_bf16 v[28:31], a[8:11], a[68:71], v[28:31]\n\t"
      "ds_read_b64_tr_b16 a[40:41], v222 offset:8192\n\t"
      "v_mfma_f32_16x16x32_bf16 v[32:35], a[8:11], a[72:75], v[32:35]\n\t"
      "ds_read_b64_tr_b16 a[42:43], v226 offset:9216\n\t"
      "ds_write_b128 v232, v[100:103] offset:1024\n\t"
      "v_mfma_f32_16x16x32_bf16 v[36:39], a[12:15], a[64:67], v[36:39]\n\t"
      "ds_read_b64_tr_b16 a[44:45], v223 offset:8192\n\t"
      "v_mfma_f32_16x16x32_bf16 v[40:43], a[12:15], a[68:71], v[40:43]\n\t"
      "ds_write_b128 v232, v[104:107] offset:2048\n\t"
      "v_mfma_f32_16x16x32_bf16 v[44:47], a[12:15], a[72:75], v[44:47]\n\t"
      "ds_read_b64_tr_b16 a[46:47], v227 offset:9216\n\t"
      "v_mfma_f32_16x16x32_bf16 v[48:51], a[16:19], a[64:67], v[48:51]\n\t"
      "ds_read_b64_tr_b16 a[48:49], v224 offset:8192\n\t"
      "v_mfma_f32_16x16x32_bf16 v[52:55], a[16:19], a[68:71], v[52:55]\n\t"
      "ds_read_b64_tr_b16 a[50:51], v220 offset:9216\n\t"
      "v_mfma_f32_16x16x32_bf16 v[56:59], a[16:19], a[72:75], v[56:59]\n\t"
      "ds_read_b64_tr_b16 a[52:53], v225 offset:8192\n\t"
      "v_mfma_f32_16x16x32_bf16 v[60:63], a[20:23], a[64:67], v[60:63]\n\t"
      "ds_read_b64_tr_b16 a[54:55], v221 offset:9216\n\t"
      "v_mfma_f32_16x16x32_bf16 v[64:67], a[20:23], a[68:71], v[64:67]\n\t"
      "ds_read_b64_tr_b16 a[56:57], v226 offset:8192\n\t"
      "ds_write_b128 v232, v[108:111] offset:3072\n\t"
      "v_mfma_f32_16x16x32_bf16 v[68:71], a[20:23], a[72:75], v[68:71]\n\t"
      "ds_read_b64_tr_b16 a[58:59], v222 offset:9216\n\t"
      "v_mfma_f32_16x16x32_bf16 v[72:75], a[24:27], a[64:67], v[72:75]\n\t"
      "ds_read_b64_tr_b16 a[60:61], v227 offset:8192\n\t"
      "v_mfma_f32_16x16x32_bf16 v[76:79], a[24:27], a[68:71], v[76:79]\n\t"
      "ds_read_b64_tr_b16 a[62:63], v223 offset:9216\n\t"
      "v_mfma_f32_16x16x32_bf16 v[80:83], a[24:27], a[72:75], v[80:83]\n\t"
      "ds_write_b128 v232, v[112:115] offset:4096\n\t"
      "v_mfma_f32_16x16x32_bf16 v[84:87], a[28:31], a[64:67], v[84:87]\n\t"
      "ds_write_b128 v232, v[116:119] offset:5120\n\t"
      "v_mfma_f32_16x16x32_bf16 v[88:91], a[28:31], a[68:71], v[88:91]\n\t"
      "ds_write_b128 v232, v[120:123] offset:6144\n\t"
      "v_mfma_f32_16x16x32_bf16 v[92:95], a[28:31], a[72:75], v[92:95]\n\t"
      "ds_write_b128 v232, v[124:127] offset:7168\n\t"
      "s_waitcnt lgkmcnt(4)\n\t"
      "s_waitcnt vmcnt(8)\n\t"
      "v_mfma_f32_16x16x32_bf16 v[0:3], a[32:35], a[76:79], v[0:3]\n\t"
      "ds_read_b128 a[64:67], v230 offset:12288\n\t"
      "ds_write_b128 v232, v[128:131] offset:8192\n\t"
      "v_mfma_f32_16x16x32_bf16 v[4:7], a[32:35], a[80:83], v[4:7]\n\t"
      "ds_read_b128 a[68:71], v230 offset:16384\n\t"
      "v_mfma_f32_16x16x32_bf16 v[8:11], a[32:35], a[84:87], v[8:11]\n\t"
      "ds_read_b128 a[72:75], v230 offset:20480\n\t"
      "v_mfma_f32_16x16x32_bf16 v[12:15], a[36:39], a[76:79], v[12:15]\n\t"
      "ds_read_b64_tr_b16 a[0:1], v220 offset:16384\n\t"
      "v_mfma_f32_16x16x32_bf16 v[16:19], a[36:39], a[80:83], v[16:19]\n\t"
      "ds_read_b64_tr_b16 a[2:3], v224 offset:17408\n\t"
      "v_mfma_f32_16x16x32_bf16 v[20:23], a[36:39], a[84:87], v[20:23]\n\t"
      "ds_read_b64_tr_b16 a[4:5], v221 offset:16384\n\t"
      "v_mfma_f32_16x16x32_bf16 v[24:27], a[40:43], a[76:79], v[24:27]\n\t"
      "ds_read_b64_tr_b16 a[6:7], v225 offset:17408\n\t"
      "v_mfma_f32_16x16x32_bf16 v[28:31], a[40:43], a[80:83], v[28:31]\n\t"
      "ds_read_b64_tr_b16 a[8:9], v222 offset:16384\n\t"
      "v_mfma_f32_16x16x32_bf16 v[32:35], a[40:43], a[84:87], v[32:35]\n\t"
      "ds_read_b64_tr_b16 a[10:11], v226 offset:17408\n\t"
      "ds_write_b128 v232, v[132:135] offset:9216\n\t"
      "v_mfma_f32_16x16x32_bf16 v[36:39], a[44:47], a[76:79], v[36:39]\n\t"
      "ds_read_b64_tr_b16 a[12:13], v223 offset:16384\n\t"
      "v_mfma_f32_16x16x32_bf16 v[40:43], a[44:47], a[80:83], v[40:43]\n\t"
      "ds_write_b128 v232, v[136:139] offset:10240\n\t"
      "v_mfma_f32_16x16x32_bf16 v[44:47], a[44:47], a[84:87], v[44:47]\n\t"
      "ds_read_b64_tr_b16 a[14:15], v227 offset:17408\n\t"
      "v_mfma_f32_16x16x32_bf16 v[48:51], a[48:51], a[76:79], v[48:51]\n\t"
      "ds_read_b64_tr_b16 a[16:17], v224 offset:16384\n\t"
      "v_mfma_f32_16x16x32_bf16 v[52:55], a[48:51], a[80:83], v[52:55]\n\t"
      "ds_read_b64_tr_b16 a[18:19], v220 offset:17408\n\t"
      "v_mfma_f32_16x16x32_bf16 v[56:59], a[48:51], a[84:87], v[56:59]\n\t"
      "ds_read_b64_tr_b16 a[20:21], v225 offset:16384\n\t"
      "v_mfma_f32_16x16x32_bf16 v[60:63], a[52:55], a[76:79], v[60:63]\n\t"
      "ds_read_b64_tr_b16 a[22:23], v221 offset:17408\n\t"
      "v_mfma_f32_16x16x32_bf16 v[64:67], a[52:55], a[80:83], v[64:67]\n\t"
      "ds_read_b64_tr_b16 a[24:25], v226 offset:16384\n\t"
      "ds_write_b128 v232, v[140:143] offset:11264\n\t"
      "v_mfma_f32_16x16x32_bf16 v[68:71], a[52:55], a[84:87], v[68:71]\n\t"
      "ds_read_b64_tr_b16 a[26:27], v222 offset:17408\n\t"
      "v_mfma_f32_16x16x32_bf16 v[72:75], a[56:59], a[76:79], v[72:75]\n\t"
      "ds_read_b64_tr_b16 a[28:29], v227 offset:16384\n\t"
      "v_mfma_f32_16x16x32_bf16 v[76:79], a[56:59], a[80:83], v[76:79]\n\t"
      "ds_read_b64_tr_b16 a[30:31], v223 offset:17408\n\t"
      "v_mfma_f32_16x16x32_bf16 v[80:83], a[56:59], a[84:87], v[80:83]\n\t"
      "ds_write_b128 v232, v[144:147] offset:12288\n\t"
      "v_mfma_f32_16x16x32_bf16 v[84:87], a[60:63], a[76:79], v[84:87]\n\t"
      "ds_write_b128 v232, v[148:151] offset:13312\n\t"
      "v_mfma_f32_16x16x32_bf16 v[88:91], a[60:63], a[80:83], v[88:91]\n\t"
      "ds_write_b128 v232, v[152:155] offset:14336\n\t"
      "s_lshl_b32 s19, s10, 19\n\t"
      "s_lshl_b32 s22, s6, 8\n\t"
      "s_add_u32 s19, s19, s22\n\t"
      "s_add_u32 s44, s80, s19\n\t"
      "s_addc_u32 s45, s81, 0\n\t"
      "s_sub_u32 s46, 0x400000, s19\n\t"
      "s_mov_b32 s47, 0x20000\n\t"
      "v_mfma_f32_16x16x32_bf16 v[92:95], a[60:63], a[84:87], v[92:95]\n\t"
      "ds_write_b128 v232, v[156:159] offset:15360\n\t"
      "s_waitcnt lgkmcnt(4)\n\t"
      "s_waitcnt vmcnt(0)\n\t"
      "v_mfma_f32_16x16x32_bf16 v[0:3], a[0:3], a[64:67], v[0:3]\n\t"
      "ds_read_b128 a[76:79], v231 offset:12288\n\t"
      "ds_write_b128 v232, v[160:163] offset:16384\n\t"
      "v_mfma_f32_16x16x32_bf16 v[4:7], a[0:3], a[68:71], v[4:7]\n\t"
      "ds_read_b128 a[80:83], v231 offset:16384\n\t"
      "v_mfma_f32_16x16x32_bf16 v[8:11], a[0:3], a[72:75], v[8:11]\n\t"
      "ds_read_b128 a[84:87], v231 offset:20480\n\t"
      "v_mfma_f32_16x16x32_bf16 v[12:15], a[4:7], a[64:67], v[12:15]\n\t"
      "ds_read_b64_tr_b16 a[32:33], v220 offset:24576\n\t"
      "v_mfma_f32_16x16x32_bf16 v[16:19], a[4:7], a[68:71], v[16:19]\n\t"
      "ds_read_b64_tr_b16 a[34:35], v224 offset:25600\n\t"
      "v_mfma_f32_16x16x32_bf16 v[20:23], a[4:7], a[72:75], v[20:23]\n\t"
      "ds_read_b64_tr_b16 a[36:37], v221 offset:24576\n\t"
      "v_mfma_f32_16x16x32_bf16 v[24:27], a[8:11], a[64:67], v[24:27]\n\t"
      "ds_read_b64_tr_b16 a[38:39], v225 offset:25600\n\t"
      "v_mfma_f32_16x16x32_bf16 v[28:31], a[8:11], a[68:71], v[28:31]\n\t"
      "ds_read_b64_tr_b16 a[40:41], v222 offset:24576\n\t"
      "v_mfma_f32_16x16x32_bf16 v[32:35], a[8:11], a[72:75], v[32:35]\n\t"
      "ds_read_b64_tr_b16 a[42:43], v226 offset:25600\n\t"
      "ds_write_b128 v232, v[164:167] offset:17408\n\t"
      "v_mfma_f32_16x16x32_bf16 v[36:39], a[12:15], a[64:67], v[36:39]\n\t"
      "ds_read_b64_tr_b16 a[44:45], v223 offset:24576\n\t"
      "v_mfma_f32_16x16x32_bf16 v[40:43], a[12:15], a[68:71], v[40:43]\n\t"
      "ds_write_b128 v232, v[168:171] offset:18432\n\t"
      "v_mfma_f32_16x16x32_bf16 v[44:47], a[12:15], a[72:75], v[44:47]\n\t"
      "ds_read_b64_tr_b16 a[46:47], v227 offset:25600\n\t"
      "v_mfma_f32_16x16x32_bf16 v[48:51], a[16:19], a[64:67], v[48:51]\n\t"
      "ds_read_b64_tr_b16 a[48:49], v224 offset:24576\n\t"
      "v_mfma_f32_16x16x32_bf16 v[52:55], a[16:19], a[68:71], v[52:55]\n\t"
      "ds_read_b64_tr_b16 a[50:51], v220 offset:25600\n\t"
      "v_mfma_f32_16x16x32_bf16 v[56:59], a[16:19], a[72:75], v[56:59]\n\t"
      "ds_read_b64_tr_b16 a[52:53], v225 offset:24576\n\t"
      "v_mfma_f32_16x16x32_bf16 v[60:63], a[20:23], a[64:67], v[60:63]\n\t"
      "ds_read_b64_tr_b16 a[54:55], v221 offset:25600\n\t"
      "v_mfma_f32_16x16x32_bf16 v[64:67], a[20:23], a[68:71], v[64:67]\n\t"
      "ds_read_b64_tr_b16 a[56:57], v226 offset:24576\n\t"
      "ds_write_b128 v232, v[172:175] offset:19456\n\t"
      "v_mfma_f32_16x16x32_bf16 v[68:71], a[20:23], a[72:75], v[68:71]\n\t"
      "ds_read_b64_tr_b16 a[58:59], v222 offset:25600\n\t"
      "v_mfma_f32_16x16x32_bf16 v[72:75], a[24:27], a[64:67], v[72:75]\n\t"
      "ds_read_b64_tr_b16 a[60:61], v227 offset:24576\n\t"
      "v_mfma_f32_16x16x32_bf16 v[76:79], a[24:27], a[68:71], v[76:79]\n\t"
      "ds_read_b64_tr_b16 a[62:63], v223 offset:25600\n\t"
      "v_mfma_f32_16x16x32_bf16 v[80:83], a[24:27], a[72:75], v[80:83]\n\t"
      "ds_write_b128 v232, v[176:179] offset:20480\n\t"
      "v_mfma_f32_16x16x32_bf16 v[84:87], a[28:31], a[64:67], v[84:87]\n\t"
      "ds_write_b128 v232, v[180:183] offset:21504\n\t"
      "v_mfma_f32_16x16x32_bf16 v[88:91], a[28:31], a[68:71], v[88:91]\n\t"
      "ds_write_b128 v232, v[184:187] offset:22528\n\t"
      "v_mfma_f32_16x16x32_bf16 v[92:95], a[28:31], a[72:75], v[92:95]\n\t"
      "ds_write_b128 v232, v[188:191] offset:23552\n\t"
      "s_waitcnt lgkmcnt(4)\n\t"
      "v_mfma_f32_16x16x32_bf16 v[0:3], a[32:35], a[76:79], v[0:3]\n\t"
      "ds_write_b128 v232, a[112:115] offset:24576\n\t"
      "v_mfma_f32_16x16x32_bf16 v[4:7], a[32:35], a[80:83], v[4:7]\n\t"
      "v_mfma_f32_16x16x32_bf16 v[8:11], a[32:35], a[84:87], v[8:11]\n\t"
      "v_mfma_f32_16x16x32_bf16 v[12:15], a[36:39], a[76:79], v[12:15]\n\t"
      "ds_write_b128 v232, a[116:119] offset:25600\n\t"
      "v_mfma_f32_16x16x32_bf16 v[16:19], a[36:39], a[80:83], v[16:19]\n\t"
      "v_mfma_f32_16x16x32_bf16 v[20:23], a[36:39], a[84:87], v[20:23]\n\t"
      "v_mfma_f32_16x16x32_bf16 v[24:27], a[40:43], a[76:79], v[24:27]\n\t"
      "ds_write_b128 v232, a[120:123] offset:26624\n\t"
      "v_mfma_f32_16x16x32_bf16 v[28:31], a[40:43], a[80:83], v[28:31]\n\t"
      "v_mfma_f32_16x16x32_bf16 v[32:35], a[40:43], a[84:87], v[32:35]\n\t"
      "v_mfma_f32_16x16x32_bf16 v[36:39], a[44:47], a[76:79], v[36:39]\n\t"
      "ds_write_b128 v232, a[124:127] offset:27648\n\t"
      "v_mfma_f32_16x16x32_bf16 v[40:43], a[44:47], a[80:83], v[40:43]\n\t"
      "v_mfma_f32_16x16x32_bf16 v[44:47], a[44:47], a[84:87], v[44:47]\n\t"
      "v_mfma_f32_16x16x32_bf16 v[48:51], a[48:51], a[76:79], v[48:51]\n\t"
      "ds_write_b128 v232, a[128:131] offset:28672\n\t"
      "v_mfma_f32_16x16x32_bf16 v[52:55], a[48:51], a[80:83], v[52:55]\n\t"
      "v_mfma_f32_16x16x32_bf16 v[56:59], a[48:51], a[84:87], v[56:59]\n\t"
      "v_mfma_f32_16x16x32_bf16 v[60:63], a[52:55], a[76:79], v[60:63]\n\t"
      "ds_write_b128 v232, a[132:135] offset:29696\n\t"
      "v_mfma_f32_16x16x32_bf16 v[64:67], a[52:55], a[80:83], v[64:67]\n\t"
      "v_mfma_f32_16x16x32_bf16 v[68:71], a[52:55], a[84:87], v[68:71]\n\t"
      "v_mfma_f32_16x16x32_bf16 v[72:75], a[56:59], a[76:79], v[72:75]\n\t"
      "ds_write_b128 v232, a[136:139] offset:30720\n\t"
      "v_mfma_f32_16x16x32_bf16 v[76:79], a[56:59], a[80:83], v[76:79]\n\t"
      "v_mfma_f32_16x16x32_bf16 v[80:83], a[56:59], a[84:87], v[80:83]\n\t"
      "v_mfma_f32_16x16x32_bf16 v[84:87], a[60:63], a[76:79], v[84:87]\n\t"
      "ds_write_b128 v232, a[140:143] offset:31744\n\t"
      "v_mfma_f32_16x16x32_bf16 v[88:91], a[60:63], a[80:83], v[88:91]\n\t"
      "v_mfma_f32_16x16x32_bf16 v[92:95], a[60:63], a[84:87], v[92:95]\n\t"
      "s_lshl_b32 s19, s6, 12\n\t"
      "s_add_u32 s22, s19, 0x20000\n\t"
      "v_xad_u32 v220, v239, 0, s22\n\t"
      "v_xad_u32 v221, v239, 32, s22\n\t"
      "v_xad_u32 v222, v239, 64, s22\n\t"
      "v_xor_b32 v223, 96, v239\n\t"
      "v_add_u32 v223, s22, v223\n\t"
      "v_xor_b32 v224, 128, v239\n\t"
      "v_add_u32 v224, s22, v224\n\t"
      "v_xor_b32 v225, 160, v239\n\t"
      "v_add_u32 v225, s22, v225\n\t"
      "v_xor_b32 v226, 192, v239\n\t"
      "v_add_u32 v226, s22, v226\n\t"
      "v_xor_b32 v227, 224, v239\n\t"
      "v_add_u32 v227, s22, v227\n\t"
      "v_add_u32 v228, s19, v228\n\t"
      "v_add_u32 v229, s19, v229\n\t"
      "v_add_u32 v230, s19, v230\n\t"
      "v_add_u32 v231, s19, v231\n\t"
      "v_cvt_pk_bf16_f32 v0, v0, v1\n\t"
      "v_cvt_pk_bf16_f32 v1, v2, v3\n\t"
      "v_cvt_pk_bf16_f32 v4, v4, v5\n\t"
      "v_cvt_pk_bf16_f32 v5, v6, v7\n\t"
      "v_cvt_pk_bf16_f32 v8, v8, v9\n\t"
      "v_cvt_pk_bf16_f32 v9, v10, v11\n\t"
      "v_cvt_pk_bf16_f32 v12, v12, v13\n\t"
      "v_cvt_pk_bf16_f32 v13, v14, v15\n\t"
      "v_cvt_pk_bf16_f32 v16, v16, v17\n\t"
      "v_cvt_pk_bf16_f32 v17, v18, v19\n\t"
      "v_cvt_pk_bf16_f32 v20, v20, v21\n\t"
      "v_cvt_pk_bf16_f32 v21, v22, v23\n\t"
      "v_cvt_pk_bf16_f32 v24, v24, v25\n\t"
      "v_cvt_pk_bf16_f32 v25, v26, v27\n\t"
      "v_cvt_pk_bf16_f32 v28, v28, v29\n\t"
      "v_cvt_pk_bf16_f32 v29, v30, v31\n\t"
      "v_cvt_pk_bf16_f32 v32, v32, v33\n\t"
      "v_cvt_pk_bf16_f32 v33, v34, v35\n\t"
      "v_cvt_pk_bf16_f32 v36, v36, v37\n\t"
      "v_cvt_pk_bf16_f32 v37, v38, v39\n\t"
      "v_cvt_pk_bf16_f32 v40, v40, v41\n\t"
      "v_cvt_pk_bf16_f32 v41, v42, v43\n\t"
      "v_cvt_pk_bf16_f32 v44, v44, v45\n\t"
      "v_cvt_pk_bf16_f32 v45, v46, v47\n\t"
      "v_cvt_pk_bf16_f32 v48, v48, v49\n\t"
      "v_cvt_pk_bf16_f32 v49, v50, v51\n\t"
      "v_cvt_pk_bf16_f32 v52, v52, v53\n\t"
      "v_cvt_pk_bf16_f32 v53, v54, v55\n\t"
      "v_cvt_pk_bf16_f32 v56, v56, v57\n\t"
      "v_cvt_pk_bf16_f32 v57, v58, v59\n\t"
      "v_cvt_pk_bf16_f32 v60, v60, v61\n\t"
      "v_cvt_pk_bf16_f32 v61, v62, v63\n\t"
      "v_cvt_pk_bf16_f32 v64, v64, v65\n\t"
      "v_cvt_pk_bf16_f32 v65, v66, v67\n\t"
      "v_cvt_pk_bf16_f32 v68, v68, v69\n\t"
      "v_cvt_pk_bf16_f32 v69, v70, v71\n\t"
      "v_cvt_pk_bf16_f32 v72, v72, v73\n\t"
      "v_cvt_pk_bf16_f32 v73, v74, v75\n\t"
      "v_cvt_pk_bf16_f32 v76, v76, v77\n\t"
      "v_cvt_pk_bf16_f32 v77, v78, v79\n\t"
      "v_cvt_pk_bf16_f32 v80, v80, v81\n\t"
      "v_cvt_pk_bf16_f32 v81, v82, v83\n\t"
      "v_cvt_pk_bf16_f32 v84, v84, v85\n\t"
      "v_cvt_pk_bf16_f32 v85, v86, v87\n\t"
      "v_cvt_pk_bf16_f32 v88, v88, v89\n\t"
      "v_cvt_pk_bf16_f32 v89, v90, v91\n\t"
      "v_cvt_pk_bf16_f32 v92, v92, v93\n\t"
      "v_cvt_pk_bf16_f32 v93, v94, v95\n\t"
      "s_waitcnt lgkmcnt(0)\n\t"
      "s_barrier\n\t"
      "ds_write_b64 v220, v[0:1]\n\t"
      "ds_write_b64 v221, v[12:13]\n\t"
      "ds_write_b64 v222, v[24:25]\n\t"
      "ds_write_b64 v223, v[36:37]\n\t"
      "ds_write_b64 v224, v[48:49]\n\t"
      "ds_write_b64 v225, v[60:61]\n\t"
      "ds_write_b64 v226, v[72:73]\n\t"
      "ds_write_b64 v227, v[84:85]\n\t"
      "ds_read_b128 a[64:67], v228\n\t"
      "ds_read_b128 a[76:79], v229\n\t"
      "ds_read_b128 a[88:91], v230\n\t"
      "ds_read_b128 a[100:103], v231\n\t"
      "ds_write_b64 v220, v[4:5]\n\t"
      "ds_write_b64 v221, v[16:17]\n\t"
      "ds_write_b64 v222, v[28:29]\n\t"
      "ds_write_b64 v223, v[40:41]\n\t"
      "ds_write_b64 v224, v[52:53]\n\t"
      "ds_write_b64 v225, v[64:65]\n\t"
      "ds_write_b64 v226, v[76:77]\n\t"
      "ds_write_b64 v227, v[88:89]\n\t"
      "ds_read_b128 a[68:71], v228\n\t"
      "ds_read_b128 a[80:83], v229\n\t"
      "ds_read_b128 a[92:95], v230\n\t"
      "ds_read_b128 a[104:107], v231\n\t"
      "ds_write_b64 v220, v[8:9]\n\t"
      "ds_write_b64 v221, v[20:21]\n\t"
      "ds_write_b64 v222, v[32:33]\n\t"
      "ds_write_b64 v223, v[44:45]\n\t"
      "ds_write_b64 v224, v[56:57]\n\t"
      "ds_write_b64 v225, v[68:69]\n\t"
      "ds_write_b64 v226, v[80:81]\n\t"
      "ds_write_b64 v227, v[92:93]\n\t"
      "ds_read_b128 a[72:75], v228\n\t"
      "ds_read_b128 a[84:87], v229\n\t"
      "ds_read_b128 a[96:99], v230\n\t"
      "ds_read_b128 a[108:111], v231\n\t"
      "s_branch L_A_%=\n\t"
      "L_qB_%=:\n\t"
      "s_waitcnt lgkmcnt(0)\n\t"
      "s_waitcnt vmcnt(16)\n\t"
      "s_mov_b32 m0, s90\n\t"
      "v_mfma_f32_16x16x32_bf16 v[0:3], a[0:3], a[64:67], v[0:3]\n\t"
      "ds_read_b128 a[76:79], v229 offset:12288\n\t"
      "buffer_load_dwordx4 v204, s[36:39], s82 offen nt lds\n\t"
      "v_mfma_f32_16x16x32_bf16 v[4:7], a[0:3], a[68:71], v[4:7]\n\t"
      "ds_read_b128 a[80:83], v229 offset:16384\n\t"
      "v_mfma_f32_16x16x32_bf16 v[8:11], a[0:3], a[72:75], v[8:11]\n\t"
      "ds_read_b128 a[84:87], v229 offset:20480\n\t"
      "v_mfma_f32_16x16x32_bf16 v[12:15], a[4:7], a[64:67], v[12:15]\n\t"
      "ds_read_b64_tr_b16 a[32:33], v220 offset:8192\n\t"
      "v_mfma_f32_16x16x32_bf16 v[16:19], a[4:7], a[68:71], v[16:19]\n\t"
      "ds_read_b64_tr_b16 a[34:35], v224 offset:9216\n\t"
      "v_mfma_f32_16x16x32_bf16 v[20:23], a[4:7], a[72:75], v[20:23]\n\t"
      "ds_read_b64_tr_b16 a[36:37], v221 offset:8192\n\t"
      "v_mfma_f32_16x16x32_bf16 v[24:27], a[8:11], a[64:67], v[24:27]\n\t"
      "ds_read_b64_tr_b16 a[38:39], v225 offset:9216\n\t"
      "v_mfma_f32_16x16x32_bf16 v[28:31], a[8:11], a[68:71], v[28:31]\n\t"
      "ds_read_b64_tr_b16 a[40:41], v222 offset:8192\n\t"
      "v_mfma_f32_16x16x32_bf16 v[32:35], a[8:11], a[72:75], v[32:35]\n\t"
      "ds_read_b64_tr_b16 a[42:43], v226 offset:9216\n\t"
      "buffer_load_dwordx4 v205, s[36:39], s82 offen offset:1024 nt lds\n\t"
      "v_mfma_f32_16x16x32_bf16 v[36:39], a[12:15], a[64:67], v[36:39]\n\t"
      "ds_read_b64_tr_b16 a[44:45], v223 offset:8192\n\t"
      "v_mfma_f32_16x16x32_bf16 v[40:43], a[12:15], a[68:71], v[40:43]\n\t"
      "buffer_load_dwordx4 v206, s[36:39], s82 offen offset:2048 nt lds\n\t"
      "v_mfma_f32_16x16x32_bf16 v[44:47], a[12:15], a[72:75], v[44:47]\n\t"
      "ds_read_b64_tr_b16 a[46:47], v227 offset:9216\n\t"
      "v_mfma_f32_16x16x32_bf16 v[48:51], a[16:19], a[64:67], v[48:51]\n\t"
      "ds_read_b64_tr_b16 a[48:49], v224 offset:8192\n\t"
      "v_mfma_f32_16x16x32_bf16 v[52:55], a[16:19], a[68:71], v[52:55]\n\t"
      "ds_read_b64_tr_b16 a[50:51], v220 offset:9216\n\t"
      "v_mfma_f32_16x16x32_bf16 v[56:59], a[16:19], a[72:75], v[56:59]\n\t"
      "ds_read_b64_tr_b16 a[52:53], v225 offset:8192\n\t"
      "v_mfma_f32_16x16x32_bf16 v[60:63], a[20:23], a[64:67], v[60:63]\n\t"
      "ds_read_b64_tr_b16 a[54:55], v221 offset:9216\n\t"
      "v_mfma_f32_16x16x32_bf16 v[64:67], a[20:23], a[68:71], v[64:67]\n\t"
      "ds_read_b64_tr_b16 a[56:57], v226 offset:8192\n\t"
      "buffer_load_dwordx4 v207, s[36:39], s82 offen offset:3072 nt lds\n\t"
      "v_mfma_f32_16x16x32_bf16 v[68:71], a[20:23], a[72:75], v[68:71]\n\t"
      "ds_read_b64_tr_b16 a[58:59], v222 offset:9216\n\t"
      "v_mfma_f32_16x16x32_bf16 v[72:75], a[24:27], a[64:67], v[72:75]\n\t"
      "ds_read_b64_tr_b16 a[60:61], v227 offset:8192\n\t"
      "v_mfma_f32_16x16x32_bf16 v[76:79], a[24:27], a[68:71], v[76:79]\n\t"
      "ds_read_b64_tr_b16 a[62:63], v223 offset:9216\n\t"
      "s_mov_b32 m0, s91\n\t"
      "v_mfma_f32_16x16x32_bf16 v[80:83], a[24:27], a[72:75], v[80:83]\n\t"
      "buffer_load_dwordx4 v204, s[36:39], s83 offen nt lds\n\t"
      "v_mfma_f32_16x16x32_bf16 v[84:87], a[28:31], a[64:67], v[84:87]\n\t"
      "buffer_load_dwordx4 v205, s[36:39], s83 offen offset:1024 nt lds\n\t"
      "v_mfma_f32_16x16x32_bf16 v[88:91], a[28:31], a[68:71], v[88:91]\n\t"
      "buffer_load_dwordx4 v206, s[36:39], s83 offen offset:2048 nt lds\n\t"
      "v_mfma_f32_16x16x32_bf16 v[92:95], a[28:31], a[72:75], v[92:95]\n\t"
      "buffer_load_dwordx4 v207, s[36:39], s83 offen offset:3072 nt lds\n\t"
      "s_waitcnt lgkmcnt(0)\n\t"
      "s_waitcnt vmcnt(16)\n\t"
      "s_mov_b32 m0, s92\n\t"
      "v_mfma_f32_16x16x32_bf16 v[0:3], a[32:35], a[76:79], v[0:3]\n\t"
      "ds_read_b128 a[64:67], v230 offset:12288\n\t"
      "buffer_load_dwordx4 v204, s[40:43], s84 offen nt lds\n\t"
      "v_mfma_f32_16x16x32_bf16 v[4:7], a[32:35], a[80:83], v[4:7]\n\t"
      "ds_read_b128 a[68:71], v230 offset:16384\n\t"
      "v_mfma_f32_16x16x32_bf16 v[8:11], a[32:35], a[84:87], v[8:11]\n\t"
      "ds_read_b128 a[72:75], v230 offset:20480\n\t"
      "v_mfma_f32_16x16x32_bf16 v[12:15], a[36:39], a[76:79], v[12:15]\n\t"
      "ds_read_b64_tr_b16 a[0:1], v220 offset:16384\n\t"
      "v_mfma_f32_16x16x32_bf16 v[16:19], a[36:39], a[80:83], v[16:19]\n\t"
      "ds_read_b64_tr_b16 a[2:3], v224 offset:17408\n\t"
      "v_mfma_f32_16x16x32_bf16 v[20:23], a[36:39], a[84:87], v[20:23]\n\t"
      "ds_read_b64_tr_b16 a[4:5], v221 offset:16384\n\t"
      "v_mfma_f32_16x16x32_bf16 v[24:27], a[40:43], a[76:79], v[24:27]\n\t"
      "ds_read_b64_tr_b16 a[6:7], v225 offset:17408\n\t"
      "v_mfma_f32_16x16x32_bf16 v[28:31], a[40:43], a[80:83], v[28:31]\n\t"
      "ds_read_b64_tr_b16 a[8:9], v222 offset:16384\n\t"
      "v_mfma_f32_16x16x32_bf16 v[32:35], a[40:43], a[84:87], v[32:35]\n\t"
      "ds_read_b64_tr_b16 a[10:11], v226 offset:17408\n\t"
      "buffer_load_dwordx4 v205, s[40:43], s84 offen offset:1024 nt lds\n\t"
      "v_mfma_f32_16x16x32_bf16 v[36:39], a[44:47], a[76:79], v[36:39]\n\t"
      "ds_read_b64_tr_b16 a[12:13], v223 offset:16384\n\t"
      "v_mfma_f32_16x16x32_bf16 v[40:43], a[44:47], a[80:83], v[40:43]\n\t"
      "buffer_load_dwordx4 v206, s[40:43], s84 offen offset:2048 nt lds\n\t"
      "v_mfma_f32_16x16x32_bf16 v[44:47], a[44:47], a[84:87], v[44:47]\n\t"
      "ds_read_b64_tr_b16 a[14:15], v227 offset:17408\n\t"
      "v_mfma_f32_16x16x32_bf16 v[48:51], a[48:51], a[76:79], v[48:51]\n\t"
      "ds_read_b64_tr_b16 a[16:17], v224 offset:16384\n\t"
      "v_mfma_f32_16x16x32_bf16 v[52:55], a[48:51], a[80:83], v[52:55]\n\t"
      "ds_read_b64_tr_b16 a[18:19], v220 offset:17408\n\t"
      "v_mfma_f32_16x16x32_bf16 v[56:59], a[48:51], a[84:87], v[56:59]\n\t"
      "ds_read_b64_tr_b16 a[20:21], v225 offset:16384\n\t"
      "v_mfma_f32_16x16x32_bf16 v[60:63], a[52:55], a[76:79], v[60:63]\n\t"
      "ds_read_b64_tr_b16 a[22:23], v221 offset:17408\n\t"
      "v_mfma_f32_16x16x32_bf16 v[64:67], a[52:55], a[80:83], v[64:67]\n\t"
      "ds_read_b64_tr_b16 a[24:25], v226 offset:16384\n\t"
      "buffer_load_dwordx4 v207, s[40:43], s84 offen offset:3072 nt lds\n\t"
      "v_mfma_f32_16x16x32_bf16 v[68:71], a[52:55], a[84:87], v[68:71]\n\t"
      "ds_read_b64_tr_b16 a[26:27], v222 offset:17408\n\t"
      "v_mfma_f32_16x16x32_bf16 v[72:75], a[56:59], a[76:79], v[72:75]\n\t"
      "ds_read_b64_tr_b16 a[28:29], v227 offset:16384\n\t"
      "v_mfma_f32_16x16x32_bf16 v[76:79], a[56:59], a[80:83], v[76:79]\n\t"
      "ds_read_b64_tr_b16 a[30:31], v223 offset:17408\n\t"
      "s_mov_b32 m0, s93\n\t"
      "v_mfma_f32_16x16x32_bf16 v[80:83], a[56:59], a[84:87], v[80:83]\n\t"
      "buffer_load_dwordx4 v204, s[40:43], s85 offen nt lds\n\t"
      "v_mfma_f32_16x16x32_bf16 v[84:87], a[60:63], a[76:79], v[84:87]\n\t"
      "buffer_load_dwordx4 v205, s[40:43], s85 offen offset:1024 nt lds\n\t"
      "v_mfma_f32_16x16x32_bf16 v[88:91], a[60:63], a[80:83], v[88:91]\n\t"
      "buffer_load_dwordx4 v206, s[40:43], s85 offen offset:2048 nt lds\n\t"
      "s_lshl_b32 s19, s10, 19\n\t"
      "s_lshl_b32 s22, s6, 8\n\t"
      "s_add_u32 s19, s19, s22\n\t"
      "s_add_u32 s44, s80, s19\n\t"
      "s_addc_u32 s45, s81, 0\n\t"
      "s_sub_u32 s46, 0x400000, s19\n\t"
      "s_mov_b32 s47, 0x20000\n\t"
      "v_mfma_f32_16x16x32_bf16 v[92:95], a[60:63], a[84:87], v[92:95]\n\t"
      "buffer_load_dwordx4 v207, s[40:43], s85 offen offset:3072 nt lds\n\t"
      "s_waitcnt lgkmcnt(0)\n\t"
      "s_waitcnt vmcnt(16)\n\t"
      "s_mov_b32 m0, s94\n\t"
      "v_mfma_f32_16x16x32_bf16 v[0:3], a[0:3], a[64:67], v[0:3]\n\t"
      "ds_read_b128 a[76:79], v231 offset:12288\n\t"
      "buffer_load_dwordx4 v204, s[36:39], s86 offen nt lds\n\t"
      "v_mfma_f32_16x16x32_bf16 v[4:7], a[0:3], a[68:71], v[4:7]\n\t"
      "ds_read_b128 a[80:83], v231 offset:16384\n\t"
      "v_mfma_f32_16x16x32_bf16 v[8:11], a[0:3], a[72:75], v[8:11]\n\t"
      "ds_read_b128 a[84:87], v231 offset:20480\n\t"
      "v_mfma_f32_16x16x32_bf16 v[12:15], a[4:7], a[64:67], v[12:15]\n\t"
      "ds_read_b64_tr_b16 a[32:33], v220 offset:24576\n\t"
      "v_mfma_f32_16x16x32_bf16 v[16:19], a[4:7], a[68:71], v[16:19]\n\t"
      "ds_read_b64_tr_b16 a[34:35], v224 offset:25600\n\t"
      "v_mfma_f32_16x16x32_bf16 v[20:23], a[4:7], a[72:75], v[20:23]\n\t"
      "ds_read_b64_tr_b16 a[36:37], v221 offset:24576\n\t"
      "v_mfma_f32_16x16x32_bf16 v[24:27], a[8:11], a[64:67], v[24:27]\n\t"
      "ds_read_b64_tr_b16 a[38:39], v225 offset:25600\n\t"
      "v_mfma_f32_16x16x32_bf16 v[28:31], a[8:11], a[68:71], v[28:31]\n\t"
      "ds_read_b64_tr_b16 a[40:41], v222 offset:24576\n\t"
      "v_mfma_f32_16x16x32_bf16 v[32:35], a[8:11], a[72:75], v[32:35]\n\t"
      "ds_read_b64_tr_b16 a[42:43], v226 offset:25600\n\t"
      "buffer_load_dwordx4 v205, s[36:39], s86 offen offset:1024 nt lds\n\t"
      "v_mfma_f32_16x16x32_bf16 v[36:39], a[12:15], a[64:67], v[36:39]\n\t"
      "ds_read_b64_tr_b16 a[44:45], v223 offset:24576\n\t"
      "v_mfma_f32_16x16x32_bf16 v[40:43], a[12:15], a[68:71], v[40:43]\n\t"
      "buffer_load_dwordx4 v206, s[36:39], s86 offen offset:2048 nt lds\n\t"
      "v_mfma_f32_16x16x32_bf16 v[44:47], a[12:15], a[72:75], v[44:47]\n\t"
      "ds_read_b64_tr_b16 a[46:47], v227 offset:25600\n\t"
      "v_mfma_f32_16x16x32_bf16 v[48:51], a[16:19], a[64:67], v[48:51]\n\t"
      "ds_read_b64_tr_b16 a[48:49], v224 offset:24576\n\t"
      "v_mfma_f32_16x16x32_bf16 v[52:55], a[16:19], a[68:71], v[52:55]\n\t"
      "ds_read_b64_tr_b16 a[50:51], v220 offset:25600\n\t"
      "v_mfma_f32_16x16x32_bf16 v[56:59], a[16:19], a[72:75], v[56:59]\n\t"
      "ds_read_b64_tr_b16 a[52:53], v225 offset:24576\n\t"
      "v_mfma_f32_16x16x32_bf16 v[60:63], a[20:23], a[64:67], v[60:63]\n\t"
      "ds_read_b64_tr_b16 a[54:55], v221 offset:25600\n\t"
      "v_mfma_f32_16x16x32_bf16 v[64:67], a[20:23], a[68:71], v[64:67]\n\t"
      "ds_read_b64_tr_b16 a[56:57], v226 offset:24576\n\t"
      "buffer_load_dwordx4 v207, s[36:39], s86 offen offset:3072 nt lds\n\t"
      "v_mfma_f32_16x16x32_bf16 v[68:71], a[20:23], a[72:75], v[68:71]\n\t"
      "ds_read_b64_tr_b16 a[58:59], v222 offset:25600\n\t"
      "v_mfma_f32_16x16x32_bf16 v[72:75], a[24:27], a[64:67], v[72:75]\n\t"
      "ds_read_b64_tr_b16 a[60:61], v227 offset:24576\n\t"
      "v_mfma_f32_16x16x32_bf16 v[76:79], a[24:27], a[68:71], v[76:79]\n\t"
      "ds_read_b64_tr_b16 a[62:63], v223 offset:25600\n\t"
      "s_mov_b32 m0, s95\n\t"
      "v_mfma_f32_16x16x32_bf16 v[80:83], a[24:27], a[72:75], v[80:83]\n\t"
      "buffer_load_dwordx4 v204, s[36:39], s87 offen nt lds\n\t"
      "v_mfma_f32_16x16x32_bf16 v[84:87], a[28:31], a[64:67], v[84:87]\n\t"
      "buffer_load_dwordx4 v205, s[36:39], s87 offen offset:1024 nt lds\n\t"
      "v_mfma_f32_16x16x32_bf16 v[88:91], a[28:31], a[68:71], v[88:91]\n\t"
      "buffer_load_dwordx4 v206, s[36:39], s87 offen offset:2048 nt lds\n\t"
      "v_mfma_f32_16x16x32_bf16 v[92:95], a[28:31], a[72:75], v[92:95]\n\t"
      "buffer_load_dwordx4 v207, s[36:39], s87 offen offset:3072 nt lds\n\t"
      "s_waitcnt lgkmcnt(0)\n\t"
      "s_mov_b32 m0, s96\n\t"
      "v_mfma_f32_16x16x32_bf16 v[0:3], a[32:35], a[76:79], v[0:3]\n\t"
      "buffer_load_dwordx4 v204, s[40:43], s88 offen nt lds\n\t"
      "v_mfma_f32_16x16x32_bf16 v[4:7], a[32:35], a[80:83], v[4:7]\n\t"
      "v_mfma_f32_16x16x32_bf16 v[8:11], a[32:35], a[84:87], v[8:11]\n\t"
      "v_mfma_f32_16x16x32_bf16 v[12:15], a[36:39], a[76:79], v[12:15]\n\t"
      "buffer_load_dwordx4 v205, s[40:43], s88 offen offset:1024 nt lds\n\t"
      "v_mfma_f32_16x16x32_bf16 v[16:19], a[36:39], a[80:83], v[16:19]\n\t"
      "v_mfma_f32_16x16x32_bf16 v[20:23], a[36:39], a[84:87], v[20:23]\n\t"
      "v_mfma_f32_16x16x32_bf16 v[24:27], a[40:43], a[76:79], v[24:27]\n\t"
      "buffer_load_dwordx4 v206, s[40:43], s88 offen offset:2048 nt lds\n\t"
      "v_mfma_f32_16x16x32_bf16 v[28:31], a[40:43], a[80:83], v[28:31]\n\t"
      "v_mfma_f32_16x16x32_bf16 v[32:35], a[40:43], a[84:87], v[32:35]\n\t"
      "v_mfma_f32_16x16x32_bf16 v[36:39], a[44:47], a[76:79], v[36:39]\n\t"
      "buffer_load_dwordx4 v207, s[40:43], s88 offen offset:3072 nt lds\n\t"
      "v_mfma_f32_16x16x32_bf16 v[40:43], a[44:47], a[80:83], v[40:43]\n\t"
      "v_mfma_f32_16x16x32_bf16 v[44:47], a[44:47], a[84:87], v[44:47]\n\t"
      "s_mov_b32 m0, s97\n\t"
      "v_mfma_f32_16x16x32_bf16 v[48:51], a[48:51], a[76:79], v[48:51]\n\t"
      "buffer_load_dwordx4 v204, s[40:43], s89 offen nt lds\n\t"
      "v_mfma_f32_16x16x32_bf16 v[52:55], a[48:51], a[80:83], v[52:55]\n\t"
      "v_mfma_f32_16x16x32_bf16 v[56:59], a[48:51], a[84:87], v[56:59]\n\t"
      "v_mfma_f32_16x16x32_bf16 v[60:63], a[52:55], a[76:79], v[60:63]\n\t"
      "buffer_load_dwordx4 v205, s[40:43], s89 offen offset:1024 nt lds\n\t"
      "v_mfma_f32_16x16x32_bf16 v[64:67], a[52:55], a[80:83], v[64:67]\n\t"
      "v_mfma_f32_16x16x32_bf16 v[68:71], a[52:55], a[84:87], v[68:71]\n\t"
      "v_mfma_f32_16x16x32_bf16 v[72:75], a[56:59], a[76:79], v[72:75]\n\t"
      "buffer_load_dwordx4 v206, s[40:43], s89 offen offset:2048 nt lds\n\t"
      "v_mfma_f32_16x16x32_bf16 v[76:79], a[56:59], a[80:83], v[76:79]\n\t"
      "v_mfma_f32_16x16x32_bf16 v[80:83], a[56:59], a[84:87], v[80:83]\n\t"
      "v_mfma_f32_16x16x32_bf16 v[84:87], a[60:63], a[76:79], v[84:87]\n\t"
      "buffer_load_dwordx4 v207, s[40:43], s89 offen offset:3072 nt lds\n\t"
      "v_mfma_f32_16x16x32_bf16 v[88:91], a[60:63], a[80:83], v[88:91]\n\t"
      "v_mfma_f32_16x16x32_bf16 v[92:95], a[60:63], a[84:87], v[92:95]\n\t"
      "s_mul_i32 s19, s8, 12288\n\t"
      "s_add_u32 s19, s19, 0x20000\n\t"
      "v_xad_u32 v220, v239, 0, s19\n\t"
      "v_xad_u32 v221, v239, 32, s19\n\t"
      "v_xad_u32 v222, v239, 64, s19\n\t"
      "v_xor_b32 v223, 96, v239\n\t"
      "v_add_u32 v223, s19, v223\n\t"
      "v_xor_b32 v224, 128, v239\n\t"
      "v_add_u32 v224, s19, v224\n\t"
      "v_xor_b32 v225, 160, v239\n\t"
      "v_add_u32 v225, s19, v225\n\t"
      "v_xor_b32 v226, 192, v239\n\t"
      "v_add_u32 v226, s19, v226\n\t"
      "v_xor_b32 v227, 224, v239\n\t"
      "v_add_u32 v227, s19, v227\n\t"
      "s_mul_i32 s22, s7, 12288\n\t"
      "v_add_u32 v228, s22, v228\n\t"
      "v_add_u32 v229, s22, v229\n\t"
      "v_add_u32 v230, s22, v230\n\t"
      "v_add_u32 v231, s22, v231\n\t"
      "v_cvt_pk_bf16_f32 v0, v0, v1\n\t"
      "v_cvt_pk_bf16_f32 v1, v2, v3\n\t"
      "v_cvt_pk_bf16_f32 v4, v4, v5\n\t"
      "v_cvt_pk_bf16_f32 v5, v6, v7\n\t"
      "v_cvt_pk_bf16_f32 v8, v8, v9\n\t"
      "v_cvt_pk_bf16_f32 v9, v10, v11\n\t"
      "v_cvt_pk_bf16_f32 v12, v12, v13\n\t"
      "v_cvt_pk_bf16_f32 v13, v14, v15\n\t"
      "v_cvt_pk_bf16_f32 v16, v16, v17\n\t"
      "v_cvt_pk_bf16_f32 v17, v18, v19\n\t"
      "v_cvt_pk_bf16_f32 v20, v20, v21\n\t"
      "v_cvt_pk_bf16_f32 v21, v22, v23\n\t"
      "v_cvt_pk_bf16_f32 v24, v24, v25\n\t"
      "v_cvt_pk_bf16_f32 v25, v26, v27\n\t"
      "v_cvt_pk_bf16_f32 v28, v28, v29\n\t"
      "v_cvt_pk_bf16_f32 v29, v30, v31\n\t"
      "v_cvt_pk_bf16_f32 v32, v32, v33\n\t"
      "v_cvt_pk_bf16_f32 v33, v34, v35\n\t"
      "v_cvt_pk_bf16_f32 v36, v36, v37\n\t"
      "v_cvt_pk_bf16_f32 v37, v38, v39\n\t"
      "v_cvt_pk_bf16_f32 v40, v40, v41\n\t"
      "v_cvt_pk_bf16_f32 v41, v42, v43\n\t"
      "v_cvt_pk_bf16_f32 v44, v44, v45\n\t"
      "v_cvt_pk_bf16_f32 v45, v46, v47\n\t"
      "v_cvt_pk_bf16_f32 v48, v48, v49\n\t"
      "v_cvt_pk_bf16_f32 v49, v50, v51\n\t"
      "v_cvt_pk_bf16_f32 v52, v52, v53\n\t"
      "v_cvt_pk_bf16_f32 v53, v54, v55\n\t"
      "v_cvt_pk_bf16_f32 v56, v56, v57\n\t"
      "v_cvt_pk_bf16_f32 v57, v58, v59\n\t"
      "v_cvt_pk_bf16_f32 v60, v60, v61\n\t"
      "v_cvt_pk_bf16_f32 v61, v62, v63\n\t"
      "v_cvt_pk_bf16_f32 v64, v64, v65\n\t"
      "v_cvt_pk_bf16_f32 v65, v66, v67\n\t"
      "v_cvt_pk_bf16_f32 v68, v68, v69\n\t"
      "v_cvt_pk_bf16_f32 v69, v70, v71\n\t"
      "v_cvt_pk_bf16_f32 v72, v72, v73\n\t"
      "v_cvt_pk_bf16_f32 v73, v74, v75\n\t"
      "v_cvt_pk_bf16_f32 v76, v76, v77\n\t"
      "v_cvt_pk_bf16_f32 v77, v78, v79\n\t"
      "v_cvt_pk_bf16_f32 v80, v80, v81\n\t"
      "v_cvt_pk_bf16_f32 v81, v82, v83\n\t"
      "v_cvt_pk_bf16_f32 v84, v84, v85\n\t"
      "v_cvt_pk_bf16_f32 v85, v86, v87\n\t"
      "v_cvt_pk_bf16_f32 v88, v88, v89\n\t"
      "v_cvt_pk_bf16_f32 v89, v90, v91\n\t"
      "v_cvt_pk_bf16_f32 v92, v92, v93\n\t"
      "v_cvt_pk_bf16_f32 v93, v94, v95\n\t"
      "s_barrier\n\t"
      "s_cmp_lg_u32 s7, 0\n\t"
      "s_cbranch_scc1 L_qw0_%=\n\t"
      "ds_write_b64 v220, v[0:1]\n\t"
      "ds_write_b64 v220, v[4:5] offset:4096\n\t"
      "ds_write_b64 v220, v[8:9] offset:8192\n\t"
      "ds_write_b64 v221, v[12:13]\n\t"
      "ds_write_b64 v221, v[16:17] offset:4096\n\t"
      "ds_write_b64 v221, v[20:21] offset:8192\n\t"
      "ds_write_b64 v222, v[24:25]\n\t"
      "ds_write_b64 v222, v[28:29] offset:4096\n\t"
      "ds_write_b64 v222, v[32:33] offset:8192\n\t"
      "ds_write_b64 v223, v[36:37]\n\t"
      "ds_write_b64 v223, v[40:41] offset:4096\n\t"
      "ds_write_b64 v223, v[44:45] offset:8192\n\t"
      "ds_write_b64 v224, v[48:49]\n\t"
      "ds_write_b64 v224, v[52:53] offset:4096\n\t"
      "ds_write_b64 v224, v[56:57] offset:8192\n\t"
      "ds_write_b64 v225, v[60:61]\n\t"
      "ds_write_b64 v225, v[64:65] offset:4096\n\t"
      "ds_write_b64 v225, v[68:69] offset:8192\n\t"
      "ds_write_b64 v226, v[72:73]\n\t"
      "ds_write_b64 v226, v[76:77] offset:4096\n\t"
      "ds_write_b64 v226, v[80:81] offset:8192\n\t"
      "ds_write_b64 v227, v[84:85]\n\t"
      "ds_write_b64 v227, v[88:89] offset:4096\n\t"
      "ds_write_b64 v227, v[92:93] offset:8192\n\t"
      "s_waitcnt lgkmcnt(0)\n\t"
      "L_qw0_%=:\n\t"
      "s_barrier\n\t"
      "ds_read_b128 a[64:67], v228\n\t"
      "ds_read_b128 a[68:71], v228 offset:4096\n\t"
      "ds_read_b128 a[72:75], v228 offset:8192\n\t"
      "ds_read_b128 a[76:79], v229\n\t"
      "ds_read_b128 a[80:83], v229 offset:4096\n\t"
      "ds_read_b128 a[84:87], v229 offset:8192\n\t"
      "ds_read_b128 a[88:91], v230\n\t"
      "ds_read_b128 a[92:95], v230 offset:4096\n\t"
      "ds_read_b128 a[96:99], v230 offset:8192\n\t"
      "ds_read_b128 a[100:103], v231\n\t"
      "ds_read_b128 a[104:107], v231 offset:4096\n\t"
      "ds_read_b128 a[108:111], v231 offset:8192\n\t"
      "s_waitcnt lgkmcnt(0)\n\t"
      "s_barrier\n\t"
      "s_cmp_lg_u32 s7, 1\n\t"
      "s_cbranch_scc1 L_qw1_%=\n\t"
      "ds_write_b64 v220, v[0:1]\n\t"
      "ds_write_b64 v220, v[4:5] offset:4096\n\t"
      "ds_write_b64 v220, v[8:9] offset:8192\n\t"
      "ds_write_b64 v221, v[12:13]\n\t"
      "ds_write_b64 v221, v[16:17] offset:4096\n\t"
      "ds_write_b64 v221, v[20:21] offset:8192\n\t"
      "ds_write_b64 v222, v[24:25]\n\t"
      "ds_write_b64 v222, v[28:29] offset:4096\n\t"
      "ds_write_b64 v222, v[32:33] offset:8192\n\t"
      "ds_write_b64 v223, v[36:37]\n\t"
      "ds_write_b64 v223, v[40:41] offset:4096\n\t"
      "ds_write_b64 v223, v[44:45] offset:8192\n\t"
      "ds_write_b64 v224, v[48:49]\n\t"
      "ds_write_b64 v224, v[52:53] offset:4096\n\t"
      "ds_write_b64 v224, v[56:57] offset:8192\n\t"
      "ds_write_b64 v225, v[60:61]\n\t"
      "ds_write_b64 v225, v[64:65] offset:4096\n\t"
      "ds_write_b64 v225, v[68:69] offset:8192\n\t"
      "ds_write_b64 v226, v[72:73]\n\t"
      "ds_write_b64 v226, v[76:77] offset:4096\n\t"
      "ds_write_b64 v226, v[80:81] offset:8192\n\t"
      "ds_write_b64 v227, v[84:85]\n\t"
      "ds_write_b64 v227, v[88:89] offset:4096\n\t"
      "ds_write_b64 v227, v[92:93] offset:8192\n\t"
      "s_waitcnt lgkmcnt(0)\n\t"
      "L_qw1_%=:\n\t"
      "s_waitcnt vmcnt(24)\n\t"
      "s_barrier\n\t"
      "ds_read_b128 a[112:115], v228\n\t"
      "ds_read_b128 a[116:119], v228 offset:4096\n\t"
      "ds_read_b128 a[120:123], v228 offset:8192\n\t"
      "ds_read_b128 a[124:127], v229\n\t"
      "ds_read_b128 a[128:131], v229 offset:4096\n\t"
      "ds_read_b128 a[132:135], v229 offset:8192\n\t"
      "ds_read_b128 a[136:139], v230\n\t"
      "ds_read_b128 a[140:143], v230 offset:4096\n\t"
      "ds_read_b128 a[144:147], v230 offset:8192\n\t"
      "ds_read_b128 a[148:151], v231\n\t"
      "ds_read_b128 a[152:155], v231 offset:4096\n\t"
      "ds_read_b128 a[156:159], v231 offset:8192\n\t"
      "s_branch L_B_%=\n\t"
      "L_A_%=:\n\t"
      "s_lshl_b32 s18, s27, 15\n\t"
      "v_mov_b32 v236, 0xff800000\n\t"
      "v_mov_b32 v237, 0xff800000\n\t"
      "v_mov_b32 v238, 0xff800000\n\t"
      "v_xor_b32 v220, 0, v217\n\t"
      "v_xor_b32 v221, 64, v217\n\t"
      "v_xor_b32 v222, 128, v217\n\t"
      "v_xor_b32 v223, 192, v217\n\t"
      "v_xor_b32 v224, 0, v217\n\t"
      "v_add_u32 v224, 0x10000, v224\n\t"
      "v_xor_b32 v225, 64, v217\n\t"
      "v_add_u32 v225, 0x10000, v225\n\t"
      "v_xor_b32 v226, 128, v217\n\t"
      "v_add_u32 v226, 0x10000, v226\n\t"
      "v_xor_b32 v227, 192, v217\n\t"
      "v_add_u32 v227, 0x10000, v227\n\t"
      "ds_read_b128 a[0:3], v220\n\t"
      "ds_read_b128 a[4:7], v221\n\t"
      "ds_read_b128 a[8:11], v222\n\t"
      "ds_read_b128 a[12:15], v223\n\t"
      "ds_read_b128 a[16:19], v220 offset:32768\n\t"
      "ds_read_b128 a[20:23], v221 offset:32768\n\t"
      "ds_read_b128 a[24:27], v222 offset:32768\n\t"
      "ds_read_b128 a[28:31], v223 offset:32768\n\t"
      "s_waitcnt lgkmcnt(0)\n\t"
      "v_mfma_f32_16x16x32_bf16 v[0:3], a[0:3], a[64:67], 0\n\t"
      "ds_read_b128 a[32:35], v224\n\t"
      "v_mfma_f32_16x16x32_bf16 v[0:3], a[4:7], a[76:79], v[0:3]\n\t"
      "v_mfma_f32_16x16x32_bf16 v[0:3], a[8:11], a[88:91], v[0:3]\n\t"
      "ds_read_b128 a[36:39], v225\n\t"
      "v_mfma_f32_16x16x32_bf16 v[0:3], a[12:15], a[100:103], v[0:3]\n\t"
      "v_mfma_f32_16x16x32_bf16 v[4:7], a[0:3], a[68:71], 0\n\t"
      "ds_read_b128 a[40:43], v226\n\t"
      "v_mfma_f32_16x16x32_bf16 v[4:7], a[4:7], a[80:83], v[4:7]\n\t"
      "v_mfma_f32_16x16x32_bf16 v[4:7], a[8:11], a[92:95], v[4:7]\n\t"
      "ds_read_b128 a[44:47], v227\n\t"
      "v_mfma_f32_16x16x32_bf16 v[4:7], a[12:15], a[104:107], v[4:7]\n\t"
      "v_mfma_f32_16x16x32_bf16 v[8:11], a[0:3], a[72:75], 0\n\t"
      "ds_read_b128 a[48:51], v224 offset:32768\n\t"
      "v_mfma_f32_16x16x32_bf16 v[8:11], a[4:7], a[84:87], v[8:11]\n\t"
      "v_mfma_f32_16x16x32_bf16 v[8:11], a[8:11], a[96:99], v[8:11]\n\t"
      "ds_read_b128 a[52:55], v225 offset:32768\n\t"
      "v_mfma_f32_16x16x32_bf16 v[8:11], a[12:15], a[108:111], v[8:11]\n\t"
      "v_mfma_f32_16x16x32_bf16 v[12:15], a[16:19], a[64:67], 0\n\t"
      "ds_read_b128 a[56:59], v226 offset:32768\n\t"
      "v_mfma_f32_16x16x32_bf16 v[12:15], a[20:23], a[76:79], v[12:15]\n\t"
      "v_mfma_f32_16x16x32_bf16 v[12:15], a[24:27], a[88:91], v[12:15]\n\t"
      "ds_read_b128 a[60:63], v227 offset:32768\n\t"
      "v_mfma_f32_16x16x32_bf16 v[12:15], a[28:31], a[100:103], v[12:15]\n\t"
      "v_mfma_f32_16x16x32_bf16 v[16:19], a[16:19], a[68:71], 0\n\t"
      "v_mfma_f32_16x16x32_bf16 v[16:19], a[20:23], a[80:83], v[16:19]\n\t"
      "v_mfma_f32_16x16x32_bf16 v[16:19], a[24:27], a[92:95], v[16:19]\n\t"
      "v_mfma_f32_16x16x32_bf16 v[16:19], a[28:31], a[104:107], v[16:19]\n\t"
      "v_mfma_f32_16x16x32_bf16 v[20:23], a[16:19], a[72:75], 0\n\t"
      "v_mfma_f32_16x16x32_bf16 v[20:23], a[20:23], a[84:87], v[20:23]\n\t"
      "v_mfma_f32_16x16x32_bf16 v[20:23], a[24:27], a[96:99], v[20:23]\n\t"
      "v_mfma_f32_16x16x32_bf16 v[20:23], a[28:31], a[108:111], v[20:23]\n\t"
      "s_waitcnt lgkmcnt(0)\n\t"
      "s_barrier\n\t"
      "v_mfma_f32_16x16x32_bf16 v[24:27], a[32:35], a[64:67], 0\n\t"
      "ds_read_b128 a[0:3], v220 offset:4096\n\t"
      "s_mov_b32 m0, s90\n\t"
      "v_mfma_f32_16x16x32_bf16 v[24:27], a[36:39], a[76:79], v[24:27]\n\t"
      "buffer_load_dwordx4 v204, s[28:31], s56 offen nt lds\n\t"
      "v_mfma_f32_16x16x32_bf16 v[24:27], a[40:43], a[88:91], v[24:27]\n\t"
      "ds_read_b128 a[4:7], v221 offset:4096\n\t"
      "v_mfma_f32_16x16x32_bf16 v[24:27], a[44:47], a[100:103], v[24:27]\n\t"
      "v_mfma_f32_16x16x32_bf16 v[28:31], a[32:35], a[68:71], 0\n\t"
      "ds_read_b128 a[8:11], v222 offset:4096\n\t"
      "v_mfma_f32_16x16x32_bf16 v[28:31], a[36:39], a[80:83], v[28:31]\n\t"
      "v_mfma_f32_16x16x32_bf16 v[28:31], a[40:43], a[92:95], v[28:31]\n\t"
      "ds_read_b128 a[12:15], v223 offset:4096\n\t"
      "v_mfma_f32_16x16x32_bf16 v[28:31], a[44:47], a[104:107], v[28:31]\n\t"
      "v_mfma_f32_16x16x32_bf16 v[32:35], a[32:35], a[72:75], 0\n\t"
      "ds_read_b128 a[16:19], v220 offset:36864\n\t"
      "v_maximum3_f32 v236, v236, v0, v1\n\t"
      "v_mfma_f32_16x16x32_bf16 v[32:35], a[36:39], a[84:87], v[32:35]\n\t"
      "buffer_load_dwordx4 v205, s[28:31], s56 offen offset:1024 nt lds\n\t"
      "v_maximum3_f32 v237, v237, v4, v5\n\t"
      "v_mfma_f32_16x16x32_bf16 v[32:35], a[40:43], a[96:99], v[32:35]\n\t"
      "ds_read_b128 a[20:23], v221 offset:36864\n\t"
      "v_maximum3_f32 v238, v238, v8, v9\n\t"
      "v_mfma_f32_16x16x32_bf16 v[32:35], a[44:47], a[108:111], v[32:35]\n\t"
      "v_mfma_f32_16x16x32_bf16 v[36:39], a[48:51], a[64:67], 0\n\t"
      "ds_read_b128 a[24:27], v222 offset:36864\n\t"
      "v_maximum3_f32 v236, v236, v2, v3\n\t"
      "v_mfma_f32_16x16x32_bf16 v[36:39], a[52:55], a[76:79], v[36:39]\n\t"
      "v_maximum3_f32 v237, v237, v6, v7\n\t"
      "v_mfma_f32_16x16x32_bf16 v[36:39], a[56:59], a[88:91], v[36:39]\n\t"
      "ds_read_b128 a[28:31], v223 offset:36864\n\t"
      "v_maximum3_f32 v238, v238, v10, v11\n\t"
      "v_mfma_f32_16x16x32_bf16 v[36:39], a[60:63], a[100:103], v[36:39]\n\t"
      "v_mfma_f32_16x16x32_bf16 v[40:43], a[48:51], a[68:71], 0\n\t"
      "buffer_load_dwordx4 v206, s[28:31], s56 offen offset:2048 nt lds\n\t"
      "v_maximum3_f32 v236, v236, v12, v13\n\t"
      "v_mfma_f32_16x16x32_bf16 v[40:43], a[52:55], a[80:83], v[40:43]\n\t"
      "v_maximum3_f32 v237, v237, v16, v17\n\t"
      "v_mfma_f32_16x16x32_bf16 v[40:43], a[56:59], a[92:95], v[40:43]\n\t"
      "v_maximum3_f32 v238, v238, v20, v21\n\t"
      "v_mfma_f32_16x16x32_bf16 v[40:43], a[60:63], a[104:107], v[40:43]\n\t"
      "v_mfma_f32_16x16x32_bf16 v[44:47], a[48:51], a[72:75], 0\n\t"
      "buffer_load_dwordx4 v207, s[28:31], s56 offen offset:3072 nt lds\n\t"
      "v_maximum3_f32 v236, v236, v14, v15\n\t"
      "v_mfma_f32_16x16x32_bf16 v[44:47], a[52:55], a[84:87], v[44:47]\n\t"
      "v_maximum3_f32 v237, v237, v18, v19\n\t"
      "v_mfma_f32_16x16x32_bf16 v[44:47], a[56:59], a[96:99], v[44:47]\n\t"
      "v_maximum3_f32 v238, v238, v22, v23\n\t"
      "v_mfma_f32_16x16x32_bf16 v[44:47], a[60:63], a[108:111], v[44:47]\n\t"
      "s_waitcnt lgkmcnt(0)\n\t"
      "v_mfma_f32_16x16x32_bf16 v[48:51], a[0:3], a[64:67], 0\n\t"
      "ds_read_b128 a[32:35], v224 offset:4096\n\t"
      "v_mfma_f32_16x16x32_bf16 v[48:51], a[4:7], a[76:79], v[48:51]\n\t"
      "v_mfma_f32_16x16x32_bf16 v[48:51], a[8:11], a[88:91], v[48:51]\n\t"
      "ds_read_b128 a[36:39], v225 offset:4096\n\t"
      "v_mfma_f32_16x16x32_bf16 v[48:51], a[12:15], a[100:103], v[48:51]\n\t"
      "v_mfma_f32_16x16x32_bf16 v[52:55], a[0:3], a[68:71], 0\n\t"
      "ds_read_b128 a[40:43], v226 offset:4096\n\t"
      "v_mfma_f32_16x16x32_bf16 v[52:55], a[4:7], a[80:83], v[52:55]\n\t"
      "v_mfma_f32_16x16x32_bf16 v[52:55], a[8:11], a[92:95], v[52:55]\n\t"
      "ds_read_b128 a[44:47], v227 offset:4096\n\t"
      "v_mfma_f32_16x16x32_bf16 v[52:55], a[12:15], a[104:107], v[52:55]\n\t"
      "v_mfma_f32_16x16x32_bf16 v[56:59], a[0:3], a[72:75], 0\n\t"
      "ds_read_b128 a[48:51], v224 offset:36864\n\t"
      "v_maximum3_f32 v236, v236, v24, v25\n\t"
      "v_mfma_f32_16x16x32_bf16 v[56:59], a[4:7], a[84:87], v[56:59]\n\t"
      "v_maximum3_f32 v237, v237, v28, v29\n\t"
      "v_mfma_f32_16x16x32_bf16 v[56:59], a[8:11], a[96:99], v[56:59]\n\t"
      "ds_read_b128 a[52:55], v225 offset:36864\n\t"
      "v_maximum3_f32 v238, v238, v32, v33\n\t"
      "v_mfma_f32_16x16x32_bf16 v[56:59], a[12:15], a[108:111], v[56:59]\n\t"
      "v_mfma_f32_16x16x32_bf16 v[60:63], a[16:19], a[64:67], 0\n\t"
      "ds_read_b128 a[56:59], v226 offset:36864\n\t"
      "v_maximum3_f32 v236, v236, v26, v27\n\t"
      "v_mfma_f32_16x16x32_bf16 v[60:63], a[20:23], a[76:79], v[60:63]\n\t"
      "v_maximum3_f32 v237, v237, v30, v31\n\t"
      "v_mfma_f32_16x16x32_bf16 v[60:63], a[24:27], a[88:91], v[60:63]\n\t"
      "ds_read_b128 a[60:63], v227 offset:36864\n\t"
      "v_maximum3_f32 v238, v238, v34, v35\n\t"
      "v_mfma_f32_16x16x32_bf16 v[60:63], a[28:31], a[100:103], v[60:63]\n\t"
      "v_mfma_f32_16x16x32_bf16 v[64:67], a[16:19], a[68:71], 0\n\t"
      "v_maximum3_f32 v236, v236, v36, v37\n\t"
      "v_mfma_f32_16x16x32_bf16 v[64:67], a[20:23], a[80:83], v[64:67]\n\t"
      "v_maximum3_f32 v237, v237, v40, v41\n\t"
      "v_mfma_f32_16x16x32_bf16 v[64:67], a[24:27], a[92:95], v[64:67]\n\t"
      "v_maximum3_f32 v238, v238, v44, v45\n\t"
      "v_mfma_f32_16x16x32_bf16 v[64:67], a[28:31], a[104:107], v[64:67]\n\t"
      "v_mfma_f32_16x16x32_bf16 v[68:71], a[16:19], a[72:75], 0\n\t"
      "v_maximum3_f32 v236, v236, v38, v39\n\t"
      "v_mfma_f32_16x16x32_bf16 v[68:71], a[20:23], a[84:87], v[68:71]\n\t"
      "v_maximum3_f32 v237, v237, v42, v43\n\t"
      "v_mfma_f32_16x16x32_bf16 v[68:71], a[24:27], a[96:99], v[68:71]\n\t"
      "v_maximum3_f32 v238, v238, v46, v47\n\t"
      "v_mfma_f32_16x16x32_bf16 v[68:71], a[28:31], a[108:111], v[68:71]\n\t"
      "s_waitcnt lgkmcnt(0)\n\t"
      "s_barrier\n\t"
      "s_mov_b32 m0, s91\n\t"
      "v_mfma_f32_16x16x32_bf16 v[72:75], a[32:35], a[64:67], 0\n\t"
      "buffer_load_dwordx4 v204, s[28:31], s57 offen nt lds\n\t"
      "v_xor_b32 v220, 0, v218\n\t"
      "v_xor_b32 v221, 32, v218\n\t"
      "v_mfma_f32_16x16x32_bf16 v[72:75], a[36:39], a[76:79], v[72:75]\n\t"
      "v_xor_b32 v222, 64, v218\n\t"
      "v_xor_b32 v223, 96, v218\n\t"
      "v_mfma_f32_16x16x32_bf16 v[72:75], a[40:43], a[88:91], v[72:75]\n\t"
      "buffer_load_dwordx4 v205, s[28:31], s57 offen offset:1024 nt lds\n\t"
      "v_xor_b32 v224, 128, v218\n\t"
      "v_xor_b32 v225, 160, v218\n\t"
      "v_mfma_f32_16x16x32_bf16 v[72:75], a[44:47], a[100:103], v[72:75]\n\t"
      "v_xor_b32 v226, 192, v218\n\t"
      "v_xor_b32 v227, 224, v218\n\t"
      "v_mfma_f32_16x16x32_bf16 v[76:79], a[32:35], a[68:71], 0\n\t"
      "buffer_load_dwordx4 v206, s[28:31], s57 offen offset:2048 nt lds\n\t"
      "v_xor_b32 v228, 0, v218\n\t"
      "v_add_u32 v228, 0x10000, v228\n\t"
      "v_xor_b32 v229, 32, v218\n\t"
      "v_add_u32 v229, 0x10000, v229\n\t"
      "v_mfma_f32_16x16x32_bf16 v[76:79], a[36:39], a[80:83], v[76:79]\n\t"
      "v_xor_b32 v230, 64, v218\n\t"
      "v_add_u32 v230, 0x10000, v230\n\t"
      "v_xor_b32 v231, 96, v218\n\t"
      "v_add_u32 v231, 0x10000, v231\n\t"
      "v_mfma_f32_16x16x32_bf16 v[76:79], a[40:43], a[92:95], v[76:79]\n\t"
      "buffer_load_dwordx4 v207, s[28:31], s57 offen offset:3072 nt lds\n\t"
      "v_xor_b32 v232, 128, v218\n\t"
      "v_add_u32 v232, 0x10000, v232\n\t"
      "v_xor_b32 v233, 160, v218\n\t"
      "v_add_u32 v233, 0x10000, v233\n\t"
      "v_mfma_f32_16x16x32_bf16 v[76:79], a[44:47], a[104:107], v[76:79]\n\t"
      "ds_read_b64_tr_b16 a[0:1], v220 offset:8192\n\t"
      "v_xor_b32 v234, 192, v218\n\t"
      "v_add_u32 v234, 0x10000, v234\n\t"
      "v_xor_b32 v235, 224, v218\n\t"
      "v_add_u32 v235, 0x10000, v235\n\t"
      "v_mfma_f32_16x16x32_bf16 v[80:83], a[32:35], a[72:75], 0\n\t"
      "ds_read_b64_tr_b16 a[2:3], v220 offset:40960\n\t"
      "v_maximum3_f32 v236, v236, v48, v49\n\t"
      "v_mfma_f32_16x16x32_bf16 v[80:83], a[36:39], a[84:87], v[80:83]\n\t"
      "ds_read_b64_tr_b16 a[4:5], v221 offset:8192\n\t"
      "v_maximum3_f32 v237, v237, v52, v53\n\t"
      "v_mfma_f32_16x16x32_bf16 v[80:83], a[40:43], a[96:99], v[80:83]\n\t"
      "ds_read_b64_tr_b16 a[6:7], v221 offset:40960\n\t"
      "v_maximum3_f32 v238, v238, v56, v57\n\t"
      "v_mfma_f32_16x16x32_bf16 v[80:83], a[44:47], a[108:111], v[80:83]\n\t"
      "ds_read_b64_tr_b16 a[8:9], v222 offset:8192\n\t"
      "v_mfma_f32_16x16x32_bf16 v[84:87], a[48:51], a[64:67], 0\n\t"
      "ds_read_b64_tr_b16 a[10:11], v222 offset:40960\n\t"
      "v_maximum3_f32 v236, v236, v50, v51\n\t"
      "v_mfma_f32_16x16x32_bf16 v[84:87], a[52:55], a[76:79], v[84:87]\n\t"
      "ds_read_b64_tr_b16 a[12:13], v223 offset:8192\n\t"
      "v_maximum3_f32 v237, v237, v54, v55\n\t"
      "v_mfma_f32_16x16x32_bf16 v[84:87], a[56:59], a[88:91], v[84:87]\n\t"
      "ds_read_b64_tr_b16 a[14:15], v223 offset:40960\n\t"
      "v_maximum3_f32 v238, v238, v58, v59\n\t"
      "v_mfma_f32_16x16x32_bf16 v[84:87], a[60:63], a[100:103], v[84:87]\n\t"
      "ds_read_b64_tr_b16 a[16:17], v224 offset:8192\n\t"
      "v_mfma_f32_16x16x32_bf16 v[88:91], a[48:51], a[68:71], 0\n\t"
      "ds_read_b64_tr_b16 a[18:19], v224 offset:40960\n\t"
      "v_maximum3_f32 v236, v236, v60, v61\n\t"
      "v_mfma_f32_16x16x32_bf16 v[88:91], a[52:55], a[80:83], v[88:91]\n\t"
      "ds_read_b64_tr_b16 a[20:21], v225 offset:8192\n\t"
      "v_maximum3_f32 v237, v237, v64, v65\n\t"
      "v_mfma_f32_16x16x32_bf16 v[88:91], a[56:59], a[92:95], v[88:91]\n\t"
      "ds_read_b64_tr_b16 a[22:23], v225 offset:40960\n\t"
      "v_maximum3_f32 v238, v238, v68, v69\n\t"
      "v_mfma_f32_16x16x32_bf16 v[88:91], a[60:63], a[104:107], v[88:91]\n\t"
      "ds_read_b64_tr_b16 a[24:25], v226 offset:8192\n\t"
      "v_mfma_f32_16x16x32_bf16 v[92:95], a[48:51], a[72:75], 0\n\t"
      "ds_read_b64_tr_b16 a[26:27], v226 offset:40960\n\t"
      "v_maximum3_f32 v236, v236, v62, v63\n\t"
      "v_mfma_f32_16x16x32_bf16 v[92:95], a[52:55], a[84:87], v[92:95]\n\t"
      "ds_read_b64_tr_b16 a[28:29], v227 offset:8192\n\t"
      "v_maximum3_f32 v237, v237, v66, v67\n\t"
      "v_mfma_f32_16x16x32_bf16 v[92:95], a[56:59], a[96:99], v[92:95]\n\t"
      "ds_read_b64_tr_b16 a[30:31], v227 offset:40960\n\t"
      "v_maximum3_f32 v238, v238, v70, v71\n\t"
      "v_mfma_f32_16x16x32_bf16 v[92:95], a[60:63], a[108:111], v[92:95]\n\t"
      "ds_read_b64_tr_b16 a[32:33], v228 offset:8192\n\t"
      "ds_read_b64_tr_b16 a[34:35], v228 offset:40960\n\t"
      "ds_read_b64_tr_b16 a[36:37], v229 offset:8192\n\t"
      "ds_read_b64_tr_b16 a[38:39], v229 offset:40960\n\t"
      "ds_read_b64_tr_b16 a[40:41], v230 offset:8192\n\t"
      "v_maximum3_f32 v236, v236, v72, v73\n\t"
      "v_maximum3_f32 v237, v237, v76, v77\n\t"
      "v_maximum3_f32 v238, v238, v80, v81\n\t"
      "v_maximum3_f32 v236, v236, v74, v75\n\t"
      "v_maximum3_f32 v237, v237, v78, v79\n\t"
      "v_maximum3_f32 v238, v238, v82, v83\n\t"
      "v_maximum3_f32 v236, v236, v84, v85\n\t"
      "v_maximum3_f32 v237, v237, v88, v89\n\t"
      "v_maximum3_f32 v238, v238, v92, v93\n\t"
      "v_maximum3_f32 v236, v236, v86, v87\n\t"
      "v_maximum3_f32 v237, v237, v90, v91\n\t"
      "v_maximum3_f32 v238, v238, v94, v95\n\t"
      "v_mov_b32 v246, v236\n\t"
      "v_mov_b32 v247, v237\n\t"
      "v_mov_b32 v248, v238\n\t"
      "v_permlane16_swap_b32 v246, v236\n\t"
      "v_permlane16_swap_b32 v247, v237\n\t"
      "v_permlane16_swap_b32 v248, v238\n\t"
      "v_maximum3_f32 v236, v236, v246, v246\n\t"
      "v_maximum3_f32 v237, v237, v247, v247\n\t"
      "v_maximum3_f32 v238, v238, v248, v248\n\t"
      "v_mov_b32 v246, v236\n\t"
      "v_mov_b32 v247, v237\n\t"
      "v_mov_b32 v248, v238\n\t"
      "v_permlane32_swap_b32 v246, v236\n\t"
      "v_permlane32_swap_b32 v247, v237\n\t"
      "v_permlane32_swap_b32 v248, v238\n\t"
      "v_maximum3_f32 v236, v236, v246, v246\n\t"
      "v_maximum3_f32 v237, v237, v247, v247\n\t"
      "v_maximum3_f32 v238, v238, v248, v248\n\t"
      "v_mul_f32 v240, 0xbe0293ee, v236\n\t"
      "v_mul_f32 v242, 0xbe0293ee, v237\n\t"
      "v_mul_f32 v244, 0xbe0293ee, v238\n\t"
      "v_pk_fma_f32 v[0:1], v[0:1], s[26:27], v[240:241] op_sel_hi:[1,0,0]\n\t"
      "v_pk_fma_f32 v[2:3], v[2:3], s[26:27], v[240:241] op_sel_hi:[1,0,0]\n\t"
      "v_pk_fma_f32 v[12:13], v[12:13], s[26:27], v[240:241] op_sel_hi:[1,0,0]\n\t"
      "v_pk_fma_f32 v[14:15], v[14:15], s[26:27], v[240:241] op_sel_hi:[1,0,0]\n\t"
      "v_exp_f32 v0, v0\n\t"
      "v_exp_f32 v1, v1\n\t"
      "v_exp_f32 v2, v2\n\t"
      "v_exp_f32 v3, v3\n\t"
      "v_exp_f32 v12, v12\n\t"
      "v_exp_f32 v13, v13\n\t"
      "v_exp_f32 v14, v14\n\t"
      "v_exp_f32 v15, v15\n\t"
      "v_pk_fma_f32 v[4:5], v[4:5], s[26:27], v[242:243] op_sel_hi:[1,0,0]\n\t"
      "v_pk_fma_f32 v[6:7], v[6:7], s[26:27], v[242:243] op_sel_hi:[1,0,0]\n\t"
      "v_pk_fma_f32 v[16:17], v[16:17], s[26:27], v[242:243] op_sel_hi:[1,0,0]\n\t"
      "v_pk_fma_f32 v[18:19], v[18:19], s[26:27], v[242:243] op_sel_hi:[1,0,0]\n\t"
      "ds_read_b64_tr_b16 a[42:43], v230 offset:40960\n\t"
      "ds_read_b64_tr_b16 a[44:45], v231 offset:8192\n\t"
      "v_cvt_pk_bf16_f32 v0, v0, v1\n\t"
      "v_cvt_pk_bf16_f32 v1, v2, v3\n\t"
      "v_cvt_pk_bf16_f32 v2, v12, v13\n\t"
      "v_cvt_pk_bf16_f32 v3, v14, v15\n\t"
      "v_exp_f32 v4, v4\n\t"
      "v_exp_f32 v5, v5\n\t"
      "v_exp_f32 v6, v6\n\t"
      "v_exp_f32 v7, v7\n\t"
      "v_exp_f32 v16, v16\n\t"
      "v_exp_f32 v17, v17\n\t"
      "v_exp_f32 v18, v18\n\t"
      "v_exp_f32 v19, v19\n\t"
      "v_pk_fma_f32 v[8:9], v[8:9], s[26:27], v[244:245] op_sel_hi:[1,0,0]\n\t"
      "v_pk_fma_f32 v[10:11], v[10:11], s[26:27], v[244:245] op_sel_hi:[1,0,0]\n\t"
      "v_pk_fma_f32 v[20:21], v[20:21], s[26:27], v[244:245] op_sel_hi:[1,0,0]\n\t"
      "v_pk_fma_f32 v[22:23], v[22:23], s[26:27], v[244:245] op_sel_hi:[1,0,0]\n\t"
      "ds_read_b64_tr_b16 a[46:47], v231 offset:40960\n\t"
      "ds_read_b64_tr_b16 a[48:49], v232 offset:8192\n\t"
      "v_cvt_pk_bf16_f32 v4, v4, v5\n\t"
      "v_cvt_pk_bf16_f32 v5, v6, v7\n\t"
      "v_cvt_pk_bf16_f32 v6, v16, v17\n\t"
      "v_cvt_pk_bf16_f32 v7, v18, v19\n\t"
      "v_exp_f32 v8, v8\n\t"
      "v_exp_f32 v9, v9\n\t"
      "v_exp_f32 v10, v10\n\t"
      "v_exp_f32 v11, v11\n\t"
      "v_exp_f32 v20, v20\n\t"
      "v_exp_f32 v21, v21\n\t"
      "v_exp_f32 v22, v22\n\t"
      "v_exp_f32 v23, v23\n\t"
      "v_pk_fma_f32 v[24:25], v[24:25], s[26:27], v[240:241] op_sel_hi:[1,0,0]\n\t"
      "v_pk_fma_f32 v[26:27], v[26:27], s[26:27], v[240:241] op_sel_hi:[1,0,0]\n\t"
      "v_pk_fma_f32 v[36:37], v[36:37], s[26:27], v[240:241] op_sel_hi:[1,0,0]\n\t"
      "v_pk_fma_f32 v[38:39], v[38:39], s[26:27], v[240:241] op_sel_hi:[1,0,0]\n\t"
      "ds_read_b64_tr_b16 a[50:51], v232 offset:40960\n\t"
      "ds_read_b64_tr_b16 a[52:53], v233 offset:8192\n\t"
      "v_cvt_pk_bf16_f32 v8, v8, v9\n\t"
      "v_cvt_pk_bf16_f32 v9, v10, v11\n\t"
      "v_cvt_pk_bf16_f32 v10, v20, v21\n\t"
      "v_cvt_pk_bf16_f32 v11, v22, v23\n\t"
      "v_exp_f32 v24, v24\n\t"
      "v_exp_f32 v25, v25\n\t"
      "v_exp_f32 v26, v26\n\t"
      "v_exp_f32 v27, v27\n\t"
      "v_exp_f32 v36, v36\n\t"
      "v_exp_f32 v37, v37\n\t"
      "v_exp_f32 v38, v38\n\t"
      "v_exp_f32 v39, v39\n\t"
      "v_pk_fma_f32 v[28:29], v[28:29], s[26:27], v[242:243] op_sel_hi:[1,0,0]\n\t"
      "v_pk_fma_f32 v[30:31], v[30:31], s[26:27], v[242:243] op_sel_hi:[1,0,0]\n\t"
      "v_pk_fma_f32 v[40:41], v[40:41], s[26:27], v[242:243] op_sel_hi:[1,0,0]\n\t"
      "v_pk_fma_f32 v[42:43], v[42:43], s[26:27], v[242:243] op_sel_hi:[1,0,0]\n\t"
      "ds_read_b64_tr_b16 a[54:55], v233 offset:40960\n\t"
      "ds_read_b64_tr_b16 a[56:57], v234 offset:8192\n\t"
      "v_cvt_pk_bf16_f32 v24, v24, v25\n\t"
      "v_cvt_pk_bf16_f32 v25, v26, v27\n\t"
      "v_cvt_pk_bf16_f32 v26, v36, v37\n\t"
      "v_cvt_pk_bf16_f32 v27, v38, v39\n\t"
      "v_exp_f32 v28, v28\n\t"
      "v_exp_f32 v29, v29\n\t"
      "v_exp_f32 v30, v30\n\t"
      "v_exp_f32 v31, v31\n\t"
      "v_exp_f32 v40, v40\n\t"
      "v_exp_f32 v41, v41\n\t"
      "v_exp_f32 v42, v42\n\t"
      "v_exp_f32 v43, v43\n\t"
      "v_pk_fma_f32 v[32:33], v[32:33], s[26:27], v[244:245] op_sel_hi:[1,0,0]\n\t"
      "v_pk_fma_f32 v[34:35], v[34:35], s[26:27], v[244:245] op_sel_hi:[1,0,0]\n\t"
      "v_pk_fma_f32 v[44:45], v[44:45], s[26:27], v[244:245] op_sel_hi:[1,0,0]\n\t"
      "v_pk_fma_f32 v[46:47], v[46:47], s[26:27], v[244:245] op_sel_hi:[1,0,0]\n\t"
      "ds_read_b64_tr_b16 a[58:59], v234 offset:40960\n\t"
      "ds_read_b64_tr_b16 a[60:61], v235 offset:8192\n\t"
      "v_cvt_pk_bf16_f32 v28, v28, v29\n\t"
      "v_cvt_pk_bf16_f32 v29, v30, v31\n\t"
      "v_cvt_pk_bf16_f32 v30, v40, v41\n\t"
      "v_cvt_pk_bf16_f32 v31, v42, v43\n\t"
      "v_exp_f32 v32, v32\n\t"
      "v_exp_f32 v33, v33\n\t"
      "v_exp_f32 v34, v34\n\t"
      "v_exp_f32 v35, v35\n\t"
      "v_exp_f32 v44, v44\n\t"
      "v_exp_f32 v45, v45\n\t"
      "v_exp_f32 v46, v46\n\t"
      "v_exp_f32 v47, v47\n\t"
      "v_pk_fma_f32 v[48:49], v[48:49], s[26:27], v[240:241] op_sel_hi:[1,0,0]\n\t"
      "v_pk_fma_f32 v[50:51], v[50:51], s[26:27], v[240:241] op_sel_hi:[1,0,0]\n\t"
      "v_pk_fma_f32 v[60:61], v[60:61], s[26:27], v[240:241] op_sel_hi:[1,0,0]\n\t"
      "v_pk_fma_f32 v[62:63], v[62:63], s[26:27], v[240:241] op_sel_hi:[1,0,0]\n\t"
      "ds_read_b64_tr_b16 a[62:63], v235 offset:40960\n\t"
      "s_waitcnt lgkmcnt(0)\n\t"
      "s_barrier\n\t"
      "v_cvt_pk_bf16_f32 v32, v32, v33\n\t"
      "v_cvt_pk_bf16_f32 v33, v34, v35\n\t"
      "v_cvt_pk_bf16_f32 v34, v44, v45\n\t"
      "v_cvt_pk_bf16_f32 v35, v46, v47\n\t"
      "v_exp_f32 v48, v48\n\t"
      "v_exp_f32 v49, v49\n\t"
      "v_exp_f32 v50, v50\n\t"
      "v_exp_f32 v51, v51\n\t"
      "v_exp_f32 v60, v60\n\t"
      "v_exp_f32 v61, v61\n\t"
      "v_exp_f32 v62, v62\n\t"
      "v_exp_f32 v63, v63\n\t"
      "v_pk_fma_f32 v[52:53], v[52:53], s[26:27], v[242:243] op_sel_hi:[1,0,0]\n\t"
      "v_pk_fma_f32 v[54:55], v[54:55], s[26:27], v[242:243] op_sel_hi:[1,0,0]\n\t"
      "v_pk_fma_f32 v[64:65], v[64:65], s[26:27], v[242:243] op_sel_hi:[1,0,0]\n\t"
      "v_pk_fma_f32 v[66:67], v[66:67], s[26:27], v[242:243] op_sel_hi:[1,0,0]\n\t"
      "s_mov_b32 m0, s92\n\t"
      "s_nop 0\n\t"
      "buffer_load_dwordx4 v204, s[32:35], s56 offen nt lds\n\t"
      "v_cvt_pk_bf16_f32 v48, v48, v49\n\t"
      "v_cvt_pk_bf16_f32 v49, v50, v51\n\t"
      "v_cvt_pk_bf16_f32 v50, v60, v61\n\t"
      "v_cvt_pk_bf16_f32 v51, v62, v63\n\t"
      "v_exp_f32 v52, v52\n\t"
      "v_exp_f32 v53, v53\n\t"
      "v_exp_f32 v54, v54\n\t"
      "v_exp_f32 v55, v55\n\t"
      "v_exp_f32 v64, v64\n\t"
      "v_exp_f32 v65, v65\n\t"
      "v_exp_f32 v66, v66\n\t"
      "v_exp_f32 v67, v67\n\t"
      "v_pk_fma_f32 v[56:57], v[56:57], s[26:27], v[244:245] op_sel_hi:[1,0,0]\n\t"
      "v_pk_fma_f32 v[58:59], v[58:59], s[26:27], v[244:245] op_sel_hi:[1,0,0]\n\t"
      "v_pk_fma_f32 v[68:69], v[68:69], s[26:27], v[244:245] op_sel_hi:[1,0,0]\n\t"
      "v_pk_fma_f32 v[70:71], v[70:71], s[26:27], v[244:245] op_sel_hi:[1,0,0]\n\t"
      "buffer_load_dwordx4 v205, s[32:35], s56 offen offset:1024 nt lds\n\t"
      "buffer_load_dwordx4 v206, s[32:35], s56 offen offset:2048 nt lds\n\t"
      "v_cvt_pk_bf16_f32 v52, v52, v53\n\t"
      "v_cvt_pk_bf16_f32 v53, v54, v55\n\t"
      "v_cvt_pk_bf16_f32 v54, v64, v65\n\t"
      "v_cvt_pk_bf16_f32 v55, v66, v67\n\t"
      "v_exp_f32 v56, v56\n\t"
      "v_exp_f32 v57, v57\n\t"
      "v_exp_f32 v58, v58\n\t"
      "v_exp_f32 v59, v59\n\t"
      "v_exp_f32 v68, v68\n\t"
      "v_exp_f32 v69, v69\n\t"
      "v_exp_f32 v70, v70\n\t"
      "v_exp_f32 v71, v71\n\t"
      "v_pk_fma_f32 v[72:73], v[72:73], s[26:27], v[240:241] op_sel_hi:[1,0,0]\n\t"
      "v_pk_fma_f32 v[74:75], v[74:75], s[26:27], v[240:241] op_sel_hi:[1,0,0]\n\t"
      "v_pk_fma_f32 v[84:85], v[84:85], s[26:27], v[240:241] op_sel_hi:[1,0,0]\n\t"
      "v_pk_fma_f32 v[86:87], v[86:87], s[26:27], v[240:241] op_sel_hi:[1,0,0]\n\t"
      "buffer_load_dwordx4 v207, s[32:35], s56 offen offset:3072 nt lds\n\t"
      "v_cvt_pk_bf16_f32 v56, v56, v57\n\t"
      "v_cvt_pk_bf16_f32 v57, v58, v59\n\t"
      "v_cvt_pk_bf16_f32 v58, v68, v69\n\t"
      "v_cvt_pk_bf16_f32 v59, v70, v71\n\t"
      "v_exp_f32 v72, v72\n\t"
      "v_exp_f32 v73, v73\n\t"
      "v_exp_f32 v74, v74\n\t"
      "v_exp_f32 v75, v75\n\t"
      "v_exp_f32 v84, v84\n\t"
      "v_exp_f32 v85, v85\n\t"
      "v_exp_f32 v86, v86\n\t"
      "v_exp_f32 v87, v87\n\t"
      "v_pk_fma_f32 v[76:77], v[76:77], s[26:27], v[242:243] op_sel_hi:[1,0,0]\n\t"
      "v_pk_fma_f32 v[78:79], v[78:79], s[26:27], v[242:243] op_sel_hi:[1,0,0]\n\t"
      "v_pk_fma_f32 v[88:89], v[88:89], s[26:27], v[242:243] op_sel_hi:[1,0,0]\n\t"
      "v_pk_fma_f32 v[90:91], v[90:91], s[26:27], v[242:243] op_sel_hi:[1,0,0]\n\t"
      "v_cvt_pk_bf16_f32 v72, v72, v73\n\t"
      "v_cvt_pk_bf16_f32 v73, v74, v75\n\t"
      "v_cvt_pk_bf16_f32 v74, v84, v85\n\t"
      "v_cvt_pk_bf16_f32 v75, v86, v87\n\t"
      "v_exp_f32 v76, v76\n\t"
      "v_exp_f32 v77, v77\n\t"
      "v_exp_f32 v78, v78\n\t"
      "v_exp_f32 v79, v79\n\t"
      "v_exp_f32 v88, v88\n\t"
      "v_exp_f32 v89, v89\n\t"
      "v_exp_f32 v90, v90\n\t"
      "v_exp_f32 v91, v91\n\t"
      "v_pk_fma_f32 v[80:81], v[80:81], s[26:27], v[244:245] op_sel_hi:[1,0,0]\n\t"
      "v_pk_fma_f32 v[82:83], v[82:83], s[26:27], v[244:245] op_sel_hi:[1,0,0]\n\t"
      "v_pk_fma_f32 v[92:93], v[92:93], s[26:27], v[244:245] op_sel_hi:[1,0,0]\n\t"
      "v_pk_fma_f32 v[94:95], v[94:95], s[26:27], v[244:245] op_sel_hi:[1,0,0]\n\t"
      "v_cvt_pk_bf16_f32 v76, v76, v77\n\t"
      "v_cvt_pk_bf16_f32 v77, v78, v79\n\t"
      "v_cvt_pk_bf16_f32 v78, v88, v89\n\t"
      "v_cvt_pk_bf16_f32 v79, v90, v91\n\t"
      "v_exp_f32 v80, v80\n\t"
      "v_exp_f32 v81, v81\n\t"
      "v_exp_f32 v82, v82\n\t"
      "v_exp_f32 v83, v83\n\t"
      "v_exp_f32 v92, v92\n\t"
      "v_exp_f32 v93, v93\n\t"
      "v_exp_f32 v94, v94\n\t"
      "v_exp_f32 v95, v95\n\t"
      "v_cvt_pk_bf16_f32 v80, v80, v81\n\t"
      "v_cvt_pk_bf16_f32 v81, v82, v83\n\t"
      "v_cvt_pk_bf16_f32 v82, v92, v93\n\t"
      "v_cvt_pk_bf16_f32 v83, v94, v95\n\t"
      "v_mfma_f32_16x16x32_bf16 v[192:195], a[208:211], v[0:3], 0\n\t"
      "v_mfma_f32_16x16x32_bf16 v[196:199], a[208:211], v[4:7], 0\n\t"
      "v_mfma_f32_16x16x32_bf16 v[200:203], a[208:211], v[8:11], 0\n\t"
      "v_mfma_f32_16x16x32_bf16 v[96:99], a[0:3], v[0:3], 0\n\t"
      "v_mfma_f32_16x16x32_bf16 v[100:103], a[0:3], v[4:7], 0\n\t"
      "v_mfma_f32_16x16x32_bf16 v[104:107], a[0:3], v[8:11], 0\n\t"
      "v_mfma_f32_16x16x32_bf16 v[108:111], a[4:7], v[0:3], 0\n\t"
      "v_mfma_f32_16x16x32_bf16 v[112:115], a[4:7], v[4:7], 0\n\t"
      "v_mfma_f32_16x16x32_bf16 v[116:119], a[4:7], v[8:11], 0\n\t"
      "v_mfma_f32_16x16x32_bf16 v[120:123], a[8:11], v[0:3], 0\n\t"
      "v_mfma_f32_16x16x32_bf16 v[124:127], a[8:11], v[4:7], 0\n\t"
      "v_mfma_f32_16x16x32_bf16 v[128:131], a[8:11], v[8:11], 0\n\t"
      "v_mfma_f32_16x16x32_bf16 v[132:135], a[12:15], v[0:3], 0\n\t"
      "v_mfma_f32_16x16x32_bf16 v[136:139], a[12:15], v[4:7], 0\n\t"
      "v_mfma_f32_16x16x32_bf16 v[140:143], a[12:15], v[8:11], 0\n\t"
      "v_mfma_f32_16x16x32_bf16 v[144:147], a[16:19], v[0:3], 0\n\t"
      "v_mfma_f32_16x16x32_bf16 v[148:151], a[16:19], v[4:7], 0\n\t"
      "v_mfma_f32_16x16x32_bf16 v[152:155], a[16:19], v[8:11], 0\n\t"
      "v_mfma_f32_16x16x32_bf16 v[156:159], a[20:23], v[0:3], 0\n\t"
      "v_mfma_f32_16x16x32_bf16 v[160:163], a[20:23], v[4:7], 0\n\t"
      "v_mfma_f32_16x16x32_bf16 v[164:167], a[20:23], v[8:11], 0\n\t"
      "v_mfma_f32_16x16x32_bf16 v[168:171], a[24:27], v[0:3], 0\n\t"
      "v_mfma_f32_16x16x32_bf16 v[172:175], a[24:27], v[4:7], 0\n\t"
      "v_mfma_f32_16x16x32_bf16 v[176:179], a[24:27], v[8:11], 0\n\t"
      "v_mfma_f32_16x16x32_bf16 v[180:183], a[28:31], v[0:3], 0\n\t"
      "v_mfma_f32_16x16x32_bf16 v[184:187], a[28:31], v[4:7], 0\n\t"
      "v_mfma_f32_16x16x32_bf16 v[188:191], a[28:31], v[8:11], 0\n\t"
      "s_lshl_b32 s19, s9, 3\n\t"
      "s_add_u32 s19, s19, s10\n\t"
      "s_xor_b32 s19, s19, 0\n\t"
      "s_lshl_b32 s19, s19, 2\n\t"
      "s_add_u32 s19, s19, s7\n\t"
      "s_add_u32 s19, s19, 2\n\t"
      "s_lshl_b32 s19, s19, 17\n\t"
      "s_add_u32 s40, s20, s19\n\t"
      "s_addc_u32 s41, s21, 0\n\t"
      "s_sub_u32 s42, 0x4000000, s19\n\t"
      "s_mov_b32 s43, 0x20000\n\t"
      "s_lshl_b32 s23, s7, 1\n\t"
      "s_add_u32 s23, s23, s9\n\t"
      "s_add_u32 s23, s23, s8\n\t"
      "v_lshlrev_b32 v212, 3, v216\n\t"
      "s_add_u32 s19, s23, 0\n\t"
      "s_and_b32 s19, s19, 15\n\t"
      "s_lshl_b32 s19, s19, 13\n\t"
      "s_mov_b32 m0, 0x27000\n\t"
      "s_nop 0\n\t"
      "buffer_load_dword v212, s[40:43], s19 offen lds\n\t"
      "v_mfma_f32_16x16x32_bf16 v[192:195], a[208:211], v[24:27], v[192:195]\n\t"
      "ds_read_b64_tr_b16 a[0:1], v220 offset:12288\n\t"
      "v_mfma_f32_16x16x32_bf16 v[196:199], a[208:211], v[28:31], v[196:199]\n\t"
      "ds_read_b64_tr_b16 a[2:3], v220 offset:45056\n\t"
      "v_mfma_f32_16x16x32_bf16 v[200:203], a[208:211], v[32:35], v[200:203]\n\t"
      "ds_read_b64_tr_b16 a[4:5], v221 offset:12288\n\t"
      "v_mfma_f32_16x16x32_bf16 v[96:99], a[32:35], v[24:27], v[96:99]\n\t"
      "v_mfma_f32_16x16x32_bf16 v[100:103], a[32:35], v[28:31], v[100:103]\n\t"
      "ds_read_b64_tr_b16 a[6:7], v221 offset:45056\n\t"
      "v_mfma_f32_16x16x32_bf16 v[104:107], a[32:35], v[32:35], v[104:107]\n\t"
      "ds_read_b64_tr_b16 a[8:9], v222 offset:12288\n\t"
      "v_mfma_f32_16x16x32_bf16 v[108:111], a[36:39], v[24:27], v[108:111]\n\t"
      "v_mfma_f32_16x16x32_bf16 v[112:115], a[36:39], v[28:31], v[112:115]\n\t"
      "ds_read_b64_tr_b16 a[10:11], v222 offset:45056\n\t"
      "v_mfma_f32_16x16x32_bf16 v[116:119], a[36:39], v[32:35], v[116:119]\n\t"
      "ds_read_b64_tr_b16 a[12:13], v223 offset:12288\n\t"
      "v_mfma_f32_16x16x32_bf16 v[120:123], a[40:43], v[24:27], v[120:123]\n\t"
      "v_mfma_f32_16x16x32_bf16 v[124:127], a[40:43], v[28:31], v[124:127]\n\t"
      "ds_read_b64_tr_b16 a[14:15], v223 offset:45056\n\t"
      "v_mfma_f32_16x16x32_bf16 v[128:131], a[40:43], v[32:35], v[128:131]\n\t"
      "ds_read_b64_tr_b16 a[16:17], v224 offset:12288\n\t"
      "v_mfma_f32_16x16x32_bf16 v[132:135], a[44:47], v[24:27], v[132:135]\n\t"
      "ds_read_b64_tr_b16 a[18:19], v224 offset:45056\n\t"
      "v_mfma_f32_16x16x32_bf16 v[136:139], a[44:47], v[28:31], v[136:139]\n\t"
      "v_mfma_f32_16x16x32_bf16 v[140:143], a[44:47], v[32:35], v[140:143]\n\t"
      "ds_read_b64_tr_b16 a[20:21], v225 offset:12288\n\t"
      "v_mfma_f32_16x16x32_bf16 v[144:147], a[48:51], v[24:27], v[144:147]\n\t"
      "ds_read_b64_tr_b16 a[22:23], v225 offset:45056\n\t"
      "v_mfma_f32_16x16x32_bf16 v[148:151], a[48:51], v[28:31], v[148:151]\n\t"
      "v_mfma_f32_16x16x32_bf16 v[152:155], a[48:51], v[32:35], v[152:155]\n\t"
      "ds_read_b64_tr_b16 a[24:25], v226 offset:12288\n\t"
      "v_mfma_f32_16x16x32_bf16 v[156:159], a[52:55], v[24:27], v[156:159]\n\t"
      "ds_read_b64_tr_b16 a[26:27], v226 offset:45056\n\t"
      "v_mfma_f32_16x16x32_bf16 v[160:163], a[52:55], v[28:31], v[160:163]\n\t"
      "v_mfma_f32_16x16x32_bf16 v[164:167], a[52:55], v[32:35], v[164:167]\n\t"
      "ds_read_b64_tr_b16 a[28:29], v227 offset:12288\n\t"
      "v_mfma_f32_16x16x32_bf16 v[168:171], a[56:59], v[24:27], v[168:171]\n\t"
      "ds_read_b64_tr_b16 a[30:31], v227 offset:45056\n\t"
      "v_mfma_f32_16x16x32_bf16 v[172:175], a[56:59], v[28:31], v[172:175]\n\t"
      "v_mfma_f32_16x16x32_bf16 v[176:179], a[56:59], v[32:35], v[176:179]\n\t"
      "v_mfma_f32_16x16x32_bf16 v[180:183], a[60:63], v[24:27], v[180:183]\n\t"
      "v_mfma_f32_16x16x32_bf16 v[184:187], a[60:63], v[28:31], v[184:187]\n\t"
      "v_mfma_f32_16x16x32_bf16 v[188:191], a[60:63], v[32:35], v[188:191]\n\t"
      "s_waitcnt lgkmcnt(0)\n\t"
      "v_mfma_f32_16x16x32_bf16 v[192:195], a[208:211], v[48:51], v[192:195]\n\t"
      "ds_read_b64_tr_b16 a[32:33], v228 offset:12288\n\t"
      "v_mfma_f32_16x16x32_bf16 v[196:199], a[208:211], v[52:55], v[196:199]\n\t"
      "ds_read_b64_tr_b16 a[34:35], v228 offset:45056\n\t"
      "v_mfma_f32_16x16x32_bf16 v[200:203], a[208:211], v[56:59], v[200:203]\n\t"
      "ds_read_b64_tr_b16 a[36:37], v229 offset:12288\n\t"
      "v_mfma_f32_16x16x32_bf16 v[96:99], a[0:3], v[48:51], v[96:99]\n\t"
      "v_mfma_f32_16x16x32_bf16 v[100:103], a[0:3], v[52:55], v[100:103]\n\t"
      "ds_read_b64_tr_b16 a[38:39], v229 offset:45056\n\t"
      "v_mfma_f32_16x16x32_bf16 v[104:107], a[0:3], v[56:59], v[104:107]\n\t"
      "ds_read_b64_tr_b16 a[40:41], v230 offset:12288\n\t"
      "v_mfma_f32_16x16x32_bf16 v[108:111], a[4:7], v[48:51], v[108:111]\n\t"
      "v_mfma_f32_16x16x32_bf16 v[112:115], a[4:7], v[52:55], v[112:115]\n\t"
      "ds_read_b64_tr_b16 a[42:43], v230 offset:45056\n\t"
      "v_mfma_f32_16x16x32_bf16 v[116:119], a[4:7], v[56:59], v[116:119]\n\t"
      "ds_read_b64_tr_b16 a[44:45], v231 offset:12288\n\t"
      "v_mfma_f32_16x16x32_bf16 v[120:123], a[8:11], v[48:51], v[120:123]\n\t"
      "v_mfma_f32_16x16x32_bf16 v[124:127], a[8:11], v[52:55], v[124:127]\n\t"
      "ds_read_b64_tr_b16 a[46:47], v231 offset:45056\n\t"
      "v_mfma_f32_16x16x32_bf16 v[128:131], a[8:11], v[56:59], v[128:131]\n\t"
      "ds_read_b64_tr_b16 a[48:49], v232 offset:12288\n\t"
      "v_mfma_f32_16x16x32_bf16 v[132:135], a[12:15], v[48:51], v[132:135]\n\t"
      "ds_read_b64_tr_b16 a[50:51], v232 offset:45056\n\t"
      "v_mfma_f32_16x16x32_bf16 v[136:139], a[12:15], v[52:55], v[136:139]\n\t"
      "v_mfma_f32_16x16x32_bf16 v[140:143], a[12:15], v[56:59], v[140:143]\n\t"
      "ds_read_b64_tr_b16 a[52:53], v233 offset:12288\n\t"
      "v_mfma_f32_16x16x32_bf16 v[144:147], a[16:19], v[48:51], v[144:147]\n\t"
      "ds_read_b64_tr_b16 a[54:55], v233 offset:45056\n\t"
      "v_mfma_f32_16x16x32_bf16 v[148:151], a[16:19], v[52:55], v[148:151]\n\t"
      "v_mfma_f32_16x16x32_bf16 v[152:155], a[16:19], v[56:59], v[152:155]\n\t"
      "ds_read_b64_tr_b16 a[56:57], v234 offset:12288\n\t"
      "v_mfma_f32_16x16x32_bf16 v[156:159], a[20:23], v[48:51], v[156:159]\n\t"
      "ds_read_b64_tr_b16 a[58:59], v234 offset:45056\n\t"
      "v_mfma_f32_16x16x32_bf16 v[160:163], a[20:23], v[52:55], v[160:163]\n\t"
      "v_mfma_f32_16x16x32_bf16 v[164:167], a[20:23], v[56:59], v[164:167]\n\t"
      "ds_read_b64_tr_b16 a[60:61], v235 offset:12288\n\t"
      "v_mfma_f32_16x16x32_bf16 v[168:171], a[24:27], v[48:51], v[168:171]\n\t"
      "ds_read_b64_tr_b16 a[62:63], v235 offset:45056\n\t"
      "v_mfma_f32_16x16x32_bf16 v[172:175], a[24:27], v[52:55], v[172:175]\n\t"
      "v_mfma_f32_16x16x32_bf16 v[176:179], a[24:27], v[56:59], v[176:179]\n\t"
      "v_mfma_f32_16x16x32_bf16 v[180:183], a[28:31], v[48:51], v[180:183]\n\t"
      "v_mfma_f32_16x16x32_bf16 v[184:187], a[28:31], v[52:55], v[184:187]\n\t"
      "v_mfma_f32_16x16x32_bf16 v[188:191], a[28:31], v[56:59], v[188:191]\n\t"
      "s_add_u32 s19, s23, 2\n\t"
      "s_and_b32 s19, s19, 15\n\t"
      "s_lshl_b32 s19, s19, 13\n\t"
      "s_mov_b32 m0, 0x27000\n\t"
      "s_nop 0\n\t"
      "buffer_load_dword v212, s[40:43], s19 offen lds\n\t"
      "s_waitcnt lgkmcnt(0)\n\t"
      "s_barrier\n\t"
      "s_mov_b32 m0, s93\n\t"
      "v_mfma_f32_16x16x32_bf16 v[192:195], a[208:211], v[72:75], v[192:195]\n\t"
      "buffer_load_dwordx4 v204, s[32:35], s57 offen nt lds\n\t"
      "v_xor_b32 v220, 0, v217\n\t"
      "v_xor_b32 v221, 64, v217\n\t"
      "v_mfma_f32_16x16x32_bf16 v[196:199], a[208:211], v[76:79], v[196:199]\n\t"
      "v_xor_b32 v222, 128, v217\n\t"
      "v_mfma_f32_16x16x32_bf16 v[200:203], a[208:211], v[80:83], v[200:203]\n\t"
      "v_xor_b32 v223, 192, v217\n\t"
      "v_mfma_f32_16x16x32_bf16 v[96:99], a[32:35], v[72:75], v[96:99]\n\t"
      "v_xor_b32 v224, 0, v217\n\t"
      "v_add_u32 v224, 0x10000, v224\n\t"
      "v_mfma_f32_16x16x32_bf16 v[100:103], a[32:35], v[76:79], v[100:103]\n\t"
      "buffer_load_dwordx4 v205, s[32:35], s57 offen offset:1024 nt lds\n\t"
      "v_xor_b32 v225, 64, v217\n\t"
      "v_add_u32 v225, 0x10000, v225\n\t"
      "v_mfma_f32_16x16x32_bf16 v[104:107], a[32:35], v[80:83], v[104:107]\n\t"
      "v_xor_b32 v226, 128, v217\n\t"
      "v_add_u32 v226, 0x10000, v226\n\t"
      "v_mfma_f32_16x16x32_bf16 v[108:111], a[36:39], v[72:75], v[108:111]\n\t"
      "v_xor_b32 v227, 192, v217\n\t"
      "v_add_u32 v227, 0x10000, v227\n\t"
      "v_mfma_f32_16x16x32_bf16 v[112:115], a[36:39], v[76:79], v[112:115]\n\t"
      "ds_read_b128 a[0:3], v220 offset:16384\n\t"
      "v_mfma_f32_16x16x32_bf16 v[116:119], a[36:39], v[80:83], v[116:119]\n\t"
      "v_mfma_f32_16x16x32_bf16 v[120:123], a[40:43], v[72:75], v[120:123]\n\t"
      "ds_read_b128 a[4:7], v221 offset:16384\n\t"
      "v_mfma_f32_16x16x32_bf16 v[124:127], a[40:43], v[76:79], v[124:127]\n\t"
      "v_mfma_f32_16x16x32_bf16 v[128:131], a[40:43], v[80:83], v[128:131]\n\t"
      "ds_read_b128 a[8:11], v222 offset:16384\n\t"
      "v_mfma_f32_16x16x32_bf16 v[132:135], a[44:47], v[72:75], v[132:135]\n\t"
      "buffer_load_dwordx4 v206, s[32:35], s57 offen offset:2048 nt lds\n\t"
      "v_mfma_f32_16x16x32_bf16 v[136:139], a[44:47], v[76:79], v[136:139]\n\t"
      "ds_read_b128 a[12:15], v223 offset:16384\n\t"
      "v_mfma_f32_16x16x32_bf16 v[140:143], a[44:47], v[80:83], v[140:143]\n\t"
      "v_mfma_f32_16x16x32_bf16 v[144:147], a[48:51], v[72:75], v[144:147]\n\t"
      "ds_read_b128 a[16:19], v220 offset:49152\n\t"
      "v_mfma_f32_16x16x32_bf16 v[148:151], a[48:51], v[76:79], v[148:151]\n\t"
      "v_mfma_f32_16x16x32_bf16 v[152:155], a[48:51], v[80:83], v[152:155]\n\t"
      "ds_read_b128 a[20:23], v221 offset:49152\n\t"
      "v_mfma_f32_16x16x32_bf16 v[156:159], a[52:55], v[72:75], v[156:159]\n\t"
      "v_mfma_f32_16x16x32_bf16 v[160:163], a[52:55], v[76:79], v[160:163]\n\t"
      "ds_read_b128 a[24:27], v222 offset:49152\n\t"
      "v_mfma_f32_16x16x32_bf16 v[164:167], a[52:55], v[80:83], v[164:167]\n\t"
      "v_mfma_f32_16x16x32_bf16 v[168:171], a[56:59], v[72:75], v[168:171]\n\t"
      "ds_read_b128 a[28:31], v223 offset:49152\n\t"
      "v_mfma_f32_16x16x32_bf16 v[172:175], a[56:59], v[76:79], v[172:175]\n\t"
      "buffer_load_dwordx4 v207, s[32:35], s57 offen offset:3072 nt lds\n\t"
      "v_mfma_f32_16x16x32_bf16 v[176:179], a[56:59], v[80:83], v[176:179]\n\t"
      "v_mfma_f32_16x16x32_bf16 v[180:183], a[60:63], v[72:75], v[180:183]\n\t"
      "v_mfma_f32_16x16x32_bf16 v[184:187], a[60:63], v[76:79], v[184:187]\n\t"
      "v_mfma_f32_16x16x32_bf16 v[188:191], a[60:63], v[80:83], v[188:191]\n\t"
      "s_waitcnt lgkmcnt(0)\n\t"
      "v_mfma_f32_16x16x32_bf16 v[0:3], a[0:3], a[64:67], 0\n\t"
      "ds_read_b128 a[32:35], v224 offset:16384\n\t"
      "v_mfma_f32_16x16x32_bf16 v[0:3], a[4:7], a[76:79], v[0:3]\n\t"
      "v_mfma_f32_16x16x32_bf16 v[0:3], a[8:11], a[88:91], v[0:3]\n\t"
      "ds_read_b128 a[36:39], v225 offset:16384\n\t"
      "v_mov_b32 v241, v236\n\t"
      "v_mfma_f32_16x16x32_bf16 v[0:3], a[12:15], a[100:103], v[0:3]\n\t"
      "v_mov_b32 v243, v237\n\t"
      "v_mfma_f32_16x16x32_bf16 v[4:7], a[0:3], a[68:71], 0\n\t"
      "ds_read_b128 a[40:43], v226 offset:16384\n\t"
      "v_mov_b32 v249, v238\n\t"
      "v_mfma_f32_16x16x32_bf16 v[4:7], a[4:7], a[80:83], v[4:7]\n\t"
      "v_mfma_f32_16x16x32_bf16 v[4:7], a[8:11], a[92:95], v[4:7]\n\t"
      "ds_read_b128 a[44:47], v227 offset:16384\n\t"
      "v_mfma_f32_16x16x32_bf16 v[4:7], a[12:15], a[104:107], v[4:7]\n\t"
      "v_mfma_f32_16x16x32_bf16 v[8:11], a[0:3], a[72:75], 0\n\t"
      "ds_read_b128 a[48:51], v224 offset:49152\n\t"
      "v_mfma_f32_16x16x32_bf16 v[8:11], a[4:7], a[84:87], v[8:11]\n\t"
      "v_mfma_f32_16x16x32_bf16 v[8:11], a[8:11], a[96:99], v[8:11]\n\t"
      "ds_read_b128 a[52:55], v225 offset:49152\n\t"
      "v_mfma_f32_16x16x32_bf16 v[8:11], a[12:15], a[108:111], v[8:11]\n\t"
      "v_mfma_f32_16x16x32_bf16 v[12:15], a[16:19], a[64:67], 0\n\t"
      "ds_read_b128 a[56:59], v226 offset:49152\n\t"
      "v_mfma_f32_16x16x32_bf16 v[12:15], a[20:23], a[76:79], v[12:15]\n\t"
      "v_mfma_f32_16x16x32_bf16 v[12:15], a[24:27], a[88:91], v[12:15]\n\t"
      "ds_read_b128 a[60:63], v227 offset:49152\n\t"
      "v_mfma_f32_16x16x32_bf16 v[12:15], a[28:31], a[100:103], v[12:15]\n\t"
      "v_mfma_f32_16x16x32_bf16 v[16:19], a[16:19], a[68:71], 0\n\t"
      "v_mfma_f32_16x16x32_bf16 v[16:19], a[20:23], a[80:83], v[16:19]\n\t"
      "v_mfma_f32_16x16x32_bf16 v[16:19], a[24:27], a[92:95], v[16:19]\n\t"
      "v_mfma_f32_16x16x32_bf16 v[16:19], a[28:31], a[104:107], v[16:19]\n\t"
      "v_mfma_f32_16x16x32_bf16 v[20:23], a[16:19], a[72:75], 0\n\t"
      "v_mfma_f32_16x16x32_bf16 v[20:23], a[20:23], a[84:87], v[20:23]\n\t"
      "v_mfma_f32_16x16x32_bf16 v[20:23], a[24:27], a[96:99], v[20:23]\n\t"
      "v_mfma_f32_16x16x32_bf16 v[20:23], a[28:31], a[108:111], v[20:23]\n\t"
      "s_add_u32 s19, s23, 4\n\t"
      "s_and_b32 s19, s19, 15\n\t"
      "s_lshl_b32 s19, s19, 13\n\t"
      "s_mov_b32 m0, 0x27000\n\t"
      "s_nop 0\n\t"
      "buffer_load_dword v212, s[40:43], s19 offen lds\n\t"
      "s_waitcnt lgkmcnt(0)\n\t"
      "s_barrier\n\t"
      "v_mfma_f32_16x16x32_bf16 v[24:27], a[32:35], a[64:67], 0\n\t"
      "ds_read_b128 a[0:3], v220 offset:20480\n\t"
      "s_mov_b32 m0, s94\n\t"
      "v_mfma_f32_16x16x32_bf16 v[24:27], a[36:39], a[76:79], v[24:27]\n\t"
      "buffer_load_dwordx4 v204, s[28:31], s58 offen nt lds\n\t"
      "v_mfma_f32_16x16x32_bf16 v[24:27], a[40:43], a[88:91], v[24:27]\n\t"
      "ds_read_b128 a[4:7], v221 offset:20480\n\t"
      "v_mfma_f32_16x16x32_bf16 v[24:27], a[44:47], a[100:103], v[24:27]\n\t"
      "v_mfma_f32_16x16x32_bf16 v[28:31], a[32:35], a[68:71], 0\n\t"
      "ds_read_b128 a[8:11], v222 offset:20480\n\t"
      "v_mfma_f32_16x16x32_bf16 v[28:31], a[36:39], a[80:83], v[28:31]\n\t"
      "v_mfma_f32_16x16x32_bf16 v[28:31], a[40:43], a[92:95], v[28:31]\n\t"
      "ds_read_b128 a[12:15], v223 offset:20480\n\t"
      "v_mfma_f32_16x16x32_bf16 v[28:31], a[44:47], a[104:107], v[28:31]\n\t"
      "v_mfma_f32_16x16x32_bf16 v[32:35], a[32:35], a[72:75], 0\n\t"
      "ds_read_b128 a[16:19], v220 offset:53248\n\t"
      "v_maximum3_f32 v236, v236, v0, v1\n\t"
      "v_mfma_f32_16x16x32_bf16 v[32:35], a[36:39], a[84:87], v[32:35]\n\t"
      "buffer_load_dwordx4 v205, s[28:31], s58 offen offset:1024 nt lds\n\t"
      "v_maximum3_f32 v237, v237, v4, v5\n\t"
      "v_mfma_f32_16x16x32_bf16 v[32:35], a[40:43], a[96:99], v[32:35]\n\t"
      "ds_read_b128 a[20:23], v221 offset:53248\n\t"
      "v_maximum3_f32 v238, v238, v8, v9\n\t"
      "v_mfma_f32_16x16x32_bf16 v[32:35], a[44:47], a[108:111], v[32:35]\n\t"
      "v_mfma_f32_16x16x32_bf16 v[36:39], a[48:51], a[64:67], 0\n\t"
      "ds_read_b128 a[24:27], v222 offset:53248\n\t"
      "v_maximum3_f32 v236, v236, v2, v3\n\t"
      "v_mfma_f32_16x16x32_bf16 v[36:39], a[52:55], a[76:79], v[36:39]\n\t"
      "v_maximum3_f32 v237, v237, v6, v7\n\t"
      "v_mfma_f32_16x16x32_bf16 v[36:39], a[56:59], a[88:91], v[36:39]\n\t"
      "ds_read_b128 a[28:31], v223 offset:53248\n\t"
      "v_maximum3_f32 v238, v238, v10, v11\n\t"
      "v_mfma_f32_16x16x32_bf16 v[36:39], a[60:63], a[100:103], v[36:39]\n\t"
      "v_mfma_f32_16x16x32_bf16 v[40:43], a[48:51], a[68:71], 0\n\t"
      "buffer_load_dwordx4 v206, s[28:31], s58 offen offset:2048 nt lds\n\t"
      "v_maximum3_f32 v236, v236, v12, v13\n\t"
      "v_mfma_f32_16x16x32_bf16 v[40:43], a[52:55], a[80:83], v[40:43]\n\t"
      "v_maximum3_f32 v237, v237, v16, v17\n\t"
      "v_mfma_f32_16x16x32_bf16 v[40:43], a[56:59], a[92:95], v[40:43]\n\t"
      "v_maximum3_f32 v238, v238, v20, v21\n\t"
      "v_mfma_f32_16x16x32_bf16 v[40:43], a[60:63], a[104:107], v[40:43]\n\t"
      "v_mfma_f32_16x16x32_bf16 v[44:47], a[48:51], a[72:75], 0\n\t"
      "buffer_load_dwordx4 v207, s[28:31], s58 offen offset:3072 nt lds\n\t"
      "v_maximum3_f32 v236, v236, v14, v15\n\t"
      "v_mfma_f32_16x16x32_bf16 v[44:47], a[52:55], a[84:87], v[44:47]\n\t"
      "v_maximum3_f32 v237, v237, v18, v19\n\t"
      "v_mfma_f32_16x16x32_bf16 v[44:47], a[56:59], a[96:99], v[44:47]\n\t"
      "v_maximum3_f32 v238, v238, v22, v23\n\t"
      "v_mfma_f32_16x16x32_bf16 v[44:47], a[60:63], a[108:111], v[44:47]\n\t"
      "s_waitcnt lgkmcnt(0)\n\t"
      "v_mfma_f32_16x16x32_bf16 v[48:51], a[0:3], a[64:67], 0\n\t"
      "ds_read_b128 a[32:35], v224 offset:20480\n\t"
      "v_mfma_f32_16x16x32_bf16 v[48:51], a[4:7], a[76:79], v[48:51]\n\t"
      "v_mfma_f32_16x16x32_bf16 v[48:51], a[8:11], a[88:91], v[48:51]\n\t"
      "ds_read_b128 a[36:39], v225 offset:20480\n\t"
      "v_mfma_f32_16x16x32_bf16 v[48:51], a[12:15], a[100:103], v[48:51]\n\t"
      "v_mfma_f32_16x16x32_bf16 v[52:55], a[0:3], a[68:71], 0\n\t"
      "ds_read_b128 a[40:43], v226 offset:20480\n\t"
      "v_mfma_f32_16x16x32_bf16 v[52:55], a[4:7], a[80:83], v[52:55]\n\t"
      "v_mfma_f32_16x16x32_bf16 v[52:55], a[8:11], a[92:95], v[52:55]\n\t"
      "ds_read_b128 a[44:47], v227 offset:20480\n\t"
      "v_mfma_f32_16x16x32_bf16 v[52:55], a[12:15], a[104:107], v[52:55]\n\t"
      "v_mfma_f32_16x16x32_bf16 v[56:59], a[0:3], a[72:75], 0\n\t"
      "ds_read_b128 a[48:51], v224 offset:53248\n\t"
      "v_maximum3_f32 v236, v236, v24, v25\n\t"
      "v_mfma_f32_16x16x32_bf16 v[56:59], a[4:7], a[84:87], v[56:59]\n\t"
      "v_maximum3_f32 v237, v237, v28, v29\n\t"
      "v_mfma_f32_16x16x32_bf16 v[56:59], a[8:11], a[96:99], v[56:59]\n\t"
      "ds_read_b128 a[52:55], v225 offset:53248\n\t"
      "v_maximum3_f32 v238, v238, v32, v33\n\t"
      "v_mfma_f32_16x16x32_bf16 v[56:59], a[12:15], a[108:111], v[56:59]\n\t"
      "v_mfma_f32_16x16x32_bf16 v[60:63], a[16:19], a[64:67], 0\n\t"
      "ds_read_b128 a[56:59], v226 offset:53248\n\t"
      "v_maximum3_f32 v236, v236, v26, v27\n\t"
      "v_mfma_f32_16x16x32_bf16 v[60:63], a[20:23], a[76:79], v[60:63]\n\t"
      "v_maximum3_f32 v237, v237, v30, v31\n\t"
      "v_mfma_f32_16x16x32_bf16 v[60:63], a[24:27], a[88:91], v[60:63]\n\t"
      "ds_read_b128 a[60:63], v227 offset:53248\n\t"
      "v_maximum3_f32 v238, v238, v34, v35\n\t"
      "v_mfma_f32_16x16x32_bf16 v[60:63], a[28:31], a[100:103], v[60:63]\n\t"
      "v_mfma_f32_16x16x32_bf16 v[64:67], a[16:19], a[68:71], 0\n\t"
      "v_maximum3_f32 v236, v236, v36, v37\n\t"
      "v_mfma_f32_16x16x32_bf16 v[64:67], a[20:23], a[80:83], v[64:67]\n\t"
      "v_maximum3_f32 v237, v237, v40, v41\n\t"
      "v_mfma_f32_16x16x32_bf16 v[64:67], a[24:27], a[92:95], v[64:67]\n\t"
      "v_maximum3_f32 v238, v238, v44, v45\n\t"
      "v_mfma_f32_16x16x32_bf16 v[64:67], a[28:31], a[104:107], v[64:67]\n\t"
      "v_mfma_f32_16x16x32_bf16 v[68:71], a[16:19], a[72:75], 0\n\t"
      "v_maximum3_f32 v236, v236, v38, v39\n\t"
      "v_mfma_f32_16x16x32_bf16 v[68:71], a[20:23], a[84:87], v[68:71]\n\t"
      "v_maximum3_f32 v237, v237, v42, v43\n\t"
      "v_mfma_f32_16x16x32_bf16 v[68:71], a[24:27], a[96:99], v[68:71]\n\t"
      "v_maximum3_f32 v238, v238, v46, v47\n\t"
      "v_mfma_f32_16x16x32_bf16 v[68:71], a[28:31], a[108:111], v[68:71]\n\t"
      "s_add_u32 s19, s23, 6\n\t"
      "s_and_b32 s19, s19, 15\n\t"
      "s_lshl_b32 s19, s19, 13\n\t"
      "s_mov_b32 m0, 0x27000\n\t"
      "s_nop 0\n\t"
      "buffer_load_dword v212, s[40:43], s19 offen lds\n\t"
      "s_waitcnt lgkmcnt(0)\n\t"
      "s_barrier\n\t"
      "s_mov_b32 m0, s95\n\t"
      "v_mfma_f32_16x16x32_bf16 v[72:75], a[32:35], a[64:67], 0\n\t"
      "buffer_load_dwordx4 v204, s[28:31], s59 offen nt lds\n\t"
      "v_xor_b32 v220, 0, v218\n\t"
      "v_xor_b32 v221, 32, v218\n\t"
      "v_mfma_f32_16x16x32_bf16 v[72:75], a[36:39], a[76:79], v[72:75]\n\t"
      "v_xor_b32 v222, 64, v218\n\t"
      "v_xor_b32 v223, 96, v218\n\t"
      "v_mfma_f32_16x16x32_bf16 v[72:75], a[40:43], a[88:91], v[72:75]\n\t"
      "buffer_load_dwordx4 v205, s[28:31], s59 offen offset:1024 nt lds\n\t"
      "v_xor_b32 v224, 128, v218\n\t"
      "v_xor_b32 v225, 160, v218\n\t"
      "v_mfma_f32_16x16x32_bf16 v[72:75], a[44:47], a[100:103], v[72:75]\n\t"
      "v_xor_b32 v226, 192, v218\n\t"
      "v_xor_b32 v227, 224, v218\n\t"
      "v_mfma_f32_16x16x32_bf16 v[76:79], a[32:35], a[68:71], 0\n\t"
      "buffer_load_dwordx4 v206, s[28:31], s59 offen offset:2048 nt lds\n\t"
      "v_xor_b32 v228, 0, v218\n\t"
      "v_add_u32 v228, 0x10000, v228\n\t"
      "v_xor_b32 v229, 32, v218\n\t"
      "v_add_u32 v229, 0x10000, v229\n\t"
      "v_mfma_f32_16x16x32_bf16 v[76:79], a[36:39], a[80:83], v[76:79]\n\t"
      "v_xor_b32 v230, 64, v218\n\t"
      "v_add_u32 v230, 0x10000, v230\n\t"
      "v_xor_b32 v231, 96, v218\n\t"
      "v_add_u32 v231, 0x10000, v231\n\t"
      "v_mfma_f32_16x16x32_bf16 v[76:79], a[40:43], a[92:95], v[76:79]\n\t"
      "buffer_load_dwordx4 v207, s[28:31], s59 offen offset:3072 nt lds\n\t"
      "v_xor_b32 v232, 128, v218\n\t"
      "v_add_u32 v232, 0x10000, v232\n\t"
      "v_xor_b32 v233, 160, v218\n\t"
      "v_add_u32 v233, 0x10000, v233\n\t"
      "v_mfma_f32_16x16x32_bf16 v[76:79], a[44:47], a[104:107], v[76:79]\n\t"
      "ds_read_b64_tr_b16 a[0:1], v220 offset:24576\n\t"
      "v_xor_b32 v234, 192, v218\n\t"
      "v_add_u32 v234, 0x10000, v234\n\t"
      "v_xor_b32 v235, 224, v218\n\t"
      "v_add_u32 v235, 0x10000, v235\n\t"
      "v_mfma_f32_16x16x32_bf16 v[80:83], a[32:35], a[72:75], 0\n\t"
      "ds_read_b64_tr_b16 a[2:3], v220 offset:57344\n\t"
      "v_maximum3_f32 v236, v236, v48, v49\n\t"
      "v_mfma_f32_16x16x32_bf16 v[80:83], a[36:39], a[84:87], v[80:83]\n\t"
      "ds_read_b64_tr_b16 a[4:5], v221 offset:24576\n\t"
      "v_maximum3_f32 v237, v237, v52, v53\n\t"
      "v_mfma_f32_16x16x32_bf16 v[80:83], a[40:43], a[96:99], v[80:83]\n\t"
      "ds_read_b64_tr_b16 a[6:7], v221 offset:57344\n\t"
      "v_maximum3_f32 v238, v238, v56, v57\n\t"
      "v_mfma_f32_16x16x32_bf16 v[80:83], a[44:47], a[108:111], v[80:83]\n\t"
      "ds_read_b64_tr_b16 a[8:9], v222 offset:24576\n\t"
      "v_mfma_f32_16x16x32_bf16 v[84:87], a[48:51], a[64:67], 0\n\t"
      "ds_read_b64_tr_b16 a[10:11], v222 offset:57344\n\t"
      "v_maximum3_f32 v236, v236, v50, v51\n\t"
      "v_mfma_f32_16x16x32_bf16 v[84:87], a[52:55], a[76:79], v[84:87]\n\t"
      "ds_read_b64_tr_b16 a[12:13], v223 offset:24576\n\t"
      "v_maximum3_f32 v237, v237, v54, v55\n\t"
      "v_mfma_f32_16x16x32_bf16 v[84:87], a[56:59], a[88:91], v[84:87]\n\t"
      "ds_read_b64_tr_b16 a[14:15], v223 offset:57344\n\t"
      "v_maximum3_f32 v238, v238, v58, v59\n\t"
      "v_mfma_f32_16x16x32_bf16 v[84:87], a[60:63], a[100:103], v[84:87]\n\t"
      "ds_read_b64_tr_b16 a[16:17], v224 offset:24576\n\t"
      "v_mfma_f32_16x16x32_bf16 v[88:91], a[48:51], a[68:71], 0\n\t"
      "ds_read_b64_tr_b16 a[18:19], v224 offset:57344\n\t"
      "v_maximum3_f32 v236, v236, v60, v61\n\t"
      "v_mfma_f32_16x16x32_bf16 v[88:91], a[52:55], a[80:83], v[88:91]\n\t"
      "ds_read_b64_tr_b16 a[20:21], v225 offset:24576\n\t"
      "v_maximum3_f32 v237, v237, v64, v65\n\t"
      "v_mfma_f32_16x16x32_bf16 v[88:91], a[56:59], a[92:95], v[88:91]\n\t"
      "ds_read_b64_tr_b16 a[22:23], v225 offset:57344\n\t"
      "v_maximum3_f32 v238, v238, v68, v69\n\t"
      "v_mfma_f32_16x16x32_bf16 v[88:91], a[60:63], a[104:107], v[88:91]\n\t"
      "ds_read_b64_tr_b16 a[24:25], v226 offset:24576\n\t"
      "v_mfma_f32_16x16x32_bf16 v[92:95], a[48:51], a[72:75], 0\n\t"
      "ds_read_b64_tr_b16 a[26:27], v226 offset:57344\n\t"
      "v_maximum3_f32 v236, v236, v62, v63\n\t"
      "v_mfma_f32_16x16x32_bf16 v[92:95], a[52:55], a[84:87], v[92:95]\n\t"
      "ds_read_b64_tr_b16 a[28:29], v227 offset:24576\n\t"
      "v_maximum3_f32 v237, v237, v66, v67\n\t"
      "v_mfma_f32_16x16x32_bf16 v[92:95], a[56:59], a[96:99], v[92:95]\n\t"
      "ds_read_b64_tr_b16 a[30:31], v227 offset:57344\n\t"
      "v_maximum3_f32 v238, v238, v70, v71\n\t"
      "v_mfma_f32_16x16x32_bf16 v[92:95], a[60:63], a[108:111], v[92:95]\n\t"
      "ds_read_b64_tr_b16 a[32:33], v228 offset:24576\n\t"
      "ds_read_b64_tr_b16 a[34:35], v228 offset:57344\n\t"
      "ds_read_b64_tr_b16 a[36:37], v229 offset:24576\n\t"
      "ds_read_b64_tr_b16 a[38:39], v229 offset:57344\n\t"
      "ds_read_b64_tr_b16 a[40:41], v230 offset:24576\n\t"
      "v_maximum3_f32 v236, v236, v72, v73\n\t"
      "v_maximum3_f32 v237, v237, v76, v77\n\t"
      "v_maximum3_f32 v238, v238, v80, v81\n\t"
      "v_maximum3_f32 v236, v236, v74, v75\n\t"
      "v_maximum3_f32 v237, v237, v78, v79\n\t"
      "v_maximum3_f32 v238, v238, v82, v83\n\t"
      "v_maximum3_f32 v236, v236, v84, v85\n\t"
      "v_maximum3_f32 v237, v237, v88, v89\n\t"
      "v_maximum3_f32 v238, v238, v92, v93\n\t"
      "v_maximum3_f32 v236, v236, v86, v87\n\t"
      "v_maximum3_f32 v237, v237, v90, v91\n\t"
      "v_maximum3_f32 v238, v238, v94, v95\n\t"
      "v_mov_b32 v246, v236\n\t"
      "v_mov_b32 v247, v237\n\t"
      "v_mov_b32 v248, v238\n\t"
      "v_permlane16_swap_b32 v246, v236\n\t"
      "v_permlane16_swap_b32 v247, v237\n\t"
      "v_permlane16_swap_b32 v248, v238\n\t"
      "v_maximum3_f32 v236, v236, v246, v246\n\t"
      "v_maximum3_f32 v237, v237, v247, v247\n\t"
      "v_maximum3_f32 v238, v238, v248, v248\n\t"
      "v_mov_b32 v246, v236\n\t"
      "v_mov_b32 v247, v237\n\t"
      "v_mov_b32 v248, v238\n\t"
      "v_permlane32_swap_b32 v246, v236\n\t"
      "v_permlane32_swap_b32 v247, v237\n\t"
      "v_permlane32_swap_b32 v248, v238\n\t"
      "v_maximum3_f32 v236, v236, v246, v246\n\t"
      "v_maximum3_f32 v237, v237, v247, v247\n\t"
      "v_maximum3_f32 v238, v238, v248, v248\n\t"
      "v_mul_f32 v240, 0xbe0293ee, v236\n\t"
      "v_mul_f32 v242, 0xbe0293ee, v237\n\t"
      "v_mul_f32 v244, 0xbe0293ee, v238\n\t"
      "v_fma_f32 v246, v241, s26, v240\n\t"
      "v_fma_f32 v248, v243, s26, v242\n\t"
      "v_fma_f32 v252, v249, s26, v244\n\t"
      "v_exp_f32 v246, v246\n\t"
      "v_exp_f32 v248, v248\n\t"
      "v_exp_f32 v252, v252\n\t"
      "v_pk_fma_f32 v[0:1], v[0:1], s[26:27], v[240:241] op_sel_hi:[1,0,0]\n\t"
      "v_pk_fma_f32 v[2:3], v[2:3], s[26:27], v[240:241] op_sel_hi:[1,0,0]\n\t"
      "v_pk_fma_f32 v[12:13], v[12:13], s[26:27], v[240:241] op_sel_hi:[1,0,0]\n\t"
      "v_pk_fma_f32 v[14:15], v[14:15], s[26:27], v[240:241] op_sel_hi:[1,0,0]\n\t"
      "v_exp_f32 v0, v0\n\t"
      "v_exp_f32 v1, v1\n\t"
      "v_exp_f32 v2, v2\n\t"
      "v_exp_f32 v3, v3\n\t"
      "v_exp_f32 v12, v12\n\t"
      "v_exp_f32 v13, v13\n\t"
      "v_exp_f32 v14, v14\n\t"
      "v_exp_f32 v15, v15\n\t"
      "v_pk_fma_f32 v[4:5], v[4:5], s[26:27], v[242:243] op_sel_hi:[1,0,0]\n\t"
      "v_pk_fma_f32 v[6:7], v[6:7], s[26:27], v[242:243] op_sel_hi:[1,0,0]\n\t"
      "v_pk_fma_f32 v[16:17], v[16:17], s[26:27], v[242:243] op_sel_hi:[1,0,0]\n\t"
      "v_pk_fma_f32 v[18:19], v[18:19], s[26:27], v[242:243] op_sel_hi:[1,0,0]\n\t"
      "ds_read_b64_tr_b16 a[42:43], v230 offset:57344\n\t"
      "ds_read_b64_tr_b16 a[44:45], v231 offset:24576\n\t"
      "v_pk_mul_f32 v[192:193], v[192:193], v[246:247] op_sel_hi:[1,0]\n\t"
      "v_pk_mul_f32 v[96:97], v[96:97], v[246:247] op_sel_hi:[1,0]\n\t"
      "v_pk_mul_f32 v[98:99], v[98:99], v[246:247] op_sel_hi:[1,0]\n\t"
      "v_pk_mul_f32 v[108:109], v[108:109], v[246:247] op_sel_hi:[1,0]\n\t"
      "v_pk_mul_f32 v[110:111], v[110:111], v[246:247] op_sel_hi:[1,0]\n\t"
      "v_cvt_pk_bf16_f32 v0, v0, v1\n\t"
      "v_cvt_pk_bf16_f32 v1, v2, v3\n\t"
      "v_cvt_pk_bf16_f32 v2, v12, v13\n\t"
      "v_cvt_pk_bf16_f32 v3, v14, v15\n\t"
      "v_exp_f32 v4, v4\n\t"
      "v_exp_f32 v5, v5\n\t"
      "v_exp_f32 v6, v6\n\t"
      "v_exp_f32 v7, v7\n\t"
      "v_exp_f32 v16, v16\n\t"
      "v_exp_f32 v17, v17\n\t"
      "v_exp_f32 v18, v18\n\t"
      "v_exp_f32 v19, v19\n\t"
      "v_pk_fma_f32 v[8:9], v[8:9], s[26:27], v[244:245] op_sel_hi:[1,0,0]\n\t"
      "v_pk_fma_f32 v[10:11], v[10:11], s[26:27], v[244:245] op_sel_hi:[1,0,0]\n\t"
      "v_pk_fma_f32 v[20:21], v[20:21], s[26:27], v[244:245] op_sel_hi:[1,0,0]\n\t"
      "v_pk_fma_f32 v[22:23], v[22:23], s[26:27], v[244:245] op_sel_hi:[1,0,0]\n\t"
      "ds_read_b64_tr_b16 a[46:47], v231 offset:57344\n\t"
      "ds_read_b64_tr_b16 a[48:49], v232 offset:24576\n\t"
      "v_pk_mul_f32 v[120:121], v[120:121], v[246:247] op_sel_hi:[1,0]\n\t"
      "v_pk_mul_f32 v[122:123], v[122:123], v[246:247] op_sel_hi:[1,0]\n\t"
      "v_pk_mul_f32 v[132:133], v[132:133], v[246:247] op_sel_hi:[1,0]\n\t"
      "v_pk_mul_f32 v[134:135], v[134:135], v[246:247] op_sel_hi:[1,0]\n\t"
      "v_pk_mul_f32 v[144:145], v[144:145], v[246:247] op_sel_hi:[1,0]\n\t"
      "v_cvt_pk_bf16_f32 v4, v4, v5\n\t"
      "v_cvt_pk_bf16_f32 v5, v6, v7\n\t"
      "v_cvt_pk_bf16_f32 v6, v16, v17\n\t"
      "v_cvt_pk_bf16_f32 v7, v18, v19\n\t"
      "v_exp_f32 v8, v8\n\t"
      "v_exp_f32 v9, v9\n\t"
      "v_exp_f32 v10, v10\n\t"
      "v_exp_f32 v11, v11\n\t"
      "v_exp_f32 v20, v20\n\t"
      "v_exp_f32 v21, v21\n\t"
      "v_exp_f32 v22, v22\n\t"
      "v_exp_f32 v23, v23\n\t"
      "v_pk_fma_f32 v[24:25], v[24:25], s[26:27], v[240:241] op_sel_hi:[1,0,0]\n\t"
      "v_pk_fma_f32 v[26:27], v[26:27], s[26:27], v[240:241] op_sel_hi:[1,0,0]\n\t"
      "v_pk_fma_f32 v[36:37], v[36:37], s[26:27], v[240:241] op_sel_hi:[1,0,0]\n\t"
      "v_pk_fma_f32 v[38:39], v[38:39], s[26:27], v[240:241] op_sel_hi:[1,0,0]\n\t"
      "ds_read_b64_tr_b16 a[50:51], v232 offset:57344\n\t"
      "ds_read_b64_tr_b16 a[52:53], v233 offset:24576\n\t"
      "v_pk_mul_f32 v[146:147], v[146:147], v[246:247] op_sel_hi:[1,0]\n\t"
      "v_pk_mul_f32 v[156:157], v[156:157], v[246:247] op_sel_hi:[1,0]\n\t"
      "v_pk_mul_f32 v[158:159], v[158:159], v[246:247] op_sel_hi:[1,0]\n\t"
      "v_pk_mul_f32 v[168:169], v[168:169], v[246:247] op_sel_hi:[1,0]\n\t"
      "v_pk_mul_f32 v[170:171], v[170:171], v[246:247] op_sel_hi:[1,0]\n\t"
      "v_cvt_pk_bf16_f32 v8, v8, v9\n\t"
      "v_cvt_pk_bf16_f32 v9, v10, v11\n\t"
      "v_cvt_pk_bf16_f32 v10, v20, v21\n\t"
      "v_cvt_pk_bf16_f32 v11, v22, v23\n\t"
      "v_exp_f32 v24, v24\n\t"
      "v_exp_f32 v25, v25\n\t"
      "v_exp_f32 v26, v26\n\t"
      "v_exp_f32 v27, v27\n\t"
      "v_exp_f32 v36, v36\n\t"
      "v_exp_f32 v37, v37\n\t"
      "v_exp_f32 v38, v38\n\t"
      "v_exp_f32 v39, v39\n\t"
      "v_pk_fma_f32 v[28:29], v[28:29], s[26:27], v[242:243] op_sel_hi:[1,0,0]\n\t"
      "v_pk_fma_f32 v[30:31], v[30:31], s[26:27], v[242:243] op_sel_hi:[1,0,0]\n\t"
      "v_pk_fma_f32 v[40:41], v[40:41], s[26:27], v[242:243] op_sel_hi:[1,0,0]\n\t"
      "v_pk_fma_f32 v[42:43], v[42:43], s[26:27], v[242:243] op_sel_hi:[1,0,0]\n\t"
      "ds_read_b64_tr_b16 a[54:55], v233 offset:57344\n\t"
      "ds_read_b64_tr_b16 a[56:57], v234 offset:24576\n\t"
      "v_pk_mul_f32 v[180:181], v[180:181], v[246:247] op_sel_hi:[1,0]\n\t"
      "v_pk_mul_f32 v[182:183], v[182:183], v[246:247] op_sel_hi:[1,0]\n\t"
      "v_pk_mul_f32 v[196:197], v[196:197], v[248:249] op_sel_hi:[1,0]\n\t"
      "v_pk_mul_f32 v[100:101], v[100:101], v[248:249] op_sel_hi:[1,0]\n\t"
      "v_pk_mul_f32 v[102:103], v[102:103], v[248:249] op_sel_hi:[1,0]\n\t"
      "v_cvt_pk_bf16_f32 v24, v24, v25\n\t"
      "v_cvt_pk_bf16_f32 v25, v26, v27\n\t"
      "v_cvt_pk_bf16_f32 v26, v36, v37\n\t"
      "v_cvt_pk_bf16_f32 v27, v38, v39\n\t"
      "v_exp_f32 v28, v28\n\t"
      "v_exp_f32 v29, v29\n\t"
      "v_exp_f32 v30, v30\n\t"
      "v_exp_f32 v31, v31\n\t"
      "v_exp_f32 v40, v40\n\t"
      "v_exp_f32 v41, v41\n\t"
      "v_exp_f32 v42, v42\n\t"
      "v_exp_f32 v43, v43\n\t"
      "v_pk_fma_f32 v[32:33], v[32:33], s[26:27], v[244:245] op_sel_hi:[1,0,0]\n\t"
      "v_pk_fma_f32 v[34:35], v[34:35], s[26:27], v[244:245] op_sel_hi:[1,0,0]\n\t"
      "v_pk_fma_f32 v[44:45], v[44:45], s[26:27], v[244:245] op_sel_hi:[1,0,0]\n\t"
      "v_pk_fma_f32 v[46:47], v[46:47], s[26:27], v[244:245] op_sel_hi:[1,0,0]\n\t"
      "ds_read_b64_tr_b16 a[58:59], v234 offset:57344\n\t"
      "ds_read_b64_tr_b16 a[60:61], v235 offset:24576\n\t"
      "v_pk_mul_f32 v[112:113], v[112:113], v[248:249] op_sel_hi:[1,0]\n\t"
      "v_pk_mul_f32 v[114:115], v[114:115], v[248:249] op_sel_hi:[1,0]\n\t"
      "v_pk_mul_f32 v[124:125], v[124:125], v[248:249] op_sel_hi:[1,0]\n\t"
      "v_pk_mul_f32 v[126:127], v[126:127], v[248:249] op_sel_hi:[1,0]\n\t"
      "v_pk_mul_f32 v[136:137], v[136:137], v[248:249] op_sel_hi:[1,0]\n\t"
      "v_cvt_pk_bf16_f32 v28, v28, v29\n\t"
      "v_cvt_pk_bf16_f32 v29, v30, v31\n\t"
      "v_cvt_pk_bf16_f32 v30, v40, v41\n\t"
      "v_cvt_pk_bf16_f32 v31, v42, v43\n\t"
      "v_exp_f32 v32, v32\n\t"
      "v_exp_f32 v33, v33\n\t"
      "v_exp_f32 v34, v34\n\t"
      "v_exp_f32 v35, v35\n\t"
      "v_exp_f32 v44, v44\n\t"
      "v_exp_f32 v45, v45\n\t"
      "v_exp_f32 v46, v46\n\t"
      "v_exp_f32 v47, v47\n\t"
      "v_pk_fma_f32 v[48:49], v[48:49], s[26:27], v[240:241] op_sel_hi:[1,0,0]\n\t"
      "v_pk_fma_f32 v[50:51], v[50:51], s[26:27], v[240:241] op_sel_hi:[1,0,0]\n\t"
      "v_pk_fma_f32 v[60:61], v[60:61], s[26:27], v[240:241] op_sel_hi:[1,0,0]\n\t"
      "v_pk_fma_f32 v[62:63], v[62:63], s[26:27], v[240:241] op_sel_hi:[1,0,0]\n\t"
      "ds_read_b64_tr_b16 a[62:63], v235 offset:57344\n\t"
      "s_waitcnt lgkmcnt(0)\n\t"
      "s_barrier\n\t"
      "v_pk_mul_f32 v[138:139], v[138:139], v[248:249] op_sel_hi:[1,0]\n\t"
      "v_pk_mul_f32 v[148:149], v[148:149], v[248:249] op_sel_hi:[1,0]\n\t"
      "v_pk_mul_f32 v[150:151], v[150:151], v[248:249] op_sel_hi:[1,0]\n\t"
      "v_pk_mul_f32 v[160:161], v[160:161], v[248:249] op_sel_hi:[1,0]\n\t"
      "v_pk_mul_f32 v[162:163], v[162:163], v[248:249] op_sel_hi:[1,0]\n\t"
      "v_cvt_pk_bf16_f32 v32, v32, v33\n\t"
      "v_cvt_pk_bf16_f32 v33, v34, v35\n\t"
      "v_cvt_pk_bf16_f32 v34, v44, v45\n\t"
      "v_cvt_pk_bf16_f32 v35, v46, v47\n\t"
      "v_exp_f32 v48, v48\n\t"
      "v_exp_f32 v49, v49\n\t"
      "v_exp_f32 v50, v50\n\t"
      "v_exp_f32 v51, v51\n\t"
      "v_exp_f32 v60, v60\n\t"
      "v_exp_f32 v61, v61\n\t"
      "v_exp_f32 v62, v62\n\t"
      "v_exp_f32 v63, v63\n\t"
      "v_pk_fma_f32 v[52:53], v[52:53], s[26:27], v[242:243] op_sel_hi:[1,0,0]\n\t"
      "v_pk_fma_f32 v[54:55], v[54:55], s[26:27], v[242:243] op_sel_hi:[1,0,0]\n\t"
      "v_pk_fma_f32 v[64:65], v[64:65], s[26:27], v[242:243] op_sel_hi:[1,0,0]\n\t"
      "v_pk_fma_f32 v[66:67], v[66:67], s[26:27], v[242:243] op_sel_hi:[1,0,0]\n\t"
      "s_mov_b32 m0, s96\n\t"
      "s_nop 0\n\t"
      "buffer_load_dwordx4 v204, s[32:35], s58 offen nt lds\n\t"
      "v_pk_mul_f32 v[172:173], v[172:173], v[248:249] op_sel_hi:[1,0]\n\t"
      "v_pk_mul_f32 v[174:175], v[174:175], v[248:249] op_sel_hi:[1,0]\n\t"
      "v_pk_mul_f32 v[184:185], v[184:185], v[248:249] op_sel_hi:[1,0]\n\t"
      "v_pk_mul_f32 v[186:187], v[186:187], v[248:249] op_sel_hi:[1,0]\n\t"
      "v_pk_mul_f32 v[200:201], v[200:201], v[252:253] op_sel_hi:[1,0]\n\t"
      "v_cvt_pk_bf16_f32 v48, v48, v49\n\t"
      "v_cvt_pk_bf16_f32 v49, v50, v51\n\t"
      "v_cvt_pk_bf16_f32 v50, v60, v61\n\t"
      "v_cvt_pk_bf16_f32 v51, v62, v63\n\t"
      "v_exp_f32 v52, v52\n\t"
      "v_exp_f32 v53, v53\n\t"
      "v_exp_f32 v54, v54\n\t"
      "v_exp_f32 v55, v55\n\t"
      "v_exp_f32 v64, v64\n\t"
      "v_exp_f32 v65, v65\n\t"
      "v_exp_f32 v66, v66\n\t"
      "v_exp_f32 v67, v67\n\t"
      "v_pk_fma_f32 v[56:57], v[56:57], s[26:27], v[244:245] op_sel_hi:[1,0,0]\n\t"
      "v_pk_fma_f32 v[58:59], v[58:59], s[26:27], v[244:245] op_sel_hi:[1,0,0]\n\t"
      "v_pk_fma_f32 v[68:69], v[68:69], s[26:27], v[244:245] op_sel_hi:[1,0,0]\n\t"
      "v_pk_fma_f32 v[70:71], v[70:71], s[26:27], v[244:245] op_sel_hi:[1,0,0]\n\t"
      "buffer_load_dwordx4 v205, s[32:35], s58 offen offset:1024 nt lds\n\t"
      "buffer_load_dwordx4 v206, s[32:35], s58 offen offset:2048 nt lds\n\t"
      "v_pk_mul_f32 v[104:105], v[104:105], v[252:253] op_sel_hi:[1,0]\n\t"
      "v_pk_mul_f32 v[106:107], v[106:107], v[252:253] op_sel_hi:[1,0]\n\t"
      "v_pk_mul_f32 v[116:117], v[116:117], v[252:253] op_sel_hi:[1,0]\n\t"
      "v_pk_mul_f32 v[118:119], v[118:119], v[252:253] op_sel_hi:[1,0]\n\t"
      "v_pk_mul_f32 v[128:129], v[128:129], v[252:253] op_sel_hi:[1,0]\n\t"
      "v_cvt_pk_bf16_f32 v52, v52, v53\n\t"
      "v_cvt_pk_bf16_f32 v53, v54, v55\n\t"
      "v_cvt_pk_bf16_f32 v54, v64, v65\n\t"
      "v_cvt_pk_bf16_f32 v55, v66, v67\n\t"
      "v_exp_f32 v56, v56\n\t"
      "v_exp_f32 v57, v57\n\t"
      "v_exp_f32 v58, v58\n\t"
      "v_exp_f32 v59, v59\n\t"
      "v_exp_f32 v68, v68\n\t"
      "v_exp_f32 v69, v69\n\t"
      "v_exp_f32 v70, v70\n\t"
      "v_exp_f32 v71, v71\n\t"
      "v_pk_fma_f32 v[72:73], v[72:73], s[26:27], v[240:241] op_sel_hi:[1,0,0]\n\t"
      "v_pk_fma_f32 v[74:75], v[74:75], s[26:27], v[240:241] op_sel_hi:[1,0,0]\n\t"
      "v_pk_fma_f32 v[84:85], v[84:85], s[26:27], v[240:241] op_sel_hi:[1,0,0]\n\t"
      "v_pk_fma_f32 v[86:87], v[86:87], s[26:27], v[240:241] op_sel_hi:[1,0,0]\n\t"
      "buffer_load_dwordx4 v207, s[32:35], s58 offen offset:3072 nt lds\n\t"
      "v_pk_mul_f32 v[130:131], v[130:131], v[252:253] op_sel_hi:[1,0]\n\t"
      "v_pk_mul_f32 v[140:141], v[140:141], v[252:253] op_sel_hi:[1,0]\n\t"
      "v_pk_mul_f32 v[142:143], v[142:143], v[252:253] op_sel_hi:[1,0]\n\t"
      "v_pk_mul_f32 v[152:153], v[152:153], v[252:253] op_sel_hi:[1,0]\n\t"
      "v_pk_mul_f32 v[154:155], v[154:155], v[252:253] op_sel_hi:[1,0]\n\t"
      "v_cvt_pk_bf16_f32 v56, v56, v57\n\t"
      "v_cvt_pk_bf16_f32 v57, v58, v59\n\t"
      "v_cvt_pk_bf16_f32 v58, v68, v69\n\t"
      "v_cvt_pk_bf16_f32 v59, v70, v71\n\t"
      "v_exp_f32 v72, v72\n\t"
      "v_exp_f32 v73, v73\n\t"
      "v_exp_f32 v74, v74\n\t"
      "v_exp_f32 v75, v75\n\t"
      "v_exp_f32 v84, v84\n\t"
      "v_exp_f32 v85, v85\n\t"
      "v_exp_f32 v86, v86\n\t"
      "v_exp_f32 v87, v87\n\t"
      "v_pk_fma_f32 v[76:77], v[76:77], s[26:27], v[242:243] op_sel_hi:[1,0,0]\n\t"
      "v_pk_fma_f32 v[78:79], v[78:79], s[26:27], v[242:243] op_sel_hi:[1,0,0]\n\t"
      "v_pk_fma_f32 v[88:89], v[88:89], s[26:27], v[242:243] op_sel_hi:[1,0,0]\n\t"
      "v_pk_fma_f32 v[90:91], v[90:91], s[26:27], v[242:243] op_sel_hi:[1,0,0]\n\t"
      "v_pk_mul_f32 v[164:165], v[164:165], v[252:253] op_sel_hi:[1,0]\n\t"
      "v_pk_mul_f32 v[166:167], v[166:167], v[252:253] op_sel_hi:[1,0]\n\t"
      "v_pk_mul_f32 v[176:177], v[176:177], v[252:253] op_sel_hi:[1,0]\n\t"
      "v_pk_mul_f32 v[178:179], v[178:179], v[252:253] op_sel_hi:[1,0]\n\t"
      "v_pk_mul_f32 v[188:189], v[188:189], v[252:253] op_sel_hi:[1,0]\n\t"
      "v_cvt_pk_bf16_f32 v72, v72, v73\n\t"
      "v_cvt_pk_bf16_f32 v73, v74, v75\n\t"
      "v_cvt_pk_bf16_f32 v74, v84, v85\n\t"
      "v_cvt_pk_bf16_f32 v75, v86, v87\n\t"
      "v_exp_f32 v76, v76\n\t"
      "v_exp_f32 v77, v77\n\t"
      "v_exp_f32 v78, v78\n\t"
      "v_exp_f32 v79, v79\n\t"
      "v_exp_f32 v88, v88\n\t"
      "v_exp_f32 v89, v89\n\t"
      "v_exp_f32 v90, v90\n\t"
      "v_exp_f32 v91, v91\n\t"
      "v_pk_fma_f32 v[80:81], v[80:81], s[26:27], v[244:245] op_sel_hi:[1,0,0]\n\t"
      "v_pk_fma_f32 v[82:83], v[82:83], s[26:27], v[244:245] op_sel_hi:[1,0,0]\n\t"
      "v_pk_fma_f32 v[92:93], v[92:93], s[26:27], v[244:245] op_sel_hi:[1,0,0]\n\t"
      "v_pk_fma_f32 v[94:95], v[94:95], s[26:27], v[244:245] op_sel_hi:[1,0,0]\n\t"
      "v_pk_mul_f32 v[190:191], v[190:191], v[252:253] op_sel_hi:[1,0]\n\t"
      "v_cvt_pk_bf16_f32 v76, v76, v77\n\t"
      "v_cvt_pk_bf16_f32 v77, v78, v79\n\t"
      "v_cvt_pk_bf16_f32 v78, v88, v89\n\t"
      "v_cvt_pk_bf16_f32 v79, v90, v91\n\t"
      "v_exp_f32 v80, v80\n\t"
      "v_exp_f32 v81, v81\n\t"
      "v_exp_f32 v82, v82\n\t"
      "v_exp_f32 v83, v83\n\t"
      "v_exp_f32 v92, v92\n\t"
      "v_exp_f32 v93, v93\n\t"
      "v_exp_f32 v94, v94\n\t"
      "v_exp_f32 v95, v95\n\t"
      "v_cvt_pk_bf16_f32 v80, v80, v81\n\t"
      "v_cvt_pk_bf16_f32 v81, v82, v83\n\t"
      "v_cvt_pk_bf16_f32 v82, v92, v93\n\t"
      "v_cvt_pk_bf16_f32 v83, v94, v95\n\t"
      "v_mfma_f32_16x16x32_bf16 v[192:195], a[208:211], v[0:3], v[192:195]\n\t"
      "v_mfma_f32_16x16x32_bf16 v[196:199], a[208:211], v[4:7], v[196:199]\n\t"
      "v_mfma_f32_16x16x32_bf16 v[200:203], a[208:211], v[8:11], v[200:203]\n\t"
      "s_xor_b32 s19, s10, 0\n\t"
      "s_lshl_b32 s19, s19, 4\n\t"
      "s_add_u32 s19, s19, s9\n\t"
      "s_lshl_b32 s19, s19, 2\n\t"
      "s_add_u32 s19, s19, s6\n\t"
      "s_lshl_b32 s19, s19, 13\n\t"
      "s_add_u32 s36, s98, s19\n\t"
      "s_addc_u32 s37, s99, 0\n\t"
      "s_sub_u32 s38, 0x400000, s19\n\t"
      "s_mov_b32 s39, 0x20000\n\t"
      "s_mov_b32 m0, 0x27000\n\t"
      "v_lshlrev_b32 v246, 3, v216\n\t"
      "v_mfma_f32_16x16x32_bf16 v[96:99], a[0:3], v[0:3], v[96:99]\n\t"
      "v_mfma_f32_16x16x32_bf16 v[100:103], a[0:3], v[4:7], v[100:103]\n\t"
      "buffer_load_dword v246, s[36:39], 0 offen lds\n\t"
      "v_mfma_f32_16x16x32_bf16 v[104:107], a[0:3], v[8:11], v[104:107]\n\t"
      "v_mfma_f32_16x16x32_bf16 v[108:111], a[4:7], v[0:3], v[108:111]\n\t"
      "v_mfma_f32_16x16x32_bf16 v[112:115], a[4:7], v[4:7], v[112:115]\n\t"
      "v_mfma_f32_16x16x32_bf16 v[116:119], a[4:7], v[8:11], v[116:119]\n\t"
      "v_mfma_f32_16x16x32_bf16 v[120:123], a[8:11], v[0:3], v[120:123]\n\t"
      "v_mfma_f32_16x16x32_bf16 v[124:127], a[8:11], v[4:7], v[124:127]\n\t"
      "v_mfma_f32_16x16x32_bf16 v[128:131], a[8:11], v[8:11], v[128:131]\n\t"
      "v_mfma_f32_16x16x32_bf16 v[132:135], a[12:15], v[0:3], v[132:135]\n\t"
      "v_mfma_f32_16x16x32_bf16 v[136:139], a[12:15], v[4:7], v[136:139]\n\t"
      "v_mfma_f32_16x16x32_bf16 v[140:143], a[12:15], v[8:11], v[140:143]\n\t"
      "v_mfma_f32_16x16x32_bf16 v[144:147], a[16:19], v[0:3], v[144:147]\n\t"
      "v_mfma_f32_16x16x32_bf16 v[148:151], a[16:19], v[4:7], v[148:151]\n\t"
      "v_mfma_f32_16x16x32_bf16 v[152:155], a[16:19], v[8:11], v[152:155]\n\t"
      "v_mfma_f32_16x16x32_bf16 v[156:159], a[20:23], v[0:3], v[156:159]\n\t"
      "v_mfma_f32_16x16x32_bf16 v[160:163], a[20:23], v[4:7], v[160:163]\n\t"
      "v_mfma_f32_16x16x32_bf16 v[164:167], a[20:23], v[8:11], v[164:167]\n\t"
      "v_mfma_f32_16x16x32_bf16 v[168:171], a[24:27], v[0:3], v[168:171]\n\t"
      "v_mfma_f32_16x16x32_bf16 v[172:175], a[24:27], v[4:7], v[172:175]\n\t"
      "v_mfma_f32_16x16x32_bf16 v[176:179], a[24:27], v[8:11], v[176:179]\n\t"
      "v_mfma_f32_16x16x32_bf16 v[180:183], a[28:31], v[0:3], v[180:183]\n\t"
      "v_mfma_f32_16x16x32_bf16 v[184:187], a[28:31], v[4:7], v[184:187]\n\t"
      "v_mfma_f32_16x16x32_bf16 v[188:191], a[28:31], v[8:11], v[188:191]\n\t"
      "v_mfma_f32_16x16x32_bf16 v[192:195], a[208:211], v[24:27], v[192:195]\n\t"
      "ds_read_b64_tr_b16 a[0:1], v220 offset:28672\n\t"
      "v_mfma_f32_16x16x32_bf16 v[196:199], a[208:211], v[28:31], v[196:199]\n\t"
      "ds_read_b64_tr_b16 a[2:3], v220 offset:61440\n\t"
      "v_mfma_f32_16x16x32_bf16 v[200:203], a[208:211], v[32:35], v[200:203]\n\t"
      "ds_read_b64_tr_b16 a[4:5], v221 offset:28672\n\t"
      "v_mfma_f32_16x16x32_bf16 v[96:99], a[32:35], v[24:27], v[96:99]\n\t"
      "v_mfma_f32_16x16x32_bf16 v[100:103], a[32:35], v[28:31], v[100:103]\n\t"
      "ds_read_b64_tr_b16 a[6:7], v221 offset:61440\n\t"
      "v_mfma_f32_16x16x32_bf16 v[104:107], a[32:35], v[32:35], v[104:107]\n\t"
      "ds_read_b64_tr_b16 a[8:9], v222 offset:28672\n\t"
      "v_mfma_f32_16x16x32_bf16 v[108:111], a[36:39], v[24:27], v[108:111]\n\t"
      "v_mfma_f32_16x16x32_bf16 v[112:115], a[36:39], v[28:31], v[112:115]\n\t"
      "ds_read_b64_tr_b16 a[10:11], v222 offset:61440\n\t"
      "v_mfma_f32_16x16x32_bf16 v[116:119], a[36:39], v[32:35], v[116:119]\n\t"
      "ds_read_b64_tr_b16 a[12:13], v223 offset:28672\n\t"
      "v_mfma_f32_16x16x32_bf16 v[120:123], a[40:43], v[24:27], v[120:123]\n\t"
      "v_mfma_f32_16x16x32_bf16 v[124:127], a[40:43], v[28:31], v[124:127]\n\t"
      "ds_read_b64_tr_b16 a[14:15], v223 offset:61440\n\t"
      "v_mfma_f32_16x16x32_bf16 v[128:131], a[40:43], v[32:35], v[128:131]\n\t"
      "ds_read_b64_tr_b16 a[16:17], v224 offset:28672\n\t"
      "v_mfma_f32_16x16x32_bf16 v[132:135], a[44:47], v[24:27], v[132:135]\n\t"
      "ds_read_b64_tr_b16 a[18:19], v224 offset:61440\n\t"
      "v_mfma_f32_16x16x32_bf16 v[136:139], a[44:47], v[28:31], v[136:139]\n\t"
      "v_mfma_f32_16x16x32_bf16 v[140:143], a[44:47], v[32:35], v[140:143]\n\t"
      "ds_read_b64_tr_b16 a[20:21], v225 offset:28672\n\t"
      "v_mfma_f32_16x16x32_bf16 v[144:147], a[48:51], v[24:27], v[144:147]\n\t"
      "ds_read_b64_tr_b16 a[22:23], v225 offset:61440\n\t"
      "v_mfma_f32_16x16x32_bf16 v[148:151], a[48:51], v[28:31], v[148:151]\n\t"
      "v_mfma_f32_16x16x32_bf16 v[152:155], a[48:51], v[32:35], v[152:155]\n\t"
      "ds_read_b64_tr_b16 a[24:25], v226 offset:28672\n\t"
      "v_mfma_f32_16x16x32_bf16 v[156:159], a[52:55], v[24:27], v[156:159]\n\t"
      "ds_read_b64_tr_b16 a[26:27], v226 offset:61440\n\t"
      "v_mfma_f32_16x16x32_bf16 v[160:163], a[52:55], v[28:31], v[160:163]\n\t"
      "v_mfma_f32_16x16x32_bf16 v[164:167], a[52:55], v[32:35], v[164:167]\n\t"
      "ds_read_b64_tr_b16 a[28:29], v227 offset:28672\n\t"
      "v_mfma_f32_16x16x32_bf16 v[168:171], a[56:59], v[24:27], v[168:171]\n\t"
      "ds_read_b64_tr_b16 a[30:31], v227 offset:61440\n\t"
      "v_mfma_f32_16x16x32_bf16 v[172:175], a[56:59], v[28:31], v[172:175]\n\t"
      "v_mfma_f32_16x16x32_bf16 v[176:179], a[56:59], v[32:35], v[176:179]\n\t"
      "v_mfma_f32_16x16x32_bf16 v[180:183], a[60:63], v[24:27], v[180:183]\n\t"
      "v_mfma_f32_16x16x32_bf16 v[184:187], a[60:63], v[28:31], v[184:187]\n\t"
      "v_mfma_f32_16x16x32_bf16 v[188:191], a[60:63], v[32:35], v[188:191]\n\t"
      "s_waitcnt lgkmcnt(0)\n\t"
      "v_mfma_f32_16x16x32_bf16 v[192:195], a[208:211], v[48:51], v[192:195]\n\t"
      "ds_read_b64_tr_b16 a[32:33], v228 offset:28672\n\t"
      "v_mfma_f32_16x16x32_bf16 v[196:199], a[208:211], v[52:55], v[196:199]\n\t"
      "ds_read_b64_tr_b16 a[34:35], v228 offset:61440\n\t"
      "v_mfma_f32_16x16x32_bf16 v[200:203], a[208:211], v[56:59], v[200:203]\n\t"
      "ds_read_b64_tr_b16 a[36:37], v229 offset:28672\n\t"
      "v_mfma_f32_16x16x32_bf16 v[96:99], a[0:3], v[48:51], v[96:99]\n\t"
      "v_mfma_f32_16x16x32_bf16 v[100:103], a[0:3], v[52:55], v[100:103]\n\t"
      "ds_read_b64_tr_b16 a[38:39], v229 offset:61440\n\t"
      "v_mfma_f32_16x16x32_bf16 v[104:107], a[0:3], v[56:59], v[104:107]\n\t"
      "ds_read_b64_tr_b16 a[40:41], v230 offset:28672\n\t"
      "v_mfma_f32_16x16x32_bf16 v[108:111], a[4:7], v[48:51], v[108:111]\n\t"
      "v_mfma_f32_16x16x32_bf16 v[112:115], a[4:7], v[52:55], v[112:115]\n\t"
      "ds_read_b64_tr_b16 a[42:43], v230 offset:61440\n\t"
      "v_mfma_f32_16x16x32_bf16 v[116:119], a[4:7], v[56:59], v[116:119]\n\t"
      "ds_read_b64_tr_b16 a[44:45], v231 offset:28672\n\t"
      "v_mfma_f32_16x16x32_bf16 v[120:123], a[8:11], v[48:51], v[120:123]\n\t"
      "v_mfma_f32_16x16x32_bf16 v[124:127], a[8:11], v[52:55], v[124:127]\n\t"
      "ds_read_b64_tr_b16 a[46:47], v231 offset:61440\n\t"
      "v_mfma_f32_16x16x32_bf16 v[128:131], a[8:11], v[56:59], v[128:131]\n\t"
      "ds_read_b64_tr_b16 a[48:49], v232 offset:28672\n\t"
      "v_mfma_f32_16x16x32_bf16 v[132:135], a[12:15], v[48:51], v[132:135]\n\t"
      "ds_read_b64_tr_b16 a[50:51], v232 offset:61440\n\t"
      "v_mfma_f32_16x16x32_bf16 v[136:139], a[12:15], v[52:55], v[136:139]\n\t"
      "v_mfma_f32_16x16x32_bf16 v[140:143], a[12:15], v[56:59], v[140:143]\n\t"
      "ds_read_b64_tr_b16 a[52:53], v233 offset:28672\n\t"
      "v_mfma_f32_16x16x32_bf16 v[144:147], a[16:19], v[48:51], v[144:147]\n\t"
      "ds_read_b64_tr_b16 a[54:55], v233 offset:61440\n\t"
      "v_mfma_f32_16x16x32_bf16 v[148:151], a[16:19], v[52:55], v[148:151]\n\t"
      "v_mfma_f32_16x16x32_bf16 v[152:155], a[16:19], v[56:59], v[152:155]\n\t"
      "ds_read_b64_tr_b16 a[56:57], v234 offset:28672\n\t"
      "v_mfma_f32_16x16x32_bf16 v[156:159], a[20:23], v[48:51], v[156:159]\n\t"
      "ds_read_b64_tr_b16 a[58:59], v234 offset:61440\n\t"
      "v_mfma_f32_16x16x32_bf16 v[160:163], a[20:23], v[52:55], v[160:163]\n\t"
      "v_mfma_f32_16x16x32_bf16 v[164:167], a[20:23], v[56:59], v[164:167]\n\t"
      "ds_read_b64_tr_b16 a[60:61], v235 offset:28672\n\t"
      "v_mfma_f32_16x16x32_bf16 v[168:171], a[24:27], v[48:51], v[168:171]\n\t"
      "ds_read_b64_tr_b16 a[62:63], v235 offset:61440\n\t"
      "v_mfma_f32_16x16x32_bf16 v[172:175], a[24:27], v[52:55], v[172:175]\n\t"
      "v_mfma_f32_16x16x32_bf16 v[176:179], a[24:27], v[56:59], v[176:179]\n\t"
      "v_mfma_f32_16x16x32_bf16 v[180:183], a[28:31], v[48:51], v[180:183]\n\t"
      "v_mfma_f32_16x16x32_bf16 v[184:187], a[28:31], v[52:55], v[184:187]\n\t"
      "v_mfma_f32_16x16x32_bf16 v[188:191], a[28:31], v[56:59], v[188:191]\n\t"
      "s_waitcnt lgkmcnt(0)\n\t"
      "s_waitcnt vmcnt(29)\n\t"
      "s_barrier\n\t"
      "s_mov_b32 m0, s97\n\t"
      "v_mfma_f32_16x16x32_bf16 v[192:195], a[208:211], v[72:75], v[192:195]\n\t"
      "buffer_load_dwordx4 v204, s[32:35], s59 offen nt lds\n\t"
      "v_xor_b32 v220, 0, v217\n\t"
      "v_xor_b32 v221, 64, v217\n\t"
      "v_mfma_f32_16x16x32_bf16 v[196:199], a[208:211], v[76:79], v[196:199]\n\t"
      "v_xor_b32 v222, 128, v217\n\t"
      "v_mfma_f32_16x16x32_bf16 v[200:203], a[208:211], v[80:83], v[200:203]\n\t"
      "v_xor_b32 v223, 192, v217\n\t"
      "v_mfma_f32_16x16x32_bf16 v[96:99], a[32:35], v[72:75], v[96:99]\n\t"
      "v_xor_b32 v224, 0, v217\n\t"
      "v_add_u32 v224, 0x10000, v224\n\t"
      "v_mfma_f32_16x16x32_bf16 v[100:103], a[32:35], v[76:79], v[100:103]\n\t"
      "buffer_load_dwordx4 v205, s[32:35], s59 offen offset:1024 nt lds\n\t"
      "v_xor_b32 v225, 64, v217\n\t"
      "v_add_u32 v225, 0x10000, v225\n\t"
      "v_mfma_f32_16x16x32_bf16 v[104:107], a[32:35], v[80:83], v[104:107]\n\t"
      "v_xor_b32 v226, 128, v217\n\t"
      "v_add_u32 v226, 0x10000, v226\n\t"
      "v_mfma_f32_16x16x32_bf16 v[108:111], a[36:39], v[72:75], v[108:111]\n\t"
      "v_xor_b32 v227, 192, v217\n\t"
      "v_add_u32 v227, 0x10000, v227\n\t"
      "v_mfma_f32_16x16x32_bf16 v[112:115], a[36:39], v[76:79], v[112:115]\n\t"
      "ds_read_b128 a[0:3], v220\n\t"
      "v_mfma_f32_16x16x32_bf16 v[116:119], a[36:39], v[80:83], v[116:119]\n\t"
      "v_mfma_f32_16x16x32_bf16 v[120:123], a[40:43], v[72:75], v[120:123]\n\t"
      "ds_read_b128 a[4:7], v221\n\t"
      "v_mfma_f32_16x16x32_bf16 v[124:127], a[40:43], v[76:79], v[124:127]\n\t"
      "v_mfma_f32_16x16x32_bf16 v[128:131], a[40:43], v[80:83], v[128:131]\n\t"
      "ds_read_b128 a[8:11], v222\n\t"
      "v_mfma_f32_16x16x32_bf16 v[132:135], a[44:47], v[72:75], v[132:135]\n\t"
      "buffer_load_dwordx4 v206, s[32:35], s59 offen offset:2048 nt lds\n\t"
      "v_mfma_f32_16x16x32_bf16 v[136:139], a[44:47], v[76:79], v[136:139]\n\t"
      "ds_read_b128 a[12:15], v223\n\t"
      "v_mfma_f32_16x16x32_bf16 v[140:143], a[44:47], v[80:83], v[140:143]\n\t"
      "v_mfma_f32_16x16x32_bf16 v[144:147], a[48:51], v[72:75], v[144:147]\n\t"
      "ds_read_b128 a[16:19], v220 offset:32768\n\t"
      "v_mfma_f32_16x16x32_bf16 v[148:151], a[48:51], v[76:79], v[148:151]\n\t"
      "v_mfma_f32_16x16x32_bf16 v[152:155], a[48:51], v[80:83], v[152:155]\n\t"
      "ds_read_b128 a[20:23], v221 offset:32768\n\t"
      "v_mfma_f32_16x16x32_bf16 v[156:159], a[52:55], v[72:75], v[156:159]\n\t"
      "v_mfma_f32_16x16x32_bf16 v[160:163], a[52:55], v[76:79], v[160:163]\n\t"
      "ds_read_b128 a[24:27], v222 offset:32768\n\t"
      "v_mfma_f32_16x16x32_bf16 v[164:167], a[52:55], v[80:83], v[164:167]\n\t"
      "v_mfma_f32_16x16x32_bf16 v[168:171], a[56:59], v[72:75], v[168:171]\n\t"
      "ds_read_b128 a[28:31], v223 offset:32768\n\t"
      "v_mfma_f32_16x16x32_bf16 v[172:175], a[56:59], v[76:79], v[172:175]\n\t"
      "buffer_load_dwordx4 v207, s[32:35], s59 offen offset:3072 nt lds\n\t"
      "v_mfma_f32_16x16x32_bf16 v[176:179], a[56:59], v[80:83], v[176:179]\n\t"
      "v_mfma_f32_16x16x32_bf16 v[180:183], a[60:63], v[72:75], v[180:183]\n\t"
      "v_mfma_f32_16x16x32_bf16 v[184:187], a[60:63], v[76:79], v[184:187]\n\t"
      "v_mfma_f32_16x16x32_bf16 v[188:191], a[60:63], v[80:83], v[188:191]\n\t"
      "s_waitcnt lgkmcnt(0)\n\t"
      "v_mfma_f32_16x16x32_bf16 v[0:3], a[0:3], a[64:67], 0\n\t"
      "ds_read_b128 a[32:35], v224\n\t"
      "v_mfma_f32_16x16x32_bf16 v[0:3], a[4:7], a[76:79], v[0:3]\n\t"
      "v_mfma_f32_16x16x32_bf16 v[0:3], a[8:11], a[88:91], v[0:3]\n\t"
      "ds_read_b128 a[36:39], v225\n\t"
      "v_mov_b32 v241, v236\n\t"
      "v_mfma_f32_16x16x32_bf16 v[0:3], a[12:15], a[100:103], v[0:3]\n\t"
      "v_mov_b32 v243, v237\n\t"
      "v_mfma_f32_16x16x32_bf16 v[4:7], a[0:3], a[68:71], 0\n\t"
      "ds_read_b128 a[40:43], v226\n\t"
      "v_mov_b32 v249, v238\n\t"
      "v_mfma_f32_16x16x32_bf16 v[4:7], a[4:7], a[80:83], v[4:7]\n\t"
      "v_mfma_f32_16x16x32_bf16 v[4:7], a[8:11], a[92:95], v[4:7]\n\t"
      "ds_read_b128 a[44:47], v227\n\t"
      "v_mfma_f32_16x16x32_bf16 v[4:7], a[12:15], a[104:107], v[4:7]\n\t"
      "v_mfma_f32_16x16x32_bf16 v[8:11], a[0:3], a[72:75], 0\n\t"
      "ds_read_b128 a[48:51], v224 offset:32768\n\t"
      "v_mfma_f32_16x16x32_bf16 v[8:11], a[4:7], a[84:87], v[8:11]\n\t"
      "v_mfma_f32_16x16x32_bf16 v[8:11], a[8:11], a[96:99], v[8:11]\n\t"
      "ds_read_b128 a[52:55], v225 offset:32768\n\t"
      "v_mfma_f32_16x16x32_bf16 v[8:11], a[12:15], a[108:111], v[8:11]\n\t"
      "v_mfma_f32_16x16x32_bf16 v[12:15], a[16:19], a[64:67], 0\n\t"
      "ds_read_b128 a[56:59], v226 offset:32768\n\t"
      "v_mfma_f32_16x16x32_bf16 v[12:15], a[20:23], a[76:79], v[12:15]\n\t"
      "v_mfma_f32_16x16x32_bf16 v[12:15], a[24:27], a[88:91], v[12:15]\n\t"
      "ds_read_b128 a[60:63], v227 offset:32768\n\t"
      "v_mfma_f32_16x16x32_bf16 v[12:15], a[28:31], a[100:103], v[12:15]\n\t"
      "v_mfma_f32_16x16x32_bf16 v[16:19], a[16:19], a[68:71], 0\n\t"
      "v_mfma_f32_16x16x32_bf16 v[16:19], a[20:23], a[80:83], v[16:19]\n\t"
      "v_mfma_f32_16x16x32_bf16 v[16:19], a[24:27], a[92:95], v[16:19]\n\t"
      "v_mfma_f32_16x16x32_bf16 v[16:19], a[28:31], a[104:107], v[16:19]\n\t"
      "v_mfma_f32_16x16x32_bf16 v[20:23], a[16:19], a[72:75], 0\n\t"
      "v_mfma_f32_16x16x32_bf16 v[20:23], a[20:23], a[84:87], v[20:23]\n\t"
      "v_mfma_f32_16x16x32_bf16 v[20:23], a[24:27], a[96:99], v[20:23]\n\t"
      "v_mfma_f32_16x16x32_bf16 v[20:23], a[28:31], a[108:111], v[20:23]\n\t"
      "s_waitcnt lgkmcnt(0)\n\t"
      "s_waitcnt vmcnt(29)\n\t"
      "s_barrier\n\t"
      "v_mfma_f32_16x16x32_bf16 v[24:27], a[32:35], a[64:67], 0\n\t"
      "ds_read_b128 a[0:3], v220 offset:4096\n\t"
      "s_mov_b32 m0, s90\n\t"
      "v_mfma_f32_16x16x32_bf16 v[24:27], a[36:39], a[76:79], v[24:27]\n\t"
      "buffer_load_dwordx4 v208, s[44:47], s18 offen lds\n\t"
      "v_mfma_f32_16x16x32_bf16 v[24:27], a[40:43], a[88:91], v[24:27]\n\t"
      "ds_read_b128 a[4:7], v221 offset:4096\n\t"
      "v_mfma_f32_16x16x32_bf16 v[24:27], a[44:47], a[100:103], v[24:27]\n\t"
      "v_mfma_f32_16x16x32_bf16 v[28:31], a[32:35], a[68:71], 0\n\t"
      "ds_read_b128 a[8:11], v222 offset:4096\n\t"
      "v_mfma_f32_16x16x32_bf16 v[28:31], a[36:39], a[80:83], v[28:31]\n\t"
      "v_mfma_f32_16x16x32_bf16 v[28:31], a[40:43], a[92:95], v[28:31]\n\t"
      "ds_read_b128 a[12:15], v223 offset:4096\n\t"
      "v_mfma_f32_16x16x32_bf16 v[28:31], a[44:47], a[104:107], v[28:31]\n\t"
      "v_mfma_f32_16x16x32_bf16 v[32:35], a[32:35], a[72:75], 0\n\t"
      "ds_read_b128 a[16:19], v220 offset:36864\n\t"
      "v_maximum3_f32 v236, v236, v0, v1\n\t"
      "v_mfma_f32_16x16x32_bf16 v[32:35], a[36:39], a[84:87], v[32:35]\n\t"
      "buffer_load_dwordx4 v209, s[44:47], s18 offen offset:1024 lds\n\t"
      "v_maximum3_f32 v237, v237, v4, v5\n\t"
      "v_mfma_f32_16x16x32_bf16 v[32:35], a[40:43], a[96:99], v[32:35]\n\t"
      "ds_read_b128 a[20:23], v221 offset:36864\n\t"
      "v_maximum3_f32 v238, v238, v8, v9\n\t"
      "v_mfma_f32_16x16x32_bf16 v[32:35], a[44:47], a[108:111], v[32:35]\n\t"
      "v_mfma_f32_16x16x32_bf16 v[36:39], a[48:51], a[64:67], 0\n\t"
      "ds_read_b128 a[24:27], v222 offset:36864\n\t"
      "v_maximum3_f32 v236, v236, v2, v3\n\t"
      "v_mfma_f32_16x16x32_bf16 v[36:39], a[52:55], a[76:79], v[36:39]\n\t"
      "v_maximum3_f32 v237, v237, v6, v7\n\t"
      "v_mfma_f32_16x16x32_bf16 v[36:39], a[56:59], a[88:91], v[36:39]\n\t"
      "ds_read_b128 a[28:31], v223 offset:36864\n\t"
      "v_maximum3_f32 v238, v238, v10, v11\n\t"
      "v_mfma_f32_16x16x32_bf16 v[36:39], a[60:63], a[100:103], v[36:39]\n\t"
      "v_mfma_f32_16x16x32_bf16 v[40:43], a[48:51], a[68:71], 0\n\t"
      "buffer_load_dwordx4 v210, s[44:47], s18 offen offset:2048 lds\n\t"
      "v_maximum3_f32 v236, v236, v12, v13\n\t"
      "v_mfma_f32_16x16x32_bf16 v[40:43], a[52:55], a[80:83], v[40:43]\n\t"
      "v_maximum3_f32 v237, v237, v16, v17\n\t"
      "v_mfma_f32_16x16x32_bf16 v[40:43], a[56:59], a[92:95], v[40:43]\n\t"
      "v_maximum3_f32 v238, v238, v20, v21\n\t"
      "v_mfma_f32_16x16x32_bf16 v[40:43], a[60:63], a[104:107], v[40:43]\n\t"
      "v_mfma_f32_16x16x32_bf16 v[44:47], a[48:51], a[72:75], 0\n\t"
      "buffer_load_dwordx4 v211, s[44:47], s18 offen offset:3072 lds\n\t"
      "s_add_u32 s18, s18, 0x4000\n\t"
      "s_and_b32 s18, s18, 0x3ffff\n\t"
      "v_maximum3_f32 v236, v236, v14, v15\n\t"
      "v_mfma_f32_16x16x32_bf16 v[44:47], a[52:55], a[84:87], v[44:47]\n\t"
      "v_maximum3_f32 v237, v237, v18, v19\n\t"
      "v_mfma_f32_16x16x32_bf16 v[44:47], a[56:59], a[96:99], v[44:47]\n\t"
      "v_maximum3_f32 v238, v238, v22, v23\n\t"
      "v_mfma_f32_16x16x32_bf16 v[44:47], a[60:63], a[108:111], v[44:47]\n\t"
      "s_waitcnt lgkmcnt(0)\n\t"
      "v_mfma_f32_16x16x32_bf16 v[48:51], a[0:3], a[64:67], 0\n\t"
      "ds_read_b128 a[32:35], v224 offset:4096\n\t"
      "v_mfma_f32_16x16x32_bf16 v[48:51], a[4:7], a[76:79], v[48:51]\n\t"
      "v_mfma_f32_16x16x32_bf16 v[48:51], a[8:11], a[88:91], v[48:51]\n\t"
      "ds_read_b128 a[36:39], v225 offset:4096\n\t"
      "v_mfma_f32_16x16x32_bf16 v[48:51], a[12:15], a[100:103], v[48:51]\n\t"
      "v_mfma_f32_16x16x32_bf16 v[52:55], a[0:3], a[68:71], 0\n\t"
      "ds_read_b128 a[40:43], v226 offset:4096\n\t"
      "v_mfma_f32_16x16x32_bf16 v[52:55], a[4:7], a[80:83], v[52:55]\n\t"
      "v_mfma_f32_16x16x32_bf16 v[52:55], a[8:11], a[92:95], v[52:55]\n\t"
      "ds_read_b128 a[44:47], v227 offset:4096\n\t"
      "v_mfma_f32_16x16x32_bf16 v[52:55], a[12:15], a[104:107], v[52:55]\n\t"
      "v_mfma_f32_16x16x32_bf16 v[56:59], a[0:3], a[72:75], 0\n\t"
      "ds_read_b128 a[48:51], v224 offset:36864\n\t"
      "v_maximum3_f32 v236, v236, v24, v25\n\t"
      "v_mfma_f32_16x16x32_bf16 v[56:59], a[4:7], a[84:87], v[56:59]\n\t"
      "v_maximum3_f32 v237, v237, v28, v29\n\t"
      "v_mfma_f32_16x16x32_bf16 v[56:59], a[8:11], a[96:99], v[56:59]\n\t"
      "ds_read_b128 a[52:55], v225 offset:36864\n\t"
      "v_maximum3_f32 v238, v238, v32, v33\n\t"
      "v_mfma_f32_16x16x32_bf16 v[56:59], a[12:15], a[108:111], v[56:59]\n\t"
      "v_mfma_f32_16x16x32_bf16 v[60:63], a[16:19], a[64:67], 0\n\t"
      "ds_read_b128 a[56:59], v226 offset:36864\n\t"
      "v_maximum3_f32 v236, v236, v26, v27\n\t"
      "v_mfma_f32_16x16x32_bf16 v[60:63], a[20:23], a[76:79], v[60:63]\n\t"
      "v_maximum3_f32 v237, v237, v30, v31\n\t"
      "v_mfma_f32_16x16x32_bf16 v[60:63], a[24:27], a[88:91], v[60:63]\n\t"
      "ds_read_b128 a[60:63], v227 offset:36864\n\t"
      "v_maximum3_f32 v238, v238, v34, v35\n\t"
      "v_mfma_f32_16x16x32_bf16 v[60:63], a[28:31], a[100:103], v[60:63]\n\t"
      "v_mfma_f32_16x16x32_bf16 v[64:67], a[16:19], a[68:71], 0\n\t"
      "v_maximum3_f32 v236, v236, v36, v37\n\t"
      "v_mfma_f32_16x16x32_bf16 v[64:67], a[20:23], a[80:83], v[64:67]\n\t"
      "v_maximum3_f32 v237, v237, v40, v41\n\t"
      "v_mfma_f32_16x16x32_bf16 v[64:67], a[24:27], a[92:95], v[64:67]\n\t"
      "v_maximum3_f32 v238, v238, v44, v45\n\t"
      "v_mfma_f32_16x16x32_bf16 v[64:67], a[28:31], a[104:107], v[64:67]\n\t"
      "v_mfma_f32_16x16x32_bf16 v[68:71], a[16:19], a[72:75], 0\n\t"
      "v_maximum3_f32 v236, v236, v38, v39\n\t"
      "v_mfma_f32_16x16x32_bf16 v[68:71], a[20:23], a[84:87], v[68:71]\n\t"
      "v_maximum3_f32 v237, v237, v42, v43\n\t"
      "v_mfma_f32_16x16x32_bf16 v[68:71], a[24:27], a[96:99], v[68:71]\n\t"
      "v_maximum3_f32 v238, v238, v46, v47\n\t"
      "v_mfma_f32_16x16x32_bf16 v[68:71], a[28:31], a[108:111], v[68:71]\n\t"
      "s_waitcnt lgkmcnt(0)\n\t"
      "s_waitcnt vmcnt(29)\n\t"
      "s_barrier\n\t"
      "s_mov_b32 m0, s91\n\t"
      "v_mfma_f32_16x16x32_bf16 v[72:75], a[32:35], a[64:67], 0\n\t"
      "buffer_load_dwordx4 v208, s[44:47], s18 offen lds\n\t"
      "v_xor_b32 v220, 0, v218\n\t"
      "v_xor_b32 v221, 32, v218\n\t"
      "v_mfma_f32_16x16x32_bf16 v[72:75], a[36:39], a[76:79], v[72:75]\n\t"
      "v_xor_b32 v222, 64, v218\n\t"
      "v_xor_b32 v223, 96, v218\n\t"
      "v_mfma_f32_16x16x32_bf16 v[72:75], a[40:43], a[88:91], v[72:75]\n\t"
      "buffer_load_dwordx4 v209, s[44:47], s18 offen offset:1024 lds\n\t"
      "v_xor_b32 v224, 128, v218\n\t"
      "v_xor_b32 v225, 160, v218\n\t"
      "v_mfma_f32_16x16x32_bf16 v[72:75], a[44:47], a[100:103], v[72:75]\n\t"
      "v_xor_b32 v226, 192, v218\n\t"
      "v_xor_b32 v227, 224, v218\n\t"
      "v_mfma_f32_16x16x32_bf16 v[76:79], a[32:35], a[68:71], 0\n\t"
      "buffer_load_dwordx4 v210, s[44:47], s18 offen offset:2048 lds\n\t"
      "v_xor_b32 v228, 0, v218\n\t"
      "v_add_u32 v228, 0x10000, v228\n\t"
      "v_xor_b32 v229, 32, v218\n\t"
      "v_add_u32 v229, 0x10000, v229\n\t"
      "v_mfma_f32_16x16x32_bf16 v[76:79], a[36:39], a[80:83], v[76:79]\n\t"
      "v_xor_b32 v230, 64, v218\n\t"
      "v_add_u32 v230, 0x10000, v230\n\t"
      "v_xor_b32 v231, 96, v218\n\t"
      "v_add_u32 v231, 0x10000, v231\n\t"
      "v_mfma_f32_16x16x32_bf16 v[76:79], a[40:43], a[92:95], v[76:79]\n\t"
      "buffer_load_dwordx4 v211, s[44:47], s18 offen offset:3072 lds\n\t"
      "s_add_u32 s18, s18, 0x4000\n\t"
      "s_and_b32 s18, s18, 0x3ffff\n\t"
      "v_xor_b32 v232, 128, v218\n\t"
      "v_add_u32 v232, 0x10000, v232\n\t"
      "v_xor_b32 v233, 160, v218\n\t"
      "v_add_u32 v233, 0x10000, v233\n\t"
      "v_mfma_f32_16x16x32_bf16 v[76:79], a[44:47], a[104:107], v[76:79]\n\t"
      "ds_read_b64_tr_b16 a[0:1], v220 offset:8192\n\t"
      "v_xor_b32 v234, 192, v218\n\t"
      "v_add_u32 v234, 0x10000, v234\n\t"
      "v_xor_b32 v235, 224, v218\n\t"
      "v_add_u32 v235, 0x10000, v235\n\t"
      "v_mfma_f32_16x16x32_bf16 v[80:83], a[32:35], a[72:75], 0\n\t"
      "ds_read_b64_tr_b16 a[2:3], v220 offset:40960\n\t"
      "v_maximum3_f32 v236, v236, v48, v49\n\t"
      "v_mfma_f32_16x16x32_bf16 v[80:83], a[36:39], a[84:87], v[80:83]\n\t"
      "ds_read_b64_tr_b16 a[4:5], v221 offset:8192\n\t"
      "v_maximum3_f32 v237, v237, v52, v53\n\t"
      "v_mfma_f32_16x16x32_bf16 v[80:83], a[40:43], a[96:99], v[80:83]\n\t"
      "ds_read_b64_tr_b16 a[6:7], v221 offset:40960\n\t"
      "v_maximum3_f32 v238, v238, v56, v57\n\t"
      "v_mfma_f32_16x16x32_bf16 v[80:83], a[44:47], a[108:111], v[80:83]\n\t"
      "ds_read_b64_tr_b16 a[8:9], v222 offset:8192\n\t"
      "v_mfma_f32_16x16x32_bf16 v[84:87], a[48:51], a[64:67], 0\n\t"
      "ds_read_b64_tr_b16 a[10:11], v222 offset:40960\n\t"
      "v_maximum3_f32 v236, v236, v50, v51\n\t"
      "v_mfma_f32_16x16x32_bf16 v[84:87], a[52:55], a[76:79], v[84:87]\n\t"
      "ds_read_b64_tr_b16 a[12:13], v223 offset:8192\n\t"
      "v_maximum3_f32 v237, v237, v54, v55\n\t"
      "v_mfma_f32_16x16x32_bf16 v[84:87], a[56:59], a[88:91], v[84:87]\n\t"
      "ds_read_b64_tr_b16 a[14:15], v223 offset:40960\n\t"
      "v_maximum3_f32 v238, v238, v58, v59\n\t"
      "v_mfma_f32_16x16x32_bf16 v[84:87], a[60:63], a[100:103], v[84:87]\n\t"
      "ds_read_b64_tr_b16 a[16:17], v224 offset:8192\n\t"
      "v_mfma_f32_16x16x32_bf16 v[88:91], a[48:51], a[68:71], 0\n\t"
      "ds_read_b64_tr_b16 a[18:19], v224 offset:40960\n\t"
      "v_maximum3_f32 v236, v236, v60, v61\n\t"
      "v_mfma_f32_16x16x32_bf16 v[88:91], a[52:55], a[80:83], v[88:91]\n\t"
      "ds_read_b64_tr_b16 a[20:21], v225 offset:8192\n\t"
      "v_maximum3_f32 v237, v237, v64, v65\n\t"
      "v_mfma_f32_16x16x32_bf16 v[88:91], a[56:59], a[92:95], v[88:91]\n\t"
      "ds_read_b64_tr_b16 a[22:23], v225 offset:40960\n\t"
      "v_maximum3_f32 v238, v238, v68, v69\n\t"
      "v_mfma_f32_16x16x32_bf16 v[88:91], a[60:63], a[104:107], v[88:91]\n\t"
      "ds_read_b64_tr_b16 a[24:25], v226 offset:8192\n\t"
      "v_mfma_f32_16x16x32_bf16 v[92:95], a[48:51], a[72:75], 0\n\t"
      "ds_read_b64_tr_b16 a[26:27], v226 offset:40960\n\t"
      "v_maximum3_f32 v236, v236, v62, v63\n\t"
      "v_mfma_f32_16x16x32_bf16 v[92:95], a[52:55], a[84:87], v[92:95]\n\t"
      "ds_read_b64_tr_b16 a[28:29], v227 offset:8192\n\t"
      "v_maximum3_f32 v237, v237, v66, v67\n\t"
      "v_mfma_f32_16x16x32_bf16 v[92:95], a[56:59], a[96:99], v[92:95]\n\t"
      "ds_read_b64_tr_b16 a[30:31], v227 offset:40960\n\t"
      "v_maximum3_f32 v238, v238, v70, v71\n\t"
      "v_mfma_f32_16x16x32_bf16 v[92:95], a[60:63], a[108:111], v[92:95]\n\t"
      "ds_read_b64_tr_b16 a[32:33], v228 offset:8192\n\t"
      "ds_read_b64_tr_b16 a[34:35], v228 offset:40960\n\t"
      "ds_read_b64_tr_b16 a[36:37], v229 offset:8192\n\t"
      "ds_read_b64_tr_b16 a[38:39], v229 offset:40960\n\t"
      "ds_read_b64_tr_b16 a[40:41], v230 offset:8192\n\t"
      "v_maximum3_f32 v236, v236, v72, v73\n\t"
      "v_maximum3_f32 v237, v237, v76, v77\n\t"
      "v_maximum3_f32 v238, v238, v80, v81\n\t"
      "v_maximum3_f32 v236, v236, v74, v75\n\t"
      "v_maximum3_f32 v237, v237, v78, v79\n\t"
      "v_maximum3_f32 v238, v238, v82, v83\n\t"
      "v_maximum3_f32 v236, v236, v84, v85\n\t"
      "v_maximum3_f32 v237, v237, v88, v89\n\t"
      "v_maximum3_f32 v238, v238, v92, v93\n\t"
      "v_maximum3_f32 v236, v236, v86, v87\n\t"
      "v_maximum3_f32 v237, v237, v90, v91\n\t"
      "v_maximum3_f32 v238, v238, v94, v95\n\t"
      "v_mov_b32 v246, v236\n\t"
      "v_mov_b32 v247, v237\n\t"
      "v_mov_b32 v248, v238\n\t"
      "v_permlane16_swap_b32 v246, v236\n\t"
      "v_permlane16_swap_b32 v247, v237\n\t"
      "v_permlane16_swap_b32 v248, v238\n\t"
      "v_maximum3_f32 v236, v236, v246, v246\n\t"
      "v_maximum3_f32 v237, v237, v247, v247\n\t"
      "v_maximum3_f32 v238, v238, v248, v248\n\t"
      "v_mov_b32 v246, v236\n\t"
      "v_mov_b32 v247, v237\n\t"
      "v_mov_b32 v248, v238\n\t"
      "v_permlane32_swap_b32 v246, v236\n\t"
      "v_permlane32_swap_b32 v247, v237\n\t"
      "v_permlane32_swap_b32 v248, v238\n\t"
      "v_maximum3_f32 v236, v236, v246, v246\n\t"
      "v_maximum3_f32 v237, v237, v247, v247\n\t"
      "v_maximum3_f32 v238, v238, v248, v248\n\t"
      "v_mul_f32 v240, 0xbe0293ee, v236\n\t"
      "v_mul_f32 v242, 0xbe0293ee, v237\n\t"
      "v_mul_f32 v244, 0xbe0293ee, v238\n\t"
      "v_fma_f32 v246, v241, s26, v240\n\t"
      "v_fma_f32 v248, v243, s26, v242\n\t"
      "v_fma_f32 v252, v249, s26, v244\n\t"
      "v_exp_f32 v246, v246\n\t"
      "v_exp_f32 v248, v248\n\t"
      "v_exp_f32 v252, v252\n\t"
      "v_pk_fma_f32 v[0:1], v[0:1], s[26:27], v[240:241] op_sel_hi:[1,0,0]\n\t"
      "v_pk_fma_f32 v[2:3], v[2:3], s[26:27], v[240:241] op_sel_hi:[1,0,0]\n\t"
      "v_pk_fma_f32 v[12:13], v[12:13], s[26:27], v[240:241] op_sel_hi:[1,0,0]\n\t"
      "v_pk_fma_f32 v[14:15], v[14:15], s[26:27], v[240:241] op_sel_hi:[1,0,0]\n\t"
      "v_exp_f32 v0, v0\n\t"
      "v_exp_f32 v1, v1\n\t"
      "v_exp_f32 v2, v2\n\t"
      "v_exp_f32 v3, v3\n\t"
      "v_exp_f32 v12, v12\n\t"
      "v_exp_f32 v13, v13\n\t"
      "v_exp_f32 v14, v14\n\t"
      "v_exp_f32 v15, v15\n\t"
      "v_pk_fma_f32 v[4:5], v[4:5], s[26:27], v[242:243] op_sel_hi:[1,0,0]\n\t"
      "v_pk_fma_f32 v[6:7], v[6:7], s[26:27], v[242:243] op_sel_hi:[1,0,0]\n\t"
      "v_pk_fma_f32 v[16:17], v[16:17], s[26:27], v[242:243] op_sel_hi:[1,0,0]\n\t"
      "v_pk_fma_f32 v[18:19], v[18:19], s[26:27], v[242:243] op_sel_hi:[1,0,0]\n\t"
      "ds_read_b64_tr_b16 a[42:43], v230 offset:40960\n\t"
      "ds_read_b64_tr_b16 a[44:45], v231 offset:8192\n\t"
      "v_pk_mul_f32 v[192:193], v[192:193], v[246:247] op_sel_hi:[1,0]\n\t"
      "v_pk_mul_f32 v[96:97], v[96:97], v[246:247] op_sel_hi:[1,0]\n\t"
      "v_pk_mul_f32 v[98:99], v[98:99], v[246:247] op_sel_hi:[1,0]\n\t"
      "v_pk_mul_f32 v[108:109], v[108:109], v[246:247] op_sel_hi:[1,0]\n\t"
      "v_pk_mul_f32 v[110:111], v[110:111], v[246:247] op_sel_hi:[1,0]\n\t"
      "v_cvt_pk_bf16_f32 v0, v0, v1\n\t"
      "v_cvt_pk_bf16_f32 v1, v2, v3\n\t"
      "v_cvt_pk_bf16_f32 v2, v12, v13\n\t"
      "v_cvt_pk_bf16_f32 v3, v14, v15\n\t"
      "v_exp_f32 v4, v4\n\t"
      "v_exp_f32 v5, v5\n\t"
      "v_exp_f32 v6, v6\n\t"
      "v_exp_f32 v7, v7\n\t"
      "v_exp_f32 v16, v16\n\t"
      "v_exp_f32 v17, v17\n\t"
      "v_exp_f32 v18, v18\n\t"
      "v_exp_f32 v19, v19\n\t"
      "v_pk_fma_f32 v[8:9], v[8:9], s[26:27], v[244:245] op_sel_hi:[1,0,0]\n\t"
      "v_pk_fma_f32 v[10:11], v[10:11], s[26:27], v[244:245] op_sel_hi:[1,0,0]\n\t"
      "v_pk_fma_f32 v[20:21], v[20:21], s[26:27], v[244:245] op_sel_hi:[1,0,0]\n\t"
      "v_pk_fma_f32 v[22:23], v[22:23], s[26:27], v[244:245] op_sel_hi:[1,0,0]\n\t"
      "ds_read_b64_tr_b16 a[46:47], v231 offset:40960\n\t"
      "ds_read_b64_tr_b16 a[48:49], v232 offset:8192\n\t"
      "v_pk_mul_f32 v[120:121], v[120:121], v[246:247] op_sel_hi:[1,0]\n\t"
      "v_pk_mul_f32 v[122:123], v[122:123], v[246:247] op_sel_hi:[1,0]\n\t"
      "v_pk_mul_f32 v[132:133], v[132:133], v[246:247] op_sel_hi:[1,0]\n\t"
      "v_pk_mul_f32 v[134:135], v[134:135], v[246:247] op_sel_hi:[1,0]\n\t"
      "v_pk_mul_f32 v[144:145], v[144:145], v[246:247] op_sel_hi:[1,0]\n\t"
      "v_cvt_pk_bf16_f32 v4, v4, v5\n\t"
      "v_cvt_pk_bf16_f32 v5, v6, v7\n\t"
      "v_cvt_pk_bf16_f32 v6, v16, v17\n\t"
      "v_cvt_pk_bf16_f32 v7, v18, v19\n\t"
      "v_exp_f32 v8, v8\n\t"
      "v_exp_f32 v9, v9\n\t"
      "v_exp_f32 v10, v10\n\t"
      "v_exp_f32 v11, v11\n\t"
      "v_exp_f32 v20, v20\n\t"
      "v_exp_f32 v21, v21\n\t"
      "v_exp_f32 v22, v22\n\t"
      "v_exp_f32 v23, v23\n\t"
      "v_pk_fma_f32 v[24:25], v[24:25], s[26:27], v[240:241] op_sel_hi:[1,0,0]\n\t"
      "v_pk_fma_f32 v[26:27], v[26:27], s[26:27], v[240:241] op_sel_hi:[1,0,0]\n\t"
      "v_pk_fma_f32 v[36:37], v[36:37], s[26:27], v[240:241] op_sel_hi:[1,0,0]\n\t"
      "v_pk_fma_f32 v[38:39], v[38:39], s[26:27], v[240:241] op_sel_hi:[1,0,0]\n\t"
      "ds_read_b64_tr_b16 a[50:51], v232 offset:40960\n\t"
      "ds_read_b64_tr_b16 a[52:53], v233 offset:8192\n\t"
      "v_pk_mul_f32 v[146:147], v[146:147], v[246:247] op_sel_hi:[1,0]\n\t"
      "v_pk_mul_f32 v[156:157], v[156:157], v[246:247] op_sel_hi:[1,0]\n\t"
      "v_pk_mul_f32 v[158:159], v[158:159], v[246:247] op_sel_hi:[1,0]\n\t"
      "v_pk_mul_f32 v[168:169], v[168:169], v[246:247] op_sel_hi:[1,0]\n\t"
      "v_pk_mul_f32 v[170:171], v[170:171], v[246:247] op_sel_hi:[1,0]\n\t"
      "v_cvt_pk_bf16_f32 v8, v8, v9\n\t"
      "v_cvt_pk_bf16_f32 v9, v10, v11\n\t"
      "v_cvt_pk_bf16_f32 v10, v20, v21\n\t"
      "v_cvt_pk_bf16_f32 v11, v22, v23\n\t"
      "v_exp_f32 v24, v24\n\t"
      "v_exp_f32 v25, v25\n\t"
      "v_exp_f32 v26, v26\n\t"
      "v_exp_f32 v27, v27\n\t"
      "v_exp_f32 v36, v36\n\t"
      "v_exp_f32 v37, v37\n\t"
      "v_exp_f32 v38, v38\n\t"
      "v_exp_f32 v39, v39\n\t"
      "v_pk_fma_f32 v[28:29], v[28:29], s[26:27], v[242:243] op_sel_hi:[1,0,0]\n\t"
      "v_pk_fma_f32 v[30:31], v[30:31], s[26:27], v[242:243] op_sel_hi:[1,0,0]\n\t"
      "v_pk_fma_f32 v[40:41], v[40:41], s[26:27], v[242:243] op_sel_hi:[1,0,0]\n\t"
      "v_pk_fma_f32 v[42:43], v[42:43], s[26:27], v[242:243] op_sel_hi:[1,0,0]\n\t"
      "ds_read_b64_tr_b16 a[54:55], v233 offset:40960\n\t"
      "ds_read_b64_tr_b16 a[56:57], v234 offset:8192\n\t"
      "v_pk_mul_f32 v[180:181], v[180:181], v[246:247] op_sel_hi:[1,0]\n\t"
      "v_pk_mul_f32 v[182:183], v[182:183], v[246:247] op_sel_hi:[1,0]\n\t"
      "v_pk_mul_f32 v[196:197], v[196:197], v[248:249] op_sel_hi:[1,0]\n\t"
      "v_pk_mul_f32 v[100:101], v[100:101], v[248:249] op_sel_hi:[1,0]\n\t"
      "v_pk_mul_f32 v[102:103], v[102:103], v[248:249] op_sel_hi:[1,0]\n\t"
      "v_cvt_pk_bf16_f32 v24, v24, v25\n\t"
      "v_cvt_pk_bf16_f32 v25, v26, v27\n\t"
      "v_cvt_pk_bf16_f32 v26, v36, v37\n\t"
      "v_cvt_pk_bf16_f32 v27, v38, v39\n\t"
      "v_exp_f32 v28, v28\n\t"
      "v_exp_f32 v29, v29\n\t"
      "v_exp_f32 v30, v30\n\t"
      "v_exp_f32 v31, v31\n\t"
      "v_exp_f32 v40, v40\n\t"
      "v_exp_f32 v41, v41\n\t"
      "v_exp_f32 v42, v42\n\t"
      "v_exp_f32 v43, v43\n\t"
      "v_pk_fma_f32 v[32:33], v[32:33], s[26:27], v[244:245] op_sel_hi:[1,0,0]\n\t"
      "v_pk_fma_f32 v[34:35], v[34:35], s[26:27], v[244:245] op_sel_hi:[1,0,0]\n\t"
      "v_pk_fma_f32 v[44:45], v[44:45], s[26:27], v[244:245] op_sel_hi:[1,0,0]\n\t"
      "v_pk_fma_f32 v[46:47], v[46:47], s[26:27], v[244:245] op_sel_hi:[1,0,0]\n\t"
      "ds_read_b64_tr_b16 a[58:59], v234 offset:40960\n\t"
      "ds_read_b64_tr_b16 a[60:61], v235 offset:8192\n\t"
      "v_pk_mul_f32 v[112:113], v[112:113], v[248:249] op_sel_hi:[1,0]\n\t"
      "v_pk_mul_f32 v[114:115], v[114:115], v[248:249] op_sel_hi:[1,0]\n\t"
      "v_pk_mul_f32 v[124:125], v[124:125], v[248:249] op_sel_hi:[1,0]\n\t"
      "v_pk_mul_f32 v[126:127], v[126:127], v[248:249] op_sel_hi:[1,0]\n\t"
      "v_pk_mul_f32 v[136:137], v[136:137], v[248:249] op_sel_hi:[1,0]\n\t"
      "v_cvt_pk_bf16_f32 v28, v28, v29\n\t"
      "v_cvt_pk_bf16_f32 v29, v30, v31\n\t"
      "v_cvt_pk_bf16_f32 v30, v40, v41\n\t"
      "v_cvt_pk_bf16_f32 v31, v42, v43\n\t"
      "v_exp_f32 v32, v32\n\t"
      "v_exp_f32 v33, v33\n\t"
      "v_exp_f32 v34, v34\n\t"
      "v_exp_f32 v35, v35\n\t"
      "v_exp_f32 v44, v44\n\t"
      "v_exp_f32 v45, v45\n\t"
      "v_exp_f32 v46, v46\n\t"
      "v_exp_f32 v47, v47\n\t"
      "v_pk_fma_f32 v[48:49], v[48:49], s[26:27], v[240:241] op_sel_hi:[1,0,0]\n\t"
      "v_pk_fma_f32 v[50:51], v[50:51], s[26:27], v[240:241] op_sel_hi:[1,0,0]\n\t"
      "v_pk_fma_f32 v[60:61], v[60:61], s[26:27], v[240:241] op_sel_hi:[1,0,0]\n\t"
      "v_pk_fma_f32 v[62:63], v[62:63], s[26:27], v[240:241] op_sel_hi:[1,0,0]\n\t"
      "ds_read_b64_tr_b16 a[62:63], v235 offset:40960\n\t"
      "s_waitcnt lgkmcnt(0)\n\t"
      "s_waitcnt vmcnt(27)\n\t"
      "s_barrier\n\t"
      "v_pk_mul_f32 v[138:139], v[138:139], v[248:249] op_sel_hi:[1,0]\n\t"
      "v_pk_mul_f32 v[148:149], v[148:149], v[248:249] op_sel_hi:[1,0]\n\t"
      "v_pk_mul_f32 v[150:151], v[150:151], v[248:249] op_sel_hi:[1,0]\n\t"
      "v_pk_mul_f32 v[160:161], v[160:161], v[248:249] op_sel_hi:[1,0]\n\t"
      "v_pk_mul_f32 v[162:163], v[162:163], v[248:249] op_sel_hi:[1,0]\n\t"
      "v_cvt_pk_bf16_f32 v32, v32, v33\n\t"
      "v_cvt_pk_bf16_f32 v33, v34, v35\n\t"
      "v_cvt_pk_bf16_f32 v34, v44, v45\n\t"
      "v_cvt_pk_bf16_f32 v35, v46, v47\n\t"
      "v_exp_f32 v48, v48\n\t"
      "v_exp_f32 v49, v49\n\t"
      "v_exp_f32 v50, v50\n\t"
      "v_exp_f32 v51, v51\n\t"
      "v_exp_f32 v60, v60\n\t"
      "v_exp_f32 v61, v61\n\t"
      "v_exp_f32 v62, v62\n\t"
      "v_exp_f32 v63, v63\n\t"
      "v_pk_fma_f32 v[52:53], v[52:53], s[26:27], v[242:243] op_sel_hi:[1,0,0]\n\t"
      "v_pk_fma_f32 v[54:55], v[54:55], s[26:27], v[242:243] op_sel_hi:[1,0,0]\n\t"
      "v_pk_fma_f32 v[64:65], v[64:65], s[26:27], v[242:243] op_sel_hi:[1,0,0]\n\t"
      "v_pk_fma_f32 v[66:67], v[66:67], s[26:27], v[242:243] op_sel_hi:[1,0,0]\n\t"
      "s_mov_b32 m0, s92\n\t"
      "s_nop 0\n\t"
      "buffer_load_dwordx4 v208, s[44:47], s18 offen lds\n\t"
      "v_pk_mul_f32 v[172:173], v[172:173], v[248:249] op_sel_hi:[1,0]\n\t"
      "v_pk_mul_f32 v[174:175], v[174:175], v[248:249] op_sel_hi:[1,0]\n\t"
      "v_pk_mul_f32 v[184:185], v[184:185], v[248:249] op_sel_hi:[1,0]\n\t"
      "v_pk_mul_f32 v[186:187], v[186:187], v[248:249] op_sel_hi:[1,0]\n\t"
      "v_pk_mul_f32 v[200:201], v[200:201], v[252:253] op_sel_hi:[1,0]\n\t"
      "v_cvt_pk_bf16_f32 v48, v48, v49\n\t"
      "v_cvt_pk_bf16_f32 v49, v50, v51\n\t"
      "v_cvt_pk_bf16_f32 v50, v60, v61\n\t"
      "v_cvt_pk_bf16_f32 v51, v62, v63\n\t"
      "v_exp_f32 v52, v52\n\t"
      "v_exp_f32 v53, v53\n\t"
      "v_exp_f32 v54, v54\n\t"
      "v_exp_f32 v55, v55\n\t"
      "v_exp_f32 v64, v64\n\t"
      "v_exp_f32 v65, v65\n\t"
      "v_exp_f32 v66, v66\n\t"
      "v_exp_f32 v67, v67\n\t"
      "v_pk_fma_f32 v[56:57], v[56:57], s[26:27], v[244:245] op_sel_hi:[1,0,0]\n\t"
      "v_pk_fma_f32 v[58:59], v[58:59], s[26:27], v[244:245] op_sel_hi:[1,0,0]\n\t"
      "v_pk_fma_f32 v[68:69], v[68:69], s[26:27], v[244:245] op_sel_hi:[1,0,0]\n\t"
      "v_pk_fma_f32 v[70:71], v[70:71], s[26:27], v[244:245] op_sel_hi:[1,0,0]\n\t"
      "buffer_load_dwordx4 v209, s[44:47], s18 offen offset:1024 lds\n\t"
      "buffer_load_dwordx4 v210, s[44:47], s18 offen offset:2048 lds\n\t"
      "v_pk_mul_f32 v[104:105], v[104:105], v[252:253] op_sel_hi:[1,0]\n\t"
      "v_pk_mul_f32 v[106:107], v[106:107], v[252:253] op_sel_hi:[1,0]\n\t"
      "v_pk_mul_f32 v[116:117], v[116:117], v[252:253] op_sel_hi:[1,0]\n\t"
      "v_pk_mul_f32 v[118:119], v[118:119], v[252:253] op_sel_hi:[1,0]\n\t"
      "v_pk_mul_f32 v[128:129], v[128:129], v[252:253] op_sel_hi:[1,0]\n\t"
      "v_cvt_pk_bf16_f32 v52, v52, v53\n\t"
      "v_cvt_pk_bf16_f32 v53, v54, v55\n\t"
      "v_cvt_pk_bf16_f32 v54, v64, v65\n\t"
      "v_cvt_pk_bf16_f32 v55, v66, v67\n\t"
      "v_exp_f32 v56, v56\n\t"
      "v_exp_f32 v57, v57\n\t"
      "v_exp_f32 v58, v58\n\t"
      "v_exp_f32 v59, v59\n\t"
      "v_exp_f32 v68, v68\n\t"
      "v_exp_f32 v69, v69\n\t"
      "v_exp_f32 v70, v70\n\t"
      "v_exp_f32 v71, v71\n\t"
      "v_pk_fma_f32 v[72:73], v[72:73], s[26:27], v[240:241] op_sel_hi:[1,0,0]\n\t"
      "v_pk_fma_f32 v[74:75], v[74:75], s[26:27], v[240:241] op_sel_hi:[1,0,0]\n\t"
      "v_pk_fma_f32 v[84:85], v[84:85], s[26:27], v[240:241] op_sel_hi:[1,0,0]\n\t"
      "v_pk_fma_f32 v[86:87], v[86:87], s[26:27], v[240:241] op_sel_hi:[1,0,0]\n\t"
      "buffer_load_dwordx4 v211, s[44:47], s18 offen offset:3072 lds\n\t"
      "s_add_u32 s18, s18, 0x4000\n\t"
      "s_and_b32 s18, s18, 0x3ffff\n\t"
      "v_pk_mul_f32 v[130:131], v[130:131], v[252:253] op_sel_hi:[1,0]\n\t"
      "v_pk_mul_f32 v[140:141], v[140:141], v[252:253] op_sel_hi:[1,0]\n\t"
      "v_pk_mul_f32 v[142:143], v[142:143], v[252:253] op_sel_hi:[1,0]\n\t"
      "v_pk_mul_f32 v[152:153], v[152:153], v[252:253] op_sel_hi:[1,0]\n\t"
      "v_pk_mul_f32 v[154:155], v[154:155], v[252:253] op_sel_hi:[1,0]\n\t"
      "v_cvt_pk_bf16_f32 v56, v56, v57\n\t"
      "v_cvt_pk_bf16_f32 v57, v58, v59\n\t"
      "v_cvt_pk_bf16_f32 v58, v68, v69\n\t"
      "v_cvt_pk_bf16_f32 v59, v70, v71\n\t"
      "v_exp_f32 v72, v72\n\t"
      "v_exp_f32 v73, v73\n\t"
      "v_exp_f32 v74, v74\n\t"
      "v_exp_f32 v75, v75\n\t"
      "v_exp_f32 v84, v84\n\t"
      "v_exp_f32 v85, v85\n\t"
      "v_exp_f32 v86, v86\n\t"
      "v_exp_f32 v87, v87\n\t"
      "v_pk_fma_f32 v[76:77], v[76:77], s[26:27], v[242:243] op_sel_hi:[1,0,0]\n\t"
      "v_pk_fma_f32 v[78:79], v[78:79], s[26:27], v[242:243] op_sel_hi:[1,0,0]\n\t"
      "v_pk_fma_f32 v[88:89], v[88:89], s[26:27], v[242:243] op_sel_hi:[1,0,0]\n\t"
      "v_pk_fma_f32 v[90:91], v[90:91], s[26:27], v[242:243] op_sel_hi:[1,0,0]\n\t"
      "v_pk_mul_f32 v[164:165], v[164:165], v[252:253] op_sel_hi:[1,0]\n\t"
      "v_pk_mul_f32 v[166:167], v[166:167], v[252:253] op_sel_hi:[1,0]\n\t"
      "v_pk_mul_f32 v[176:177], v[176:177], v[252:253] op_sel_hi:[1,0]\n\t"
      "v_pk_mul_f32 v[178:179], v[178:179], v[252:253] op_sel_hi:[1,0]\n\t"
      "v_pk_mul_f32 v[188:189], v[188:189], v[252:253] op_sel_hi:[1,0]\n\t"
      "v_cvt_pk_bf16_f32 v72, v72, v73\n\t"
      "v_cvt_pk_bf16_f32 v73, v74, v75\n\t"
      "v_cvt_pk_bf16_f32 v74, v84, v85\n\t"
      "v_cvt_pk_bf16_f32 v75, v86, v87\n\t"
      "v_exp_f32 v76, v76\n\t"
      "v_exp_f32 v77, v77\n\t"
      "v_exp_f32 v78, v78\n\t"
      "v_exp_f32 v79, v79\n\t"
      "v_exp_f32 v88, v88\n\t"
      "v_exp_f32 v89, v89\n\t"
      "v_exp_f32 v90, v90\n\t"
      "v_exp_f32 v91, v91\n\t"
      "v_pk_fma_f32 v[80:81], v[80:81], s[26:27], v[244:245] op_sel_hi:[1,0,0]\n\t"
      "v_pk_fma_f32 v[82:83], v[82:83], s[26:27], v[244:245] op_sel_hi:[1,0,0]\n\t"
      "v_pk_fma_f32 v[92:93], v[92:93], s[26:27], v[244:245] op_sel_hi:[1,0,0]\n\t"
      "v_pk_fma_f32 v[94:95], v[94:95], s[26:27], v[244:245] op_sel_hi:[1,0,0]\n\t"
      "v_pk_mul_f32 v[190:191], v[190:191], v[252:253] op_sel_hi:[1,0]\n\t"
      "v_cvt_pk_bf16_f32 v76, v76, v77\n\t"
      "v_cvt_pk_bf16_f32 v77, v78, v79\n\t"
      "v_cvt_pk_bf16_f32 v78, v88, v89\n\t"
      "v_cvt_pk_bf16_f32 v79, v90, v91\n\t"
      "v_exp_f32 v80, v80\n\t"
      "v_exp_f32 v81, v81\n\t"
      "v_exp_f32 v82, v82\n\t"
      "v_exp_f32 v83, v83\n\t"
      "v_exp_f32 v92, v92\n\t"
      "v_exp_f32 v93, v93\n\t"
      "v_exp_f32 v94, v94\n\t"
      "v_exp_f32 v95, v95\n\t"
      "v_cvt_pk_bf16_f32 v80, v80, v81\n\t"
      "v_cvt_pk_bf16_f32 v81, v82, v83\n\t"
      "v_cvt_pk_bf16_f32 v82, v92, v93\n\t"
      "v_cvt_pk_bf16_f32 v83, v94, v95\n\t"
      "v_mfma_f32_16x16x32_bf16 v[192:195], a[208:211], v[0:3], v[192:195]\n\t"
      "v_mfma_f32_16x16x32_bf16 v[196:199], a[208:211], v[4:7], v[196:199]\n\t"
      "v_mfma_f32_16x16x32_bf16 v[200:203], a[208:211], v[8:11], v[200:203]\n\t"
      "v_mfma_f32_16x16x32_bf16 v[96:99], a[0:3], v[0:3], v[96:99]\n\t"
      "v_mfma_f32_16x16x32_bf16 v[100:103], a[0:3], v[4:7], v[100:103]\n\t"
      "v_mfma_f32_16x16x32_bf16 v[104:107], a[0:3], v[8:11], v[104:107]\n\t"
      "v_mfma_f32_16x16x32_bf16 v[108:111], a[4:7], v[0:3], v[108:111]\n\t"
      "v_mfma_f32_16x16x32_bf16 v[112:115], a[4:7], v[4:7], v[112:115]\n\t"
      "v_mfma_f32_16x16x32_bf16 v[116:119], a[4:7], v[8:11], v[116:119]\n\t"
      "v_mfma_f32_16x16x32_bf16 v[120:123], a[8:11], v[0:3], v[120:123]\n\t"
      "v_mfma_f32_16x16x32_bf16 v[124:127], a[8:11], v[4:7], v[124:127]\n\t"
      "v_mfma_f32_16x16x32_bf16 v[128:131], a[8:11], v[8:11], v[128:131]\n\t"
      "v_mfma_f32_16x16x32_bf16 v[132:135], a[12:15], v[0:3], v[132:135]\n\t"
      "v_mfma_f32_16x16x32_bf16 v[136:139], a[12:15], v[4:7], v[136:139]\n\t"
      "v_mfma_f32_16x16x32_bf16 v[140:143], a[12:15], v[8:11], v[140:143]\n\t"
      "v_mfma_f32_16x16x32_bf16 v[144:147], a[16:19], v[0:3], v[144:147]\n\t"
      "v_mfma_f32_16x16x32_bf16 v[148:151], a[16:19], v[4:7], v[148:151]\n\t"
      "v_mfma_f32_16x16x32_bf16 v[152:155], a[16:19], v[8:11], v[152:155]\n\t"
      "v_mfma_f32_16x16x32_bf16 v[156:159], a[20:23], v[0:3], v[156:159]\n\t"
      "v_mfma_f32_16x16x32_bf16 v[160:163], a[20:23], v[4:7], v[160:163]\n\t"
      "v_mfma_f32_16x16x32_bf16 v[164:167], a[20:23], v[8:11], v[164:167]\n\t"
      "v_mfma_f32_16x16x32_bf16 v[168:171], a[24:27], v[0:3], v[168:171]\n\t"
      "v_mfma_f32_16x16x32_bf16 v[172:175], a[24:27], v[4:7], v[172:175]\n\t"
      "v_mfma_f32_16x16x32_bf16 v[176:179], a[24:27], v[8:11], v[176:179]\n\t"
      "v_mfma_f32_16x16x32_bf16 v[180:183], a[28:31], v[0:3], v[180:183]\n\t"
      "v_mfma_f32_16x16x32_bf16 v[184:187], a[28:31], v[4:7], v[184:187]\n\t"
      "v_mfma_f32_16x16x32_bf16 v[188:191], a[28:31], v[8:11], v[188:191]\n\t"
      "v_mfma_f32_16x16x32_bf16 v[192:195], a[208:211], v[24:27], v[192:195]\n\t"
      "ds_read_b64_tr_b16 a[0:1], v220 offset:12288\n\t"
      "v_mfma_f32_16x16x32_bf16 v[196:199], a[208:211], v[28:31], v[196:199]\n\t"
      "ds_read_b64_tr_b16 a[2:3], v220 offset:45056\n\t"
      "v_mfma_f32_16x16x32_bf16 v[200:203], a[208:211], v[32:35], v[200:203]\n\t"
      "ds_read_b64_tr_b16 a[4:5], v221 offset:12288\n\t"
      "v_mfma_f32_16x16x32_bf16 v[96:99], a[32:35], v[24:27], v[96:99]\n\t"
      "v_mfma_f32_16x16x32_bf16 v[100:103], a[32:35], v[28:31], v[100:103]\n\t"
      "ds_read_b64_tr_b16 a[6:7], v221 offset:45056\n\t"
      "v_mfma_f32_16x16x32_bf16 v[104:107], a[32:35], v[32:35], v[104:107]\n\t"
      "ds_read_b64_tr_b16 a[8:9], v222 offset:12288\n\t"
      "v_mfma_f32_16x16x32_bf16 v[108:111], a[36:39], v[24:27], v[108:111]\n\t"
      "v_mfma_f32_16x16x32_bf16 v[112:115], a[36:39], v[28:31], v[112:115]\n\t"
      "ds_read_b64_tr_b16 a[10:11], v222 offset:45056\n\t"
      "v_mfma_f32_16x16x32_bf16 v[116:119], a[36:39], v[32:35], v[116:119]\n\t"
      "ds_read_b64_tr_b16 a[12:13], v223 offset:12288\n\t"
      "v_mfma_f32_16x16x32_bf16 v[120:123], a[40:43], v[24:27], v[120:123]\n\t"
      "v_mfma_f32_16x16x32_bf16 v[124:127], a[40:43], v[28:31], v[124:127]\n\t"
      "ds_read_b64_tr_b16 a[14:15], v223 offset:45056\n\t"
      "v_mfma_f32_16x16x32_bf16 v[128:131], a[40:43], v[32:35], v[128:131]\n\t"
      "ds_read_b64_tr_b16 a[16:17], v224 offset:12288\n\t"
      "v_mfma_f32_16x16x32_bf16 v[132:135], a[44:47], v[24:27], v[132:135]\n\t"
      "ds_read_b64_tr_b16 a[18:19], v224 offset:45056\n\t"
      "v_mfma_f32_16x16x32_bf16 v[136:139], a[44:47], v[28:31], v[136:139]\n\t"
      "v_mfma_f32_16x16x32_bf16 v[140:143], a[44:47], v[32:35], v[140:143]\n\t"
      "ds_read_b64_tr_b16 a[20:21], v225 offset:12288\n\t"
      "v_mfma_f32_16x16x32_bf16 v[144:147], a[48:51], v[24:27], v[144:147]\n\t"
      "ds_read_b64_tr_b16 a[22:23], v225 offset:45056\n\t"
      "v_mfma_f32_16x16x32_bf16 v[148:151], a[48:51], v[28:31], v[148:151]\n\t"
      "v_mfma_f32_16x16x32_bf16 v[152:155], a[48:51], v[32:35], v[152:155]\n\t"
      "ds_read_b64_tr_b16 a[24:25], v226 offset:12288\n\t"
      "v_mfma_f32_16x16x32_bf16 v[156:159], a[52:55], v[24:27], v[156:159]\n\t"
      "ds_read_b64_tr_b16 a[26:27], v226 offset:45056\n\t"
      "v_mfma_f32_16x16x32_bf16 v[160:163], a[52:55], v[28:31], v[160:163]\n\t"
      "v_mfma_f32_16x16x32_bf16 v[164:167], a[52:55], v[32:35], v[164:167]\n\t"
      "ds_read_b64_tr_b16 a[28:29], v227 offset:12288\n\t"
      "v_mfma_f32_16x16x32_bf16 v[168:171], a[56:59], v[24:27], v[168:171]\n\t"
      "ds_read_b64_tr_b16 a[30:31], v227 offset:45056\n\t"
      "v_mfma_f32_16x16x32_bf16 v[172:175], a[56:59], v[28:31], v[172:175]\n\t"
      "v_mfma_f32_16x16x32_bf16 v[176:179], a[56:59], v[32:35], v[176:179]\n\t"
      "v_mfma_f32_16x16x32_bf16 v[180:183], a[60:63], v[24:27], v[180:183]\n\t"
      "v_mfma_f32_16x16x32_bf16 v[184:187], a[60:63], v[28:31], v[184:187]\n\t"
      "v_mfma_f32_16x16x32_bf16 v[188:191], a[60:63], v[32:35], v[188:191]\n\t"
      "s_waitcnt lgkmcnt(0)\n\t"
      "v_mfma_f32_16x16x32_bf16 v[192:195], a[208:211], v[48:51], v[192:195]\n\t"
      "ds_read_b64_tr_b16 a[32:33], v228 offset:12288\n\t"
      "v_mfma_f32_16x16x32_bf16 v[196:199], a[208:211], v[52:55], v[196:199]\n\t"
      "ds_read_b64_tr_b16 a[34:35], v228 offset:45056\n\t"
      "v_mfma_f32_16x16x32_bf16 v[200:203], a[208:211], v[56:59], v[200:203]\n\t"
      "ds_read_b64_tr_b16 a[36:37], v229 offset:12288\n\t"
      "v_mfma_f32_16x16x32_bf16 v[96:99], a[0:3], v[48:51], v[96:99]\n\t"
      "v_mfma_f32_16x16x32_bf16 v[100:103], a[0:3], v[52:55], v[100:103]\n\t"
      "ds_read_b64_tr_b16 a[38:39], v229 offset:45056\n\t"
      "v_mfma_f32_16x16x32_bf16 v[104:107], a[0:3], v[56:59], v[104:107]\n\t"
      "ds_read_b64_tr_b16 a[40:41], v230 offset:12288\n\t"
      "v_mfma_f32_16x16x32_bf16 v[108:111], a[4:7], v[48:51], v[108:111]\n\t"
      "v_mfma_f32_16x16x32_bf16 v[112:115], a[4:7], v[52:55], v[112:115]\n\t"
      "ds_read_b64_tr_b16 a[42:43], v230 offset:45056\n\t"
      "v_mfma_f32_16x16x32_bf16 v[116:119], a[4:7], v[56:59], v[116:119]\n\t"
      "ds_read_b64_tr_b16 a[44:45], v231 offset:12288\n\t"
      "v_mfma_f32_16x16x32_bf16 v[120:123], a[8:11], v[48:51], v[120:123]\n\t"
      "v_mfma_f32_16x16x32_bf16 v[124:127], a[8:11], v[52:55], v[124:127]\n\t"
      "ds_read_b64_tr_b16 a[46:47], v231 offset:45056\n\t"
      "v_mfma_f32_16x16x32_bf16 v[128:131], a[8:11], v[56:59], v[128:131]\n\t"
      "ds_read_b64_tr_b16 a[48:49], v232 offset:12288\n\t"
      "v_mfma_f32_16x16x32_bf16 v[132:135], a[12:15], v[48:51], v[132:135]\n\t"
      "ds_read_b64_tr_b16 a[50:51], v232 offset:45056\n\t"
      "v_mfma_f32_16x16x32_bf16 v[136:139], a[12:15], v[52:55], v[136:139]\n\t"
      "v_mfma_f32_16x16x32_bf16 v[140:143], a[12:15], v[56:59], v[140:143]\n\t"
      "ds_read_b64_tr_b16 a[52:53], v233 offset:12288\n\t"
      "v_mfma_f32_16x16x32_bf16 v[144:147], a[16:19], v[48:51], v[144:147]\n\t"
      "ds_read_b64_tr_b16 a[54:55], v233 offset:45056\n\t"
      "v_mfma_f32_16x16x32_bf16 v[148:151], a[16:19], v[52:55], v[148:151]\n\t"
      "v_mfma_f32_16x16x32_bf16 v[152:155], a[16:19], v[56:59], v[152:155]\n\t"
      "ds_read_b64_tr_b16 a[56:57], v234 offset:12288\n\t"
      "v_mfma_f32_16x16x32_bf16 v[156:159], a[20:23], v[48:51], v[156:159]\n\t"
      "ds_read_b64_tr_b16 a[58:59], v234 offset:45056\n\t"
      "v_mfma_f32_16x16x32_bf16 v[160:163], a[20:23], v[52:55], v[160:163]\n\t"
      "v_mfma_f32_16x16x32_bf16 v[164:167], a[20:23], v[56:59], v[164:167]\n\t"
      "ds_read_b64_tr_b16 a[60:61], v235 offset:12288\n\t"
      "v_mfma_f32_16x16x32_bf16 v[168:171], a[24:27], v[48:51], v[168:171]\n\t"
      "ds_read_b64_tr_b16 a[62:63], v235 offset:45056\n\t"
      "v_mfma_f32_16x16x32_bf16 v[172:175], a[24:27], v[52:55], v[172:175]\n\t"
      "v_mfma_f32_16x16x32_bf16 v[176:179], a[24:27], v[56:59], v[176:179]\n\t"
      "v_mfma_f32_16x16x32_bf16 v[180:183], a[28:31], v[48:51], v[180:183]\n\t"
      "v_mfma_f32_16x16x32_bf16 v[184:187], a[28:31], v[52:55], v[184:187]\n\t"
      "v_mfma_f32_16x16x32_bf16 v[188:191], a[28:31], v[56:59], v[188:191]\n\t"
      "s_waitcnt lgkmcnt(0)\n\t"
      "s_waitcnt vmcnt(26)\n\t"
      "s_barrier\n\t"
      "s_mov_b32 m0, s93\n\t"
      "v_mfma_f32_16x16x32_bf16 v[192:195], a[208:211], v[72:75], v[192:195]\n\t"
      "buffer_load_dwordx4 v208, s[44:47], s18 offen lds\n\t"
      "v_xor_b32 v220, 0, v217\n\t"
      "v_xor_b32 v221, 64, v217\n\t"
      "v_mfma_f32_16x16x32_bf16 v[196:199], a[208:211], v[76:79], v[196:199]\n\t"
      "v_xor_b32 v222, 128, v217\n\t"
      "v_mfma_f32_16x16x32_bf16 v[200:203], a[208:211], v[80:83], v[200:203]\n\t"
      "v_xor_b32 v223, 192, v217\n\t"
      "v_mfma_f32_16x16x32_bf16 v[96:99], a[32:35], v[72:75], v[96:99]\n\t"
      "v_xor_b32 v224, 0, v217\n\t"
      "v_add_u32 v224, 0x10000, v224\n\t"
      "v_mfma_f32_16x16x32_bf16 v[100:103], a[32:35], v[76:79], v[100:103]\n\t"
      "buffer_load_dwordx4 v209, s[44:47], s18 offen offset:1024 lds\n\t"
      "v_xor_b32 v225, 64, v217\n\t"
      "v_add_u32 v225, 0x10000, v225\n\t"
      "v_mfma_f32_16x16x32_bf16 v[104:107], a[32:35], v[80:83], v[104:107]\n\t"
      "v_xor_b32 v226, 128, v217\n\t"
      "v_add_u32 v226, 0x10000, v226\n\t"
      "v_mfma_f32_16x16x32_bf16 v[108:111], a[36:39], v[72:75], v[108:111]\n\t"
      "v_xor_b32 v227, 192, v217\n\t"
      "v_add_u32 v227, 0x10000, v227\n\t"
      "v_mfma_f32_16x16x32_bf16 v[112:115], a[36:39], v[76:79], v[112:115]\n\t"
      "ds_read_b128 a[0:3], v220 offset:16384\n\t"
      "v_mfma_f32_16x16x32_bf16 v[116:119], a[36:39], v[80:83], v[116:119]\n\t"
      "v_mfma_f32_16x16x32_bf16 v[120:123], a[40:43], v[72:75], v[120:123]\n\t"
      "ds_read_b128 a[4:7], v221 offset:16384\n\t"
      "v_mfma_f32_16x16x32_bf16 v[124:127], a[40:43], v[76:79], v[124:127]\n\t"
      "v_mfma_f32_16x16x32_bf16 v[128:131], a[40:43], v[80:83], v[128:131]\n\t"
      "ds_read_b128 a[8:11], v222 offset:16384\n\t"
      "v_mfma_f32_16x16x32_bf16 v[132:135], a[44:47], v[72:75], v[132:135]\n\t"
      "buffer_load_dwordx4 v210, s[44:47], s18 offen offset:2048 lds\n\t"
      "v_mfma_f32_16x16x32_bf16 v[136:139], a[44:47], v[76:79], v[136:139]\n\t"
      "ds_read_b128 a[12:15], v223 offset:16384\n\t"
      "v_mfma_f32_16x16x32_bf16 v[140:143], a[44:47], v[80:83], v[140:143]\n\t"
      "v_mfma_f32_16x16x32_bf16 v[144:147], a[48:51], v[72:75], v[144:147]\n\t"
      "ds_read_b128 a[16:19], v220 offset:49152\n\t"
      "v_mfma_f32_16x16x32_bf16 v[148:151], a[48:51], v[76:79], v[148:151]\n\t"
      "v_mfma_f32_16x16x32_bf16 v[152:155], a[48:51], v[80:83], v[152:155]\n\t"
      "ds_read_b128 a[20:23], v221 offset:49152\n\t"
      "v_mfma_f32_16x16x32_bf16 v[156:159], a[52:55], v[72:75], v[156:159]\n\t"
      "v_mfma_f32_16x16x32_bf16 v[160:163], a[52:55], v[76:79], v[160:163]\n\t"
      "ds_read_b128 a[24:27], v222 offset:49152\n\t"
      "v_mfma_f32_16x16x32_bf16 v[164:167], a[52:55], v[80:83], v[164:167]\n\t"
      "v_mfma_f32_16x16x32_bf16 v[168:171], a[56:59], v[72:75], v[168:171]\n\t"
      "ds_read_b128 a[28:31], v223 offset:49152\n\t"
      "v_mfma_f32_16x16x32_bf16 v[172:175], a[56:59], v[76:79], v[172:175]\n\t"
      "buffer_load_dwordx4 v211, s[44:47], s18 offen offset:3072 lds\n\t"
      "s_add_u32 s18, s18, 0x4000\n\t"
      "s_and_b32 s18, s18, 0x3ffff\n\t"
      "v_mfma_f32_16x16x32_bf16 v[176:179], a[56:59], v[80:83], v[176:179]\n\t"
      "v_mfma_f32_16x16x32_bf16 v[180:183], a[60:63], v[72:75], v[180:183]\n\t"
      "v_mfma_f32_16x16x32_bf16 v[184:187], a[60:63], v[76:79], v[184:187]\n\t"
      "v_mfma_f32_16x16x32_bf16 v[188:191], a[60:63], v[80:83], v[188:191]\n\t"
      "s_waitcnt lgkmcnt(0)\n\t"
      "v_mfma_f32_16x16x32_bf16 v[0:3], a[0:3], a[64:67], 0\n\t"
      "ds_read_b128 a[32:35], v224 offset:16384\n\t"
      "v_mfma_f32_16x16x32_bf16 v[0:3], a[4:7], a[76:79], v[0:3]\n\t"
      "v_mfma_f32_16x16x32_bf16 v[0:3], a[8:11], a[88:91], v[0:3]\n\t"
      "ds_read_b128 a[36:39], v225 offset:16384\n\t"
      "v_mov_b32 v241, v236\n\t"
      "v_mfma_f32_16x16x32_bf16 v[0:3], a[12:15], a[100:103], v[0:3]\n\t"
      "v_mov_b32 v243, v237\n\t"
      "v_mfma_f32_16x16x32_bf16 v[4:7], a[0:3], a[68:71], 0\n\t"
      "ds_read_b128 a[40:43], v226 offset:16384\n\t"
      "v_mov_b32 v249, v238\n\t"
      "v_mfma_f32_16x16x32_bf16 v[4:7], a[4:7], a[80:83], v[4:7]\n\t"
      "v_mfma_f32_16x16x32_bf16 v[4:7], a[8:11], a[92:95], v[4:7]\n\t"
      "ds_read_b128 a[44:47], v227 offset:16384\n\t"
      "v_mfma_f32_16x16x32_bf16 v[4:7], a[12:15], a[104:107], v[4:7]\n\t"
      "v_mfma_f32_16x16x32_bf16 v[8:11], a[0:3], a[72:75], 0\n\t"
      "ds_read_b128 a[48:51], v224 offset:49152\n\t"
      "v_mfma_f32_16x16x32_bf16 v[8:11], a[4:7], a[84:87], v[8:11]\n\t"
      "v_mfma_f32_16x16x32_bf16 v[8:11], a[8:11], a[96:99], v[8:11]\n\t"
      "ds_read_b128 a[52:55], v225 offset:49152\n\t"
      "v_mfma_f32_16x16x32_bf16 v[8:11], a[12:15], a[108:111], v[8:11]\n\t"
      "v_mfma_f32_16x16x32_bf16 v[12:15], a[16:19], a[64:67], 0\n\t"
      "ds_read_b128 a[56:59], v226 offset:49152\n\t"
      "v_mfma_f32_16x16x32_bf16 v[12:15], a[20:23], a[76:79], v[12:15]\n\t"
      "v_mfma_f32_16x16x32_bf16 v[12:15], a[24:27], a[88:91], v[12:15]\n\t"
      "ds_read_b128 a[60:63], v227 offset:49152\n\t"
      "v_mfma_f32_16x16x32_bf16 v[12:15], a[28:31], a[100:103], v[12:15]\n\t"
      "v_mfma_f32_16x16x32_bf16 v[16:19], a[16:19], a[68:71], 0\n\t"
      "v_mfma_f32_16x16x32_bf16 v[16:19], a[20:23], a[80:83], v[16:19]\n\t"
      "v_mfma_f32_16x16x32_bf16 v[16:19], a[24:27], a[92:95], v[16:19]\n\t"
      "v_mfma_f32_16x16x32_bf16 v[16:19], a[28:31], a[104:107], v[16:19]\n\t"
      "v_mfma_f32_16x16x32_bf16 v[20:23], a[16:19], a[72:75], 0\n\t"
      "v_mfma_f32_16x16x32_bf16 v[20:23], a[20:23], a[84:87], v[20:23]\n\t"
      "v_mfma_f32_16x16x32_bf16 v[20:23], a[24:27], a[96:99], v[20:23]\n\t"
      "v_mfma_f32_16x16x32_bf16 v[20:23], a[28:31], a[108:111], v[20:23]\n\t"
      "s_waitcnt lgkmcnt(0)\n\t"
      "s_waitcnt vmcnt(25)\n\t"
      "s_barrier\n\t"
      "v_mfma_f32_16x16x32_bf16 v[24:27], a[32:35], a[64:67], 0\n\t"
      "ds_read_b128 a[0:3], v220 offset:20480\n\t"
      "s_mov_b32 m0, s94\n\t"
      "v_mfma_f32_16x16x32_bf16 v[24:27], a[36:39], a[76:79], v[24:27]\n\t"
      "buffer_load_dwordx4 v208, s[44:47], s18 offen lds\n\t"
      "v_mfma_f32_16x16x32_bf16 v[24:27], a[40:43], a[88:91], v[24:27]\n\t"
      "ds_read_b128 a[4:7], v221 offset:20480\n\t"
      "v_mfma_f32_16x16x32_bf16 v[24:27], a[44:47], a[100:103], v[24:27]\n\t"
      "v_mfma_f32_16x16x32_bf16 v[28:31], a[32:35], a[68:71], 0\n\t"
      "ds_read_b128 a[8:11], v222 offset:20480\n\t"
      "v_mfma_f32_16x16x32_bf16 v[28:31], a[36:39], a[80:83], v[28:31]\n\t"
      "v_mfma_f32_16x16x32_bf16 v[28:31], a[40:43], a[92:95], v[28:31]\n\t"
      "ds_read_b128 a[12:15], v223 offset:20480\n\t"
      "v_mfma_f32_16x16x32_bf16 v[28:31], a[44:47], a[104:107], v[28:31]\n\t"
      "v_mfma_f32_16x16x32_bf16 v[32:35], a[32:35], a[72:75], 0\n\t"
      "ds_read_b128 a[16:19], v220 offset:53248\n\t"
      "v_maximum3_f32 v236, v236, v0, v1\n\t"
      "v_mfma_f32_16x16x32_bf16 v[32:35], a[36:39], a[84:87], v[32:35]\n\t"
      "buffer_load_dwordx4 v209, s[44:47], s18 offen offset:1024 lds\n\t"
      "v_maximum3_f32 v237, v237, v4, v5\n\t"
      "v_mfma_f32_16x16x32_bf16 v[32:35], a[40:43], a[96:99], v[32:35]\n\t"
      "ds_read_b128 a[20:23], v221 offset:53248\n\t"
      "v_maximum3_f32 v238, v238, v8, v9\n\t"
      "v_mfma_f32_16x16x32_bf16 v[32:35], a[44:47], a[108:111], v[32:35]\n\t"
      "v_mfma_f32_16x16x32_bf16 v[36:39], a[48:51], a[64:67], 0\n\t"
      "ds_read_b128 a[24:27], v222 offset:53248\n\t"
      "v_maximum3_f32 v236, v236, v2, v3\n\t"
      "v_mfma_f32_16x16x32_bf16 v[36:39], a[52:55], a[76:79], v[36:39]\n\t"
      "v_maximum3_f32 v237, v237, v6, v7\n\t"
      "v_mfma_f32_16x16x32_bf16 v[36:39], a[56:59], a[88:91], v[36:39]\n\t"
      "ds_read_b128 a[28:31], v223 offset:53248\n\t"
      "v_maximum3_f32 v238, v238, v10, v11\n\t"
      "v_mfma_f32_16x16x32_bf16 v[36:39], a[60:63], a[100:103], v[36:39]\n\t"
      "v_mfma_f32_16x16x32_bf16 v[40:43], a[48:51], a[68:71], 0\n\t"
      "buffer_load_dwordx4 v210, s[44:47], s18 offen offset:2048 lds\n\t"
      "v_maximum3_f32 v236, v236, v12, v13\n\t"
      "v_mfma_f32_16x16x32_bf16 v[40:43], a[52:55], a[80:83], v[40:43]\n\t"
      "v_maximum3_f32 v237, v237, v16, v17\n\t"
      "v_mfma_f32_16x16x32_bf16 v[40:43], a[56:59], a[92:95], v[40:43]\n\t"
      "v_maximum3_f32 v238, v238, v20, v21\n\t"
      "v_mfma_f32_16x16x32_bf16 v[40:43], a[60:63], a[104:107], v[40:43]\n\t"
      "v_mfma_f32_16x16x32_bf16 v[44:47], a[48:51], a[72:75], 0\n\t"
      "buffer_load_dwordx4 v211, s[44:47], s18 offen offset:3072 lds\n\t"
      "s_add_u32 s18, s18, 0x4000\n\t"
      "s_and_b32 s18, s18, 0x3ffff\n\t"
      "v_maximum3_f32 v236, v236, v14, v15\n\t"
      "v_mfma_f32_16x16x32_bf16 v[44:47], a[52:55], a[84:87], v[44:47]\n\t"
      "v_maximum3_f32 v237, v237, v18, v19\n\t"
      "v_mfma_f32_16x16x32_bf16 v[44:47], a[56:59], a[96:99], v[44:47]\n\t"
      "v_maximum3_f32 v238, v238, v22, v23\n\t"
      "v_mfma_f32_16x16x32_bf16 v[44:47], a[60:63], a[108:111], v[44:47]\n\t"
      "s_waitcnt lgkmcnt(0)\n\t"
      "v_mfma_f32_16x16x32_bf16 v[48:51], a[0:3], a[64:67], 0\n\t"
      "ds_read_b128 a[32:35], v224 offset:20480\n\t"
      "v_mfma_f32_16x16x32_bf16 v[48:51], a[4:7], a[76:79], v[48:51]\n\t"
      "v_mfma_f32_16x16x32_bf16 v[48:51], a[8:11], a[88:91], v[48:51]\n\t"
      "ds_read_b128 a[36:39], v225 offset:20480\n\t"
      "v_mfma_f32_16x16x32_bf16 v[48:51], a[12:15], a[100:103], v[48:51]\n\t"
      "v_mfma_f32_16x16x32_bf16 v[52:55], a[0:3], a[68:71], 0\n\t"
      "ds_read_b128 a[40:43], v226 offset:20480\n\t"
      "v_mfma_f32_16x16x32_bf16 v[52:55], a[4:7], a[80:83], v[52:55]\n\t"
      "v_mfma_f32_16x16x32_bf16 v[52:55], a[8:11], a[92:95], v[52:55]\n\t"
      "ds_read_b128 a[44:47], v227 offset:20480\n\t"
      "v_mfma_f32_16x16x32_bf16 v[52:55], a[12:15], a[104:107], v[52:55]\n\t"
      "v_mfma_f32_16x16x32_bf16 v[56:59], a[0:3], a[72:75], 0\n\t"
      "ds_read_b128 a[48:51], v224 offset:53248\n\t"
      "v_maximum3_f32 v236, v236, v24, v25\n\t"
      "v_mfma_f32_16x16x32_bf16 v[56:59], a[4:7], a[84:87], v[56:59]\n\t"
      "v_maximum3_f32 v237, v237, v28, v29\n\t"
      "v_mfma_f32_16x16x32_bf16 v[56:59], a[8:11], a[96:99], v[56:59]\n\t"
      "ds_read_b128 a[52:55], v225 offset:53248\n\t"
      "v_maximum3_f32 v238, v238, v32, v33\n\t"
      "v_mfma_f32_16x16x32_bf16 v[56:59], a[12:15], a[108:111], v[56:59]\n\t"
      "v_mfma_f32_16x16x32_bf16 v[60:63], a[16:19], a[64:67], 0\n\t"
      "ds_read_b128 a[56:59], v226 offset:53248\n\t"
      "v_maximum3_f32 v236, v236, v26, v27\n\t"
      "v_mfma_f32_16x16x32_bf16 v[60:63], a[20:23], a[76:79], v[60:63]\n\t"
      "v_maximum3_f32 v237, v237, v30, v31\n\t"
      "v_mfma_f32_16x16x32_bf16 v[60:63], a[24:27], a[88:91], v[60:63]\n\t"
      "ds_read_b128 a[60:63], v227 offset:53248\n\t"
      "v_maximum3_f32 v238, v238, v34, v35\n\t"
      "v_mfma_f32_16x16x32_bf16 v[60:63], a[28:31], a[100:103], v[60:63]\n\t"
      "v_mfma_f32_16x16x32_bf16 v[64:67], a[16:19], a[68:71], 0\n\t"
      "v_maximum3_f32 v236, v236, v36, v37\n\t"
      "v_mfma_f32_16x16x32_bf16 v[64:67], a[20:23], a[80:83], v[64:67]\n\t"
      "v_maximum3_f32 v237, v237, v40, v41\n\t"
      "v_mfma_f32_16x16x32_bf16 v[64:67], a[24:27], a[92:95], v[64:67]\n\t"
      "v_maximum3_f32 v238, v238, v44, v45\n\t"
      "v_mfma_f32_16x16x32_bf16 v[64:67], a[28:31], a[104:107], v[64:67]\n\t"
      "v_mfma_f32_16x16x32_bf16 v[68:71], a[16:19], a[72:75], 0\n\t"
      "v_maximum3_f32 v236, v236, v38, v39\n\t"
      "v_mfma_f32_16x16x32_bf16 v[68:71], a[20:23], a[84:87], v[68:71]\n\t"
      "v_maximum3_f32 v237, v237, v42, v43\n\t"
      "v_mfma_f32_16x16x32_bf16 v[68:71], a[24:27], a[96:99], v[68:71]\n\t"
      "v_maximum3_f32 v238, v238, v46, v47\n\t"
      "v_mfma_f32_16x16x32_bf16 v[68:71], a[28:31], a[108:111], v[68:71]\n\t"
      "s_waitcnt lgkmcnt(0)\n\t"
      "s_waitcnt vmcnt(25)\n\t"
      "s_barrier\n\t"
      "s_mov_b32 m0, s95\n\t"
      "v_mfma_f32_16x16x32_bf16 v[72:75], a[32:35], a[64:67], 0\n\t"
      "buffer_load_dwordx4 v208, s[44:47], s18 offen lds\n\t"
      "v_xor_b32 v220, 0, v218\n\t"
      "v_xor_b32 v221, 32, v218\n\t"
      "v_mfma_f32_16x16x32_bf16 v[72:75], a[36:39], a[76:79], v[72:75]\n\t"
      "v_xor_b32 v222, 64, v218\n\t"
      "v_xor_b32 v223, 96, v218\n\t"
      "v_mfma_f32_16x16x32_bf16 v[72:75], a[40:43], a[88:91], v[72:75]\n\t"
      "buffer_load_dwordx4 v209, s[44:47], s18 offen offset:1024 lds\n\t"
      "v_xor_b32 v224, 128, v218\n\t"
      "v_xor_b32 v225, 160, v218\n\t"
      "v_mfma_f32_16x16x32_bf16 v[72:75], a[44:47], a[100:103], v[72:75]\n\t"
      "v_xor_b32 v226, 192, v218\n\t"
      "v_xor_b32 v227, 224, v218\n\t"
      "v_mfma_f32_16x16x32_bf16 v[76:79], a[32:35], a[68:71], 0\n\t"
      "buffer_load_dwordx4 v210, s[44:47], s18 offen offset:2048 lds\n\t"
      "v_xor_b32 v228, 0, v218\n\t"
      "v_add_u32 v228, 0x10000, v228\n\t"
      "v_xor_b32 v229, 32, v218\n\t"
      "v_add_u32 v229, 0x10000, v229\n\t"
      "v_mfma_f32_16x16x32_bf16 v[76:79], a[36:39], a[80:83], v[76:79]\n\t"
      "v_xor_b32 v230, 64, v218\n\t"
      "v_add_u32 v230, 0x10000, v230\n\t"
      "v_xor_b32 v231, 96, v218\n\t"
      "v_add_u32 v231, 0x10000, v231\n\t"
      "v_mfma_f32_16x16x32_bf16 v[76:79], a[40:43], a[92:95], v[76:79]\n\t"
      "buffer_load_dwordx4 v211, s[44:47], s18 offen offset:3072 lds\n\t"
      "s_add_u32 s18, s18, 0x4000\n\t"
      "s_and_b32 s18, s18, 0x3ffff\n\t"
      "v_xor_b32 v232, 128, v218\n\t"
      "v_add_u32 v232, 0x10000, v232\n\t"
      "v_xor_b32 v233, 160, v218\n\t"
      "v_add_u32 v233, 0x10000, v233\n\t"
      "v_mfma_f32_16x16x32_bf16 v[76:79], a[44:47], a[104:107], v[76:79]\n\t"
      "ds_read_b64_tr_b16 a[0:1], v220 offset:24576\n\t"
      "v_xor_b32 v234, 192, v218\n\t"
      "v_add_u32 v234, 0x10000, v234\n\t"
      "v_xor_b32 v235, 224, v218\n\t"
      "v_add_u32 v235, 0x10000, v235\n\t"
      "v_mfma_f32_16x16x32_bf16 v[80:83], a[32:35], a[72:75], 0\n\t"
      "ds_read_b64_tr_b16 a[2:3], v220 offset:57344\n\t"
      "v_maximum3_f32 v236, v236, v48, v49\n\t"
      "v_mfma_f32_16x16x32_bf16 v[80:83], a[36:39], a[84:87], v[80:83]\n\t"
      "ds_read_b64_tr_b16 a[4:5], v221 offset:24576\n\t"
      "v_maximum3_f32 v237, v237, v52, v53\n\t"
      "v_mfma_f32_16x16x32_bf16 v[80:83], a[40:43], a[96:99], v[80:83]\n\t"
      "ds_read_b64_tr_b16 a[6:7], v221 offset:57344\n\t"
      "v_maximum3_f32 v238, v238, v56, v57\n\t"
      "v_mfma_f32_16x16x32_bf16 v[80:83], a[44:47], a[108:111], v[80:83]\n\t"
      "ds_read_b64_tr_b16 a[8:9], v222 offset:24576\n\t"
      "v_mfma_f32_16x16x32_bf16 v[84:87], a[48:51], a[64:67], 0\n\t"
      "ds_read_b64_tr_b16 a[10:11], v222 offset:57344\n\t"
      "v_maximum3_f32 v236, v236, v50, v51\n\t"
      "v_mfma_f32_16x16x32_bf16 v[84:87], a[52:55], a[76:79], v[84:87]\n\t"
      "ds_read_b64_tr_b16 a[12:13], v223 offset:24576\n\t"
      "v_maximum3_f32 v237, v237, v54, v55\n\t"
      "v_mfma_f32_16x16x32_bf16 v[84:87], a[56:59], a[88:91], v[84:87]\n\t"
      "ds_read_b64_tr_b16 a[14:15], v223 offset:57344\n\t"
      "v_maximum3_f32 v238, v238, v58, v59\n\t"
      "v_mfma_f32_16x16x32_bf16 v[84:87], a[60:63], a[100:103], v[84:87]\n\t"
      "ds_read_b64_tr_b16 a[16:17], v224 offset:24576\n\t"
      "v_mfma_f32_16x16x32_bf16 v[88:91], a[48:51], a[68:71], 0\n\t"
      "ds_read_b64_tr_b16 a[18:19], v224 offset:57344\n\t"
      "v_maximum3_f32 v236, v236, v60, v61\n\t"
      "v_mfma_f32_16x16x32_bf16 v[88:91], a[52:55], a[80:83], v[88:91]\n\t"
      "ds_read_b64_tr_b16 a[20:21], v225 offset:24576\n\t"
      "v_maximum3_f32 v237, v237, v64, v65\n\t"
      "v_mfma_f32_16x16x32_bf16 v[88:91], a[56:59], a[92:95], v[88:91]\n\t"
      "ds_read_b64_tr_b16 a[22:23], v225 offset:57344\n\t"
      "v_maximum3_f32 v238, v238, v68, v69\n\t"
      "v_mfma_f32_16x16x32_bf16 v[88:91], a[60:63], a[104:107], v[88:91]\n\t"
      "ds_read_b64_tr_b16 a[24:25], v226 offset:24576\n\t"
      "v_mfma_f32_16x16x32_bf16 v[92:95], a[48:51], a[72:75], 0\n\t"
      "ds_read_b64_tr_b16 a[26:27], v226 offset:57344\n\t"
      "v_maximum3_f32 v236, v236, v62, v63\n\t"
      "v_mfma_f32_16x16x32_bf16 v[92:95], a[52:55], a[84:87], v[92:95]\n\t"
      "ds_read_b64_tr_b16 a[28:29], v227 offset:24576\n\t"
      "v_maximum3_f32 v237, v237, v66, v67\n\t"
      "v_mfma_f32_16x16x32_bf16 v[92:95], a[56:59], a[96:99], v[92:95]\n\t"
      "ds_read_b64_tr_b16 a[30:31], v227 offset:57344\n\t"
      "v_maximum3_f32 v238, v238, v70, v71\n\t"
      "v_mfma_f32_16x16x32_bf16 v[92:95], a[60:63], a[108:111], v[92:95]\n\t"
      "ds_read_b64_tr_b16 a[32:33], v228 offset:24576\n\t"
      "ds_read_b64_tr_b16 a[34:35], v228 offset:57344\n\t"
      "ds_read_b64_tr_b16 a[36:37], v229 offset:24576\n\t"
      "ds_read_b64_tr_b16 a[38:39], v229 offset:57344\n\t"
      "ds_read_b64_tr_b16 a[40:41], v230 offset:24576\n\t"
      "v_maximum3_f32 v236, v236, v72, v73\n\t"
      "v_maximum3_f32 v237, v237, v76, v77\n\t"
      "v_maximum3_f32 v238, v238, v80, v81\n\t"
      "v_maximum3_f32 v236, v236, v74, v75\n\t"
      "v_maximum3_f32 v237, v237, v78, v79\n\t"
      "v_maximum3_f32 v238, v238, v82, v83\n\t"
      "v_maximum3_f32 v236, v236, v84, v85\n\t"
      "v_maximum3_f32 v237, v237, v88, v89\n\t"
      "v_maximum3_f32 v238, v238, v92, v93\n\t"
      "v_maximum3_f32 v236, v236, v86, v87\n\t"
      "v_maximum3_f32 v237, v237, v90, v91\n\t"
      "v_maximum3_f32 v238, v238, v94, v95\n\t"
      "v_mov_b32 v246, v236\n\t"
      "v_mov_b32 v247, v237\n\t"
      "v_mov_b32 v248, v238\n\t"
      "v_permlane16_swap_b32 v246, v236\n\t"
      "v_permlane16_swap_b32 v247, v237\n\t"
      "v_permlane16_swap_b32 v248, v238\n\t"
      "v_maximum3_f32 v236, v236, v246, v246\n\t"
      "v_maximum3_f32 v237, v237, v247, v247\n\t"
      "v_maximum3_f32 v238, v238, v248, v248\n\t"
      "v_mov_b32 v246, v236\n\t"
      "v_mov_b32 v247, v237\n\t"
      "v_mov_b32 v248, v238\n\t"
      "v_permlane32_swap_b32 v246, v236\n\t"
      "v_permlane32_swap_b32 v247, v237\n\t"
      "v_permlane32_swap_b32 v248, v238\n\t"
      "v_maximum3_f32 v236, v236, v246, v246\n\t"
      "v_maximum3_f32 v237, v237, v247, v247\n\t"
      "v_maximum3_f32 v238, v238, v248, v248\n\t"
      "v_mul_f32 v240, 0xbe0293ee, v236\n\t"
      "v_mul_f32 v242, 0xbe0293ee, v237\n\t"
      "v_mul_f32 v244, 0xbe0293ee, v238\n\t"
      "v_fma_f32 v246, v241, s26, v240\n\t"
      "v_fma_f32 v248, v243, s26, v242\n\t"
      "v_fma_f32 v252, v249, s26, v244\n\t"
      "v_exp_f32 v246, v246\n\t"
      "v_exp_f32 v248, v248\n\t"
      "v_exp_f32 v252, v252\n\t"
      "v_pk_fma_f32 v[0:1], v[0:1], s[26:27], v[240:241] op_sel_hi:[1,0,0]\n\t"
      "v_pk_fma_f32 v[2:3], v[2:3], s[26:27], v[240:241] op_sel_hi:[1,0,0]\n\t"
      "v_pk_fma_f32 v[12:13], v[12:13], s[26:27], v[240:241] op_sel_hi:[1,0,0]\n\t"
      "v_pk_fma_f32 v[14:15], v[14:15], s[26:27], v[240:241] op_sel_hi:[1,0,0]\n\t"
      "v_exp_f32 v0, v0\n\t"
      "v_exp_f32 v1, v1\n\t"
      "v_exp_f32 v2, v2\n\t"
      "v_exp_f32 v3, v3\n\t"
      "v_exp_f32 v12, v12\n\t"
      "v_exp_f32 v13, v13\n\t"
      "v_exp_f32 v14, v14\n\t"
      "v_exp_f32 v15, v15\n\t"
      "v_pk_fma_f32 v[4:5], v[4:5], s[26:27], v[242:243] op_sel_hi:[1,0,0]\n\t"
      "v_pk_fma_f32 v[6:7], v[6:7], s[26:27], v[242:243] op_sel_hi:[1,0,0]\n\t"
      "v_pk_fma_f32 v[16:17], v[16:17], s[26:27], v[242:243] op_sel_hi:[1,0,0]\n\t"
      "v_pk_fma_f32 v[18:19], v[18:19], s[26:27], v[242:243] op_sel_hi:[1,0,0]\n\t"
      "ds_read_b64_tr_b16 a[42:43], v230 offset:57344\n\t"
      "ds_read_b64_tr_b16 a[44:45], v231 offset:24576\n\t"
      "v_pk_mul_f32 v[192:193], v[192:193], v[246:247] op_sel_hi:[1,0]\n\t"
      "v_pk_mul_f32 v[96:97], v[96:97], v[246:247] op_sel_hi:[1,0]\n\t"
      "v_pk_mul_f32 v[98:99], v[98:99], v[246:247] op_sel_hi:[1,0]\n\t"
      "v_pk_mul_f32 v[108:109], v[108:109], v[246:247] op_sel_hi:[1,0]\n\t"
      "v_pk_mul_f32 v[110:111], v[110:111], v[246:247] op_sel_hi:[1,0]\n\t"
      "v_cvt_pk_bf16_f32 v0, v0, v1\n\t"
      "v_cvt_pk_bf16_f32 v1, v2, v3\n\t"
      "v_cvt_pk_bf16_f32 v2, v12, v13\n\t"
      "v_cvt_pk_bf16_f32 v3, v14, v15\n\t"
      "v_exp_f32 v4, v4\n\t"
      "v_exp_f32 v5, v5\n\t"
      "v_exp_f32 v6, v6\n\t"
      "v_exp_f32 v7, v7\n\t"
      "v_exp_f32 v16, v16\n\t"
      "v_exp_f32 v17, v17\n\t"
      "v_exp_f32 v18, v18\n\t"
      "v_exp_f32 v19, v19\n\t"
      "v_pk_fma_f32 v[8:9], v[8:9], s[26:27], v[244:245] op_sel_hi:[1,0,0]\n\t"
      "v_pk_fma_f32 v[10:11], v[10:11], s[26:27], v[244:245] op_sel_hi:[1,0,0]\n\t"
      "v_pk_fma_f32 v[20:21], v[20:21], s[26:27], v[244:245] op_sel_hi:[1,0,0]\n\t"
      "v_pk_fma_f32 v[22:23], v[22:23], s[26:27], v[244:245] op_sel_hi:[1,0,0]\n\t"
      "ds_read_b64_tr_b16 a[46:47], v231 offset:57344\n\t"
      "ds_read_b64_tr_b16 a[48:49], v232 offset:24576\n\t"
      "v_pk_mul_f32 v[120:121], v[120:121], v[246:247] op_sel_hi:[1,0]\n\t"
      "v_pk_mul_f32 v[122:123], v[122:123], v[246:247] op_sel_hi:[1,0]\n\t"
      "v_pk_mul_f32 v[132:133], v[132:133], v[246:247] op_sel_hi:[1,0]\n\t"
      "v_pk_mul_f32 v[134:135], v[134:135], v[246:247] op_sel_hi:[1,0]\n\t"
      "v_pk_mul_f32 v[144:145], v[144:145], v[246:247] op_sel_hi:[1,0]\n\t"
      "v_cvt_pk_bf16_f32 v4, v4, v5\n\t"
      "v_cvt_pk_bf16_f32 v5, v6, v7\n\t"
      "v_cvt_pk_bf16_f32 v6, v16, v17\n\t"
      "v_cvt_pk_bf16_f32 v7, v18, v19\n\t"
      "v_exp_f32 v8, v8\n\t"
      "v_exp_f32 v9, v9\n\t"
      "v_exp_f32 v10, v10\n\t"
      "v_exp_f32 v11, v11\n\t"
      "v_exp_f32 v20, v20\n\t"
      "v_exp_f32 v21, v21\n\t"
      "v_exp_f32 v22, v22\n\t"
      "v_exp_f32 v23, v23\n\t"
      "v_pk_fma_f32 v[24:25], v[24:25], s[26:27], v[240:241] op_sel_hi:[1,0,0]\n\t"
      "v_pk_fma_f32 v[26:27], v[26:27], s[26:27], v[240:241] op_sel_hi:[1,0,0]\n\t"
      "v_pk_fma_f32 v[36:37], v[36:37], s[26:27], v[240:241] op_sel_hi:[1,0,0]\n\t"
      "v_pk_fma_f32 v[38:39], v[38:39], s[26:27], v[240:241] op_sel_hi:[1,0,0]\n\t"
      "ds_read_b64_tr_b16 a[50:51], v232 offset:57344\n\t"
      "ds_read_b64_tr_b16 a[52:53], v233 offset:24576\n\t"
      "v_pk_mul_f32 v[146:147], v[146:147], v[246:247] op_sel_hi:[1,0]\n\t"
      "v_pk_mul_f32 v[156:157], v[156:157], v[246:247] op_sel_hi:[1,0]\n\t"
      "v_pk_mul_f32 v[158:159], v[158:159], v[246:247] op_sel_hi:[1,0]\n\t"
      "v_pk_mul_f32 v[168:169], v[168:169], v[246:247] op_sel_hi:[1,0]\n\t"
      "v_pk_mul_f32 v[170:171], v[170:171], v[246:247] op_sel_hi:[1,0]\n\t"
      "v_cvt_pk_bf16_f32 v8, v8, v9\n\t"
      "v_cvt_pk_bf16_f32 v9, v10, v11\n\t"
      "v_cvt_pk_bf16_f32 v10, v20, v21\n\t"
      "v_cvt_pk_bf16_f32 v11, v22, v23\n\t"
      "v_exp_f32 v24, v24\n\t"
      "v_exp_f32 v25, v25\n\t"
      "v_exp_f32 v26, v26\n\t"
      "v_exp_f32 v27, v27\n\t"
      "v_exp_f32 v36, v36\n\t"
      "v_exp_f32 v37, v37\n\t"
      "v_exp_f32 v38, v38\n\t"
      "v_exp_f32 v39, v39\n\t"
      "v_pk_fma_f32 v[28:29], v[28:29], s[26:27], v[242:243] op_sel_hi:[1,0,0]\n\t"
      "v_pk_fma_f32 v[30:31], v[30:31], s[26:27], v[242:243] op_sel_hi:[1,0,0]\n\t"
      "v_pk_fma_f32 v[40:41], v[40:41], s[26:27], v[242:243] op_sel_hi:[1,0,0]\n\t"
      "v_pk_fma_f32 v[42:43], v[42:43], s[26:27], v[242:243] op_sel_hi:[1,0,0]\n\t"
      "ds_read_b64_tr_b16 a[54:55], v233 offset:57344\n\t"
      "ds_read_b64_tr_b16 a[56:57], v234 offset:24576\n\t"
      "v_pk_mul_f32 v[180:181], v[180:181], v[246:247] op_sel_hi:[1,0]\n\t"
      "v_pk_mul_f32 v[182:183], v[182:183], v[246:247] op_sel_hi:[1,0]\n\t"
      "v_pk_mul_f32 v[196:197], v[196:197], v[248:249] op_sel_hi:[1,0]\n\t"
      "v_pk_mul_f32 v[100:101], v[100:101], v[248:249] op_sel_hi:[1,0]\n\t"
      "v_pk_mul_f32 v[102:103], v[102:103], v[248:249] op_sel_hi:[1,0]\n\t"
      "v_cvt_pk_bf16_f32 v24, v24, v25\n\t"
      "v_cvt_pk_bf16_f32 v25, v26, v27\n\t"
      "v_cvt_pk_bf16_f32 v26, v36, v37\n\t"
      "v_cvt_pk_bf16_f32 v27, v38, v39\n\t"
      "v_exp_f32 v28, v28\n\t"
      "v_exp_f32 v29, v29\n\t"
      "v_exp_f32 v30, v30\n\t"
      "v_exp_f32 v31, v31\n\t"
      "v_exp_f32 v40, v40\n\t"
      "v_exp_f32 v41, v41\n\t"
      "v_exp_f32 v42, v42\n\t"
      "v_exp_f32 v43, v43\n\t"
      "v_pk_fma_f32 v[32:33], v[32:33], s[26:27], v[244:245] op_sel_hi:[1,0,0]\n\t"
      "v_pk_fma_f32 v[34:35], v[34:35], s[26:27], v[244:245] op_sel_hi:[1,0,0]\n\t"
      "v_pk_fma_f32 v[44:45], v[44:45], s[26:27], v[244:245] op_sel_hi:[1,0,0]\n\t"
      "v_pk_fma_f32 v[46:47], v[46:47], s[26:27], v[244:245] op_sel_hi:[1,0,0]\n\t"
      "ds_read_b64_tr_b16 a[58:59], v234 offset:57344\n\t"
      "ds_read_b64_tr_b16 a[60:61], v235 offset:24576\n\t"
      "v_pk_mul_f32 v[112:113], v[112:113], v[248:249] op_sel_hi:[1,0]\n\t"
      "v_pk_mul_f32 v[114:115], v[114:115], v[248:249] op_sel_hi:[1,0]\n\t"
      "v_pk_mul_f32 v[124:125], v[124:125], v[248:249] op_sel_hi:[1,0]\n\t"
      "v_pk_mul_f32 v[126:127], v[126:127], v[248:249] op_sel_hi:[1,0]\n\t"
      "v_pk_mul_f32 v[136:137], v[136:137], v[248:249] op_sel_hi:[1,0]\n\t"
      "v_cvt_pk_bf16_f32 v28, v28, v29\n\t"
      "v_cvt_pk_bf16_f32 v29, v30, v31\n\t"
      "v_cvt_pk_bf16_f32 v30, v40, v41\n\t"
      "v_cvt_pk_bf16_f32 v31, v42, v43\n\t"
      "v_exp_f32 v32, v32\n\t"
      "v_exp_f32 v33, v33\n\t"
      "v_exp_f32 v34, v34\n\t"
      "v_exp_f32 v35, v35\n\t"
      "v_exp_f32 v44, v44\n\t"
      "v_exp_f32 v45, v45\n\t"
      "v_exp_f32 v46, v46\n\t"
      "v_exp_f32 v47, v47\n\t"
      "v_pk_fma_f32 v[48:49], v[48:49], s[26:27], v[240:241] op_sel_hi:[1,0,0]\n\t"
      "v_pk_fma_f32 v[50:51], v[50:51], s[26:27], v[240:241] op_sel_hi:[1,0,0]\n\t"
      "v_pk_fma_f32 v[60:61], v[60:61], s[26:27], v[240:241] op_sel_hi:[1,0,0]\n\t"
      "v_pk_fma_f32 v[62:63], v[62:63], s[26:27], v[240:241] op_sel_hi:[1,0,0]\n\t"
      "ds_read_b64_tr_b16 a[62:63], v235 offset:57344\n\t"
      "s_waitcnt lgkmcnt(0)\n\t"
      "s_waitcnt vmcnt(24)\n\t"
      "s_barrier\n\t"
      "v_pk_mul_f32 v[138:139], v[138:139], v[248:249] op_sel_hi:[1,0]\n\t"
      "v_pk_mul_f32 v[148:149], v[148:149], v[248:249] op_sel_hi:[1,0]\n\t"
      "v_pk_mul_f32 v[150:151], v[150:151], v[248:249] op_sel_hi:[1,0]\n\t"
      "v_pk_mul_f32 v[160:161], v[160:161], v[248:249] op_sel_hi:[1,0]\n\t"
      "v_pk_mul_f32 v[162:163], v[162:163], v[248:249] op_sel_hi:[1,0]\n\t"
      "v_cvt_pk_bf16_f32 v32, v32, v33\n\t"
      "v_cvt_pk_bf16_f32 v33, v34, v35\n\t"
      "v_cvt_pk_bf16_f32 v34, v44, v45\n\t"
      "v_cvt_pk_bf16_f32 v35, v46, v47\n\t"
      "v_exp_f32 v48, v48\n\t"
      "v_exp_f32 v49, v49\n\t"
      "v_exp_f32 v50, v50\n\t"
      "v_exp_f32 v51, v51\n\t"
      "v_exp_f32 v60, v60\n\t"
      "v_exp_f32 v61, v61\n\t"
      "v_exp_f32 v62, v62\n\t"
      "v_exp_f32 v63, v63\n\t"
      "v_pk_fma_f32 v[52:53], v[52:53], s[26:27], v[242:243] op_sel_hi:[1,0,0]\n\t"
      "v_pk_fma_f32 v[54:55], v[54:55], s[26:27], v[242:243] op_sel_hi:[1,0,0]\n\t"
      "v_pk_fma_f32 v[64:65], v[64:65], s[26:27], v[242:243] op_sel_hi:[1,0,0]\n\t"
      "v_pk_fma_f32 v[66:67], v[66:67], s[26:27], v[242:243] op_sel_hi:[1,0,0]\n\t"
      "s_mov_b32 m0, s96\n\t"
      "s_nop 0\n\t"
      "buffer_load_dwordx4 v208, s[44:47], s18 offen lds\n\t"
      "v_pk_mul_f32 v[172:173], v[172:173], v[248:249] op_sel_hi:[1,0]\n\t"
      "v_pk_mul_f32 v[174:175], v[174:175], v[248:249] op_sel_hi:[1,0]\n\t"
      "v_pk_mul_f32 v[184:185], v[184:185], v[248:249] op_sel_hi:[1,0]\n\t"
      "v_pk_mul_f32 v[186:187], v[186:187], v[248:249] op_sel_hi:[1,0]\n\t"
      "v_pk_mul_f32 v[200:201], v[200:201], v[252:253] op_sel_hi:[1,0]\n\t"
      "v_cvt_pk_bf16_f32 v48, v48, v49\n\t"
      "v_cvt_pk_bf16_f32 v49, v50, v51\n\t"
      "v_cvt_pk_bf16_f32 v50, v60, v61\n\t"
      "v_cvt_pk_bf16_f32 v51, v62, v63\n\t"
      "v_exp_f32 v52, v52\n\t"
      "v_exp_f32 v53, v53\n\t"
      "v_exp_f32 v54, v54\n\t"
      "v_exp_f32 v55, v55\n\t"
      "v_exp_f32 v64, v64\n\t"
      "v_exp_f32 v65, v65\n\t"
      "v_exp_f32 v66, v66\n\t"
      "v_exp_f32 v67, v67\n\t"
      "v_pk_fma_f32 v[56:57], v[56:57], s[26:27], v[244:245] op_sel_hi:[1,0,0]\n\t"
      "v_pk_fma_f32 v[58:59], v[58:59], s[26:27], v[244:245] op_sel_hi:[1,0,0]\n\t"
      "v_pk_fma_f32 v[68:69], v[68:69], s[26:27], v[244:245] op_sel_hi:[1,0,0]\n\t"
      "v_pk_fma_f32 v[70:71], v[70:71], s[26:27], v[244:245] op_sel_hi:[1,0,0]\n\t"
      "buffer_load_dwordx4 v209, s[44:47], s18 offen offset:1024 lds\n\t"
      "buffer_load_dwordx4 v210, s[44:47], s18 offen offset:2048 lds\n\t"
      "v_pk_mul_f32 v[104:105], v[104:105], v[252:253] op_sel_hi:[1,0]\n\t"
      "v_pk_mul_f32 v[106:107], v[106:107], v[252:253] op_sel_hi:[1,0]\n\t"
      "v_pk_mul_f32 v[116:117], v[116:117], v[252:253] op_sel_hi:[1,0]\n\t"
      "v_pk_mul_f32 v[118:119], v[118:119], v[252:253] op_sel_hi:[1,0]\n\t"
      "v_pk_mul_f32 v[128:129], v[128:129], v[252:253] op_sel_hi:[1,0]\n\t"
      "v_cvt_pk_bf16_f32 v52, v52, v53\n\t"
      "v_cvt_pk_bf16_f32 v53, v54, v55\n\t"
      "v_cvt_pk_bf16_f32 v54, v64, v65\n\t"
      "v_cvt_pk_bf16_f32 v55, v66, v67\n\t"
      "v_exp_f32 v56, v56\n\t"
      "v_exp_f32 v57, v57\n\t"
      "v_exp_f32 v58, v58\n\t"
      "v_exp_f32 v59, v59\n\t"
      "v_exp_f32 v68, v68\n\t"
      "v_exp_f32 v69, v69\n\t"
      "v_exp_f32 v70, v70\n\t"
      "v_exp_f32 v71, v71\n\t"
      "v_pk_fma_f32 v[72:73], v[72:73], s[26:27], v[240:241] op_sel_hi:[1,0,0]\n\t"
      "v_pk_fma_f32 v[74:75], v[74:75], s[26:27], v[240:241] op_sel_hi:[1,0,0]\n\t"
      "v_pk_fma_f32 v[84:85], v[84:85], s[26:27], v[240:241] op_sel_hi:[1,0,0]\n\t"
      "v_pk_fma_f32 v[86:87], v[86:87], s[26:27], v[240:241] op_sel_hi:[1,0,0]\n\t"
      "buffer_load_dwordx4 v211, s[44:47], s18 offen offset:3072 lds\n\t"
      "s_add_u32 s18, s18, 0x4000\n\t"
      "s_and_b32 s18, s18, 0x3ffff\n\t"
      "v_pk_mul_f32 v[130:131], v[130:131], v[252:253] op_sel_hi:[1,0]\n\t"
      "v_pk_mul_f32 v[140:141], v[140:141], v[252:253] op_sel_hi:[1,0]\n\t"
      "v_pk_mul_f32 v[142:143], v[142:143], v[252:253] op_sel_hi:[1,0]\n\t"
      "v_pk_mul_f32 v[152:153], v[152:153], v[252:253] op_sel_hi:[1,0]\n\t"
      "v_pk_mul_f32 v[154:155], v[154:155], v[252:253] op_sel_hi:[1,0]\n\t"
      "v_cvt_pk_bf16_f32 v56, v56, v57\n\t"
      "v_cvt_pk_bf16_f32 v57, v58, v59\n\t"
      "v_cvt_pk_bf16_f32 v58, v68, v69\n\t"
      "v_cvt_pk_bf16_f32 v59, v70, v71\n\t"
      "v_exp_f32 v72, v72\n\t"
      "v_exp_f32 v73, v73\n\t"
      "v_exp_f32 v74, v74\n\t"
      "v_exp_f32 v75, v75\n\t"
      "v_exp_f32 v84, v84\n\t"
      "v_exp_f32 v85, v85\n\t"
      "v_exp_f32 v86, v86\n\t"
      "v_exp_f32 v87, v87\n\t"
      "v_pk_fma_f32 v[76:77], v[76:77], s[26:27], v[242:243] op_sel_hi:[1,0,0]\n\t"
      "v_pk_fma_f32 v[78:79], v[78:79], s[26:27], v[242:243] op_sel_hi:[1,0,0]\n\t"
      "v_pk_fma_f32 v[88:89], v[88:89], s[26:27], v[242:243] op_sel_hi:[1,0,0]\n\t"
      "v_pk_fma_f32 v[90:91], v[90:91], s[26:27], v[242:243] op_sel_hi:[1,0,0]\n\t"
      "v_pk_mul_f32 v[164:165], v[164:165], v[252:253] op_sel_hi:[1,0]\n\t"
      "v_pk_mul_f32 v[166:167], v[166:167], v[252:253] op_sel_hi:[1,0]\n\t"
      "v_pk_mul_f32 v[176:177], v[176:177], v[252:253] op_sel_hi:[1,0]\n\t"
      "v_pk_mul_f32 v[178:179], v[178:179], v[252:253] op_sel_hi:[1,0]\n\t"
      "v_pk_mul_f32 v[188:189], v[188:189], v[252:253] op_sel_hi:[1,0]\n\t"
      "v_cvt_pk_bf16_f32 v72, v72, v73\n\t"
      "v_cvt_pk_bf16_f32 v73, v74, v75\n\t"
      "v_cvt_pk_bf16_f32 v74, v84, v85\n\t"
      "v_cvt_pk_bf16_f32 v75, v86, v87\n\t"
      "v_exp_f32 v76, v76\n\t"
      "v_exp_f32 v77, v77\n\t"
      "v_exp_f32 v78, v78\n\t"
      "v_exp_f32 v79, v79\n\t"
      "v_exp_f32 v88, v88\n\t"
      "v_exp_f32 v89, v89\n\t"
      "v_exp_f32 v90, v90\n\t"
      "v_exp_f32 v91, v91\n\t"
      "v_pk_fma_f32 v[80:81], v[80:81], s[26:27], v[244:245] op_sel_hi:[1,0,0]\n\t"
      "v_pk_fma_f32 v[82:83], v[82:83], s[26:27], v[244:245] op_sel_hi:[1,0,0]\n\t"
      "v_pk_fma_f32 v[92:93], v[92:93], s[26:27], v[244:245] op_sel_hi:[1,0,0]\n\t"
      "v_pk_fma_f32 v[94:95], v[94:95], s[26:27], v[244:245] op_sel_hi:[1,0,0]\n\t"
      "v_pk_mul_f32 v[190:191], v[190:191], v[252:253] op_sel_hi:[1,0]\n\t"
      "v_cvt_pk_bf16_f32 v76, v76, v77\n\t"
      "v_cvt_pk_bf16_f32 v77, v78, v79\n\t"
      "v_cvt_pk_bf16_f32 v78, v88, v89\n\t"
      "v_cvt_pk_bf16_f32 v79, v90, v91\n\t"
      "v_exp_f32 v80, v80\n\t"
      "v_exp_f32 v81, v81\n\t"
      "v_exp_f32 v82, v82\n\t"
      "v_exp_f32 v83, v83\n\t"
      "v_exp_f32 v92, v92\n\t"
      "v_exp_f32 v93, v93\n\t"
      "v_exp_f32 v94, v94\n\t"
      "v_exp_f32 v95, v95\n\t"
      "v_cvt_pk_bf16_f32 v80, v80, v81\n\t"
      "v_cvt_pk_bf16_f32 v81, v82, v83\n\t"
      "v_cvt_pk_bf16_f32 v82, v92, v93\n\t"
      "v_cvt_pk_bf16_f32 v83, v94, v95\n\t"
      "v_mfma_f32_16x16x32_bf16 v[192:195], a[208:211], v[0:3], v[192:195]\n\t"
      "v_accvgpr_read_b32 v12, a216\n\t"
      "v_mfma_f32_16x16x32_bf16 v[196:199], a[208:211], v[4:7], v[196:199]\n\t"
      "v_mfma_f32_16x16x32_bf16 v[200:203], a[208:211], v[8:11], v[200:203]\n\t"
      "v_accvgpr_read_b32 v13, a217\n\t"
      "v_mfma_f32_16x16x32_bf16 v[96:99], a[0:3], v[0:3], v[96:99]\n\t"
      "v_mfma_f32_16x16x32_bf16 v[100:103], a[0:3], v[4:7], v[100:103]\n\t"
      "v_accvgpr_read_b32 v14, a218\n\t"
      "v_mfma_f32_16x16x32_bf16 v[104:107], a[0:3], v[8:11], v[104:107]\n\t"
      "v_mfma_f32_16x16x32_bf16 v[108:111], a[4:7], v[0:3], v[108:111]\n\t"
      "v_accvgpr_read_b32 v15, a219\n\t"
      "v_mfma_f32_16x16x32_bf16 v[112:115], a[4:7], v[4:7], v[112:115]\n\t"
      "v_mfma_f32_16x16x32_bf16 v[116:119], a[4:7], v[8:11], v[116:119]\n\t"
      "v_accvgpr_read_b32 v16, a220\n\t"
      "v_mfma_f32_16x16x32_bf16 v[120:123], a[8:11], v[0:3], v[120:123]\n\t"
      "v_mfma_f32_16x16x32_bf16 v[124:127], a[8:11], v[4:7], v[124:127]\n\t"
      "v_accvgpr_read_b32 v17, a221\n\t"
      "v_mfma_f32_16x16x32_bf16 v[128:131], a[8:11], v[8:11], v[128:131]\n\t"
      "v_mfma_f32_16x16x32_bf16 v[132:135], a[12:15], v[0:3], v[132:135]\n\t"
      "v_accvgpr_read_b32 v18, a222\n\t"
      "v_mfma_f32_16x16x32_bf16 v[136:139], a[12:15], v[4:7], v[136:139]\n\t"
      "v_mfma_f32_16x16x32_bf16 v[140:143], a[12:15], v[8:11], v[140:143]\n\t"
      "v_accvgpr_read_b32 v19, a223\n\t"
      "v_mfma_f32_16x16x32_bf16 v[144:147], a[16:19], v[0:3], v[144:147]\n\t"
      "v_mfma_f32_16x16x32_bf16 v[148:151], a[16:19], v[4:7], v[148:151]\n\t"
      "v_mfma_f32_16x16x32_bf16 v[152:155], a[16:19], v[8:11], v[152:155]\n\t"
      "v_mfma_f32_16x16x32_bf16 v[156:159], a[20:23], v[0:3], v[156:159]\n\t"
      "v_mfma_f32_16x16x32_bf16 v[160:163], a[20:23], v[4:7], v[160:163]\n\t"
      "v_mfma_f32_16x16x32_bf16 v[164:167], a[20:23], v[8:11], v[164:167]\n\t"
      "v_mfma_f32_16x16x32_bf16 v[168:171], a[24:27], v[0:3], v[168:171]\n\t"
      "v_mfma_f32_16x16x32_bf16 v[172:175], a[24:27], v[4:7], v[172:175]\n\t"
      "v_mfma_f32_16x16x32_bf16 v[176:179], a[24:27], v[8:11], v[176:179]\n\t"
      "v_mfma_f32_16x16x32_bf16 v[180:183], a[28:31], v[0:3], v[180:183]\n\t"
      "v_mfma_f32_16x16x32_bf16 v[184:187], a[28:31], v[4:7], v[184:187]\n\t"
      "v_mfma_f32_16x16x32_bf16 v[188:191], a[28:31], v[8:11], v[188:191]\n\t"
      "v_mfma_f32_16x16x32_bf16 v[192:195], a[208:211], v[24:27], v[192:195]\n\t"
      "ds_read_b64_tr_b16 a[0:1], v220 offset:28672\n\t"
      "v_mfma_f32_16x16x32_bf16 v[196:199], a[208:211], v[28:31], v[196:199]\n\t"
      "ds_read_b64_tr_b16 a[2:3], v220 offset:61440\n\t"
      "v_mfma_f32_16x16x32_bf16 v[200:203], a[208:211], v[32:35], v[200:203]\n\t"
      "ds_read_b64_tr_b16 a[4:5], v221 offset:28672\n\t"
      "v_mfma_f32_16x16x32_bf16 v[96:99], a[32:35], v[24:27], v[96:99]\n\t"
      "v_mfma_f32_16x16x32_bf16 v[100:103], a[32:35], v[28:31], v[100:103]\n\t"
      "ds_read_b64_tr_b16 a[6:7], v221 offset:61440\n\t"
      "v_mfma_f32_16x16x32_bf16 v[104:107], a[32:35], v[32:35], v[104:107]\n\t"
      "ds_read_b64_tr_b16 a[8:9], v222 offset:28672\n\t"
      "v_mfma_f32_16x16x32_bf16 v[108:111], a[36:39], v[24:27], v[108:111]\n\t"
      "v_mfma_f32_16x16x32_bf16 v[112:115], a[36:39], v[28:31], v[112:115]\n\t"
      "ds_read_b64_tr_b16 a[10:11], v222 offset:61440\n\t"
      "v_mfma_f32_16x16x32_bf16 v[116:119], a[36:39], v[32:35], v[116:119]\n\t"
      "ds_read_b64_tr_b16 a[12:13], v223 offset:28672\n\t"
      "v_mfma_f32_16x16x32_bf16 v[120:123], a[40:43], v[24:27], v[120:123]\n\t"
      "v_mfma_f32_16x16x32_bf16 v[124:127], a[40:43], v[28:31], v[124:127]\n\t"
      "ds_read_b64_tr_b16 a[14:15], v223 offset:61440\n\t"
      "v_mfma_f32_16x16x32_bf16 v[128:131], a[40:43], v[32:35], v[128:131]\n\t"
      "ds_read_b64_tr_b16 a[16:17], v224 offset:28672\n\t"
      "v_mfma_f32_16x16x32_bf16 v[132:135], a[44:47], v[24:27], v[132:135]\n\t"
      "ds_read_b64_tr_b16 a[18:19], v224 offset:61440\n\t"
      "v_mfma_f32_16x16x32_bf16 v[136:139], a[44:47], v[28:31], v[136:139]\n\t"
      "v_mfma_f32_16x16x32_bf16 v[140:143], a[44:47], v[32:35], v[140:143]\n\t"
      "ds_read_b64_tr_b16 a[20:21], v225 offset:28672\n\t"
      "v_mfma_f32_16x16x32_bf16 v[144:147], a[48:51], v[24:27], v[144:147]\n\t"
      "ds_read_b64_tr_b16 a[22:23], v225 offset:61440\n\t"
      "v_mfma_f32_16x16x32_bf16 v[148:151], a[48:51], v[28:31], v[148:151]\n\t"
      "v_mfma_f32_16x16x32_bf16 v[152:155], a[48:51], v[32:35], v[152:155]\n\t"
      "ds_read_b64_tr_b16 a[24:25], v226 offset:28672\n\t"
      "v_mfma_f32_16x16x32_bf16 v[156:159], a[52:55], v[24:27], v[156:159]\n\t"
      "ds_read_b64_tr_b16 a[26:27], v226 offset:61440\n\t"
      "v_mfma_f32_16x16x32_bf16 v[160:163], a[52:55], v[28:31], v[160:163]\n\t"
      "v_mfma_f32_16x16x32_bf16 v[164:167], a[52:55], v[32:35], v[164:167]\n\t"
      "ds_read_b64_tr_b16 a[28:29], v227 offset:28672\n\t"
      "v_mfma_f32_16x16x32_bf16 v[168:171], a[56:59], v[24:27], v[168:171]\n\t"
      "ds_read_b64_tr_b16 a[30:31], v227 offset:61440\n\t"
      "v_mfma_f32_16x16x32_bf16 v[172:175], a[56:59], v[28:31], v[172:175]\n\t"
      "v_mfma_f32_16x16x32_bf16 v[176:179], a[56:59], v[32:35], v[176:179]\n\t"
      "v_mfma_f32_16x16x32_bf16 v[180:183], a[60:63], v[24:27], v[180:183]\n\t"
      "v_mfma_f32_16x16x32_bf16 v[184:187], a[60:63], v[28:31], v[184:187]\n\t"
      "v_mfma_f32_16x16x32_bf16 v[188:191], a[60:63], v[32:35], v[188:191]\n\t"
      "s_waitcnt lgkmcnt(0)\n\t"
      "v_mfma_f32_16x16x32_bf16 v[192:195], a[208:211], v[48:51], v[192:195]\n\t"
      "ds_read_b64_tr_b16 a[32:33], v228 offset:28672\n\t"
      "v_mfma_f32_16x16x32_bf16 v[196:199], a[208:211], v[52:55], v[196:199]\n\t"
      "ds_read_b64_tr_b16 a[34:35], v228 offset:61440\n\t"
      "v_mfma_f32_16x16x32_bf16 v[200:203], a[208:211], v[56:59], v[200:203]\n\t"
      "ds_read_b64_tr_b16 a[36:37], v229 offset:28672\n\t"
      "v_mfma_f32_16x16x32_bf16 v[96:99], a[0:3], v[48:51], v[96:99]\n\t"
      "v_mfma_f32_16x16x32_bf16 v[100:103], a[0:3], v[52:55], v[100:103]\n\t"
      "ds_read_b64_tr_b16 a[38:39], v229 offset:61440\n\t"
      "v_mfma_f32_16x16x32_bf16 v[104:107], a[0:3], v[56:59], v[104:107]\n\t"
      "ds_read_b64_tr_b16 a[40:41], v230 offset:28672\n\t"
      "v_mfma_f32_16x16x32_bf16 v[108:111], a[4:7], v[48:51], v[108:111]\n\t"
      "v_mfma_f32_16x16x32_bf16 v[112:115], a[4:7], v[52:55], v[112:115]\n\t"
      "ds_read_b64_tr_b16 a[42:43], v230 offset:61440\n\t"
      "v_mfma_f32_16x16x32_bf16 v[116:119], a[4:7], v[56:59], v[116:119]\n\t"
      "ds_read_b64_tr_b16 a[44:45], v231 offset:28672\n\t"
      "v_mfma_f32_16x16x32_bf16 v[120:123], a[8:11], v[48:51], v[120:123]\n\t"
      "v_mfma_f32_16x16x32_bf16 v[124:127], a[8:11], v[52:55], v[124:127]\n\t"
      "ds_read_b64_tr_b16 a[46:47], v231 offset:61440\n\t"
      "v_mfma_f32_16x16x32_bf16 v[128:131], a[8:11], v[56:59], v[128:131]\n\t"
      "ds_read_b64_tr_b16 a[48:49], v232 offset:28672\n\t"
      "v_mfma_f32_16x16x32_bf16 v[132:135], a[12:15], v[48:51], v[132:135]\n\t"
      "ds_read_b64_tr_b16 a[50:51], v232 offset:61440\n\t"
      "v_mfma_f32_16x16x32_bf16 v[136:139], a[12:15], v[52:55], v[136:139]\n\t"
      "v_mfma_f32_16x16x32_bf16 v[140:143], a[12:15], v[56:59], v[140:143]\n\t"
      "ds_read_b64_tr_b16 a[52:53], v233 offset:28672\n\t"
      "v_mfma_f32_16x16x32_bf16 v[144:147], a[16:19], v[48:51], v[144:147]\n\t"
      "ds_read_b64_tr_b16 a[54:55], v233 offset:61440\n\t"
      "v_mfma_f32_16x16x32_bf16 v[148:151], a[16:19], v[52:55], v[148:151]\n\t"
      "v_mfma_f32_16x16x32_bf16 v[152:155], a[16:19], v[56:59], v[152:155]\n\t"
      "ds_read_b64_tr_b16 a[56:57], v234 offset:28672\n\t"
      "v_mfma_f32_16x16x32_bf16 v[156:159], a[20:23], v[48:51], v[156:159]\n\t"
      "ds_read_b64_tr_b16 a[58:59], v234 offset:61440\n\t"
      "v_mfma_f32_16x16x32_bf16 v[160:163], a[20:23], v[52:55], v[160:163]\n\t"
      "v_mfma_f32_16x16x32_bf16 v[164:167], a[20:23], v[56:59], v[164:167]\n\t"
      "ds_read_b64_tr_b16 a[60:61], v235 offset:28672\n\t"
      "v_mfma_f32_16x16x32_bf16 v[168:171], a[24:27], v[48:51], v[168:171]\n\t"
      "ds_read_b64_tr_b16 a[62:63], v235 offset:61440\n\t"
      "v_mfma_f32_16x16x32_bf16 v[172:175], a[24:27], v[52:55], v[172:175]\n\t"
      "v_mfma_f32_16x16x32_bf16 v[176:179], a[24:27], v[56:59], v[176:179]\n\t"
      "v_mfma_f32_16x16x32_bf16 v[180:183], a[28:31], v[48:51], v[180:183]\n\t"
      "v_mfma_f32_16x16x32_bf16 v[184:187], a[28:31], v[52:55], v[184:187]\n\t"
      "v_mfma_f32_16x16x32_bf16 v[188:191], a[28:31], v[56:59], v[188:191]\n\t"
      "s_waitcnt lgkmcnt(0)\n\t"
      "s_waitcnt vmcnt(20)\n\t"
      "s_barrier\n\t"
      "s_mov_b32 m0, s97\n\t"
      "v_mfma_f32_16x16x32_bf16 v[192:195], a[208:211], v[72:75], v[192:195]\n\t"
      "buffer_load_dwordx4 v208, s[44:47], s18 offen lds\n\t"
      "v_xad_u32 v220, v218, 0, s14\n\t"
      "v_xad_u32 v221, v218, 32, s14\n\t"
      "v_mfma_f32_16x16x32_bf16 v[196:199], a[208:211], v[76:79], v[196:199]\n\t"
      "v_xad_u32 v222, v218, 64, s14\n\t"
      "v_mfma_f32_16x16x32_bf16 v[200:203], a[208:211], v[80:83], v[200:203]\n\t"
      "buffer_load_dwordx4 v209, s[44:47], s18 offen offset:1024 lds\n\t"
      "v_xor_b32 v223, 96, v218\n\t"
      "v_add_u32 v223, s14, v223\n\t"
      "v_mfma_f32_16x16x32_bf16 v[96:99], a[32:35], v[72:75], v[96:99]\n\t"
      "v_xor_b32 v224, 128, v218\n\t"
      "v_add_u32 v224, s14, v224\n\t"
      "v_xor_b32 v225, 160, v218\n\t"
      "v_add_u32 v225, s14, v225\n\t"
      "v_mfma_f32_16x16x32_bf16 v[100:103], a[32:35], v[76:79], v[100:103]\n\t"
      "v_xor_b32 v226, 192, v218\n\t"
      "v_add_u32 v226, s14, v226\n\t"
      "v_mfma_f32_16x16x32_bf16 v[104:107], a[32:35], v[80:83], v[104:107]\n\t"
      "buffer_load_dwordx4 v210, s[44:47], s18 offen offset:2048 lds\n\t"
      "v_xor_b32 v227, 224, v218\n\t"
      "v_add_u32 v227, s14, v227\n\t"
      "v_mfma_f32_16x16x32_bf16 v[108:111], a[36:39], v[72:75], v[108:111]\n\t"
      "ds_read_b64_tr_b16 a[0:1], v220\n\t"
      "v_mfma_f32_16x16x32_bf16 v[112:115], a[36:39], v[76:79], v[112:115]\n\t"
      "ds_read_b64_tr_b16 a[2:3], v220 offset:4096\n\t"
      "v_mfma_f32_16x16x32_bf16 v[116:119], a[36:39], v[80:83], v[116:119]\n\t"
      "ds_read_b64_tr_b16 a[4:5], v221\n\t"
      "v_mfma_f32_16x16x32_bf16 v[120:123], a[40:43], v[72:75], v[120:123]\n\t"
      "ds_read_b64_tr_b16 a[6:7], v221 offset:4096\n\t"
      "v_mfma_f32_16x16x32_bf16 v[124:127], a[40:43], v[76:79], v[124:127]\n\t"
      "ds_read_b64_tr_b16 a[8:9], v222\n\t"
      "v_mfma_f32_16x16x32_bf16 v[128:131], a[40:43], v[80:83], v[128:131]\n\t"
      "ds_read_b64_tr_b16 a[10:11], v222 offset:4096\n\t"
      "v_mfma_f32_16x16x32_bf16 v[132:135], a[44:47], v[72:75], v[132:135]\n\t"
      "v_mfma_f32_16x16x32_bf16 v[136:139], a[44:47], v[76:79], v[136:139]\n\t"
      "ds_read_b64_tr_b16 a[12:13], v223\n\t"
      "v_mfma_f32_16x16x32_bf16 v[140:143], a[44:47], v[80:83], v[140:143]\n\t"
      "ds_read_b64_tr_b16 a[14:15], v223 offset:4096\n\t"
      "v_mfma_f32_16x16x32_bf16 v[144:147], a[48:51], v[72:75], v[144:147]\n\t"
      "ds_read_b64_tr_b16 a[16:17], v224\n\t"
      "v_mfma_f32_16x16x32_bf16 v[148:151], a[48:51], v[76:79], v[148:151]\n\t"
      "ds_read_b64_tr_b16 a[18:19], v224 offset:4096\n\t"
      "v_mfma_f32_16x16x32_bf16 v[152:155], a[48:51], v[80:83], v[152:155]\n\t"
      "ds_read_b64_tr_b16 a[20:21], v225\n\t"
      "v_mfma_f32_16x16x32_bf16 v[156:159], a[52:55], v[72:75], v[156:159]\n\t"
      "v_mfma_f32_16x16x32_bf16 v[160:163], a[52:55], v[76:79], v[160:163]\n\t"
      "ds_read_b64_tr_b16 a[22:23], v225 offset:4096\n\t"
      "v_mfma_f32_16x16x32_bf16 v[164:167], a[52:55], v[80:83], v[164:167]\n\t"
      "ds_read_b64_tr_b16 a[24:25], v226\n\t"
      "v_mfma_f32_16x16x32_bf16 v[168:171], a[56:59], v[72:75], v[168:171]\n\t"
      "ds_read_b64_tr_b16 a[26:27], v226 offset:4096\n\t"
      "v_mfma_f32_16x16x32_bf16 v[172:175], a[56:59], v[76:79], v[172:175]\n\t"
      "ds_read_b64_tr_b16 a[28:29], v227\n\t"
      "v_mfma_f32_16x16x32_bf16 v[176:179], a[56:59], v[80:83], v[176:179]\n\t"
      "ds_read_b64_tr_b16 a[30:31], v227 offset:4096\n\t"
      "v_mfma_f32_16x16x32_bf16 v[180:183], a[60:63], v[72:75], v[180:183]\n\t"
      "buffer_load_dwordx4 v211, s[44:47], s18 offen offset:3072 lds\n\t"
      "s_add_u32 s18, s18, 0x4000\n\t"
      "s_and_b32 s18, s18, 0x3ffff\n\t"
      "v_mfma_f32_16x16x32_bf16 v[184:187], a[60:63], v[76:79], v[184:187]\n\t"
      "v_mfma_f32_16x16x32_bf16 v[188:191], a[60:63], v[80:83], v[188:191]\n\t"
      "v_rcp_f32 v240, v192\n\t"
      "v_rcp_f32 v242, v196\n\t"
      "v_rcp_f32 v244, v200\n\t"
      "s_mul_i32 s19, s8, 12288\n\t"
      "s_add_u32 s19, s19, 0x20000\n\t"
      "v_add_u32 v246, s19, v216\n\t"
      "v_pk_mul_f32 v[96:97], v[96:97], v[240:241] op_sel_hi:[1,0]\n\t"
      "v_pk_mul_f32 v[98:99], v[98:99], v[240:241] op_sel_hi:[1,0]\n\t"
      "v_pk_mul_f32 v[100:101], v[100:101], v[242:243] op_sel_hi:[1,0]\n\t"
      "v_pk_mul_f32 v[102:103], v[102:103], v[242:243] op_sel_hi:[1,0]\n\t"
      "v_pk_mul_f32 v[104:105], v[104:105], v[244:245] op_sel_hi:[1,0]\n\t"
      "v_pk_mul_f32 v[106:107], v[106:107], v[244:245] op_sel_hi:[1,0]\n\t"
      "v_pk_mul_f32 v[108:109], v[108:109], v[240:241] op_sel_hi:[1,0]\n\t"
      "v_pk_mul_f32 v[110:111], v[110:111], v[240:241] op_sel_hi:[1,0]\n\t"
      "v_pk_mul_f32 v[112:113], v[112:113], v[242:243] op_sel_hi:[1,0]\n\t"
      "v_pk_mul_f32 v[114:115], v[114:115], v[242:243] op_sel_hi:[1,0]\n\t"
      "v_pk_mul_f32 v[116:117], v[116:117], v[244:245] op_sel_hi:[1,0]\n\t"
      "v_pk_mul_f32 v[118:119], v[118:119], v[244:245] op_sel_hi:[1,0]\n\t"
      "v_pk_mul_f32 v[120:121], v[120:121], v[240:241] op_sel_hi:[1,0]\n\t"
      "v_pk_mul_f32 v[122:123], v[122:123], v[240:241] op_sel_hi:[1,0]\n\t"
      "v_pk_mul_f32 v[124:125], v[124:125], v[242:243] op_sel_hi:[1,0]\n\t"
      "v_pk_mul_f32 v[126:127], v[126:127], v[242:243] op_sel_hi:[1,0]\n\t"
      "v_pk_mul_f32 v[128:129], v[128:129], v[244:245] op_sel_hi:[1,0]\n\t"
      "v_pk_mul_f32 v[130:131], v[130:131], v[244:245] op_sel_hi:[1,0]\n\t"
      "v_pk_mul_f32 v[132:133], v[132:133], v[240:241] op_sel_hi:[1,0]\n\t"
      "v_pk_mul_f32 v[134:135], v[134:135], v[240:241] op_sel_hi:[1,0]\n\t"
      "v_pk_mul_f32 v[136:137], v[136:137], v[242:243] op_sel_hi:[1,0]\n\t"
      "v_pk_mul_f32 v[138:139], v[138:139], v[242:243] op_sel_hi:[1,0]\n\t"
      "v_pk_mul_f32 v[140:141], v[140:141], v[244:245] op_sel_hi:[1,0]\n\t"
      "v_pk_mul_f32 v[142:143], v[142:143], v[244:245] op_sel_hi:[1,0]\n\t"
      "v_pk_mul_f32 v[144:145], v[144:145], v[240:241] op_sel_hi:[1,0]\n\t"
      "v_pk_mul_f32 v[146:147], v[146:147], v[240:241] op_sel_hi:[1,0]\n\t"
      "v_pk_mul_f32 v[148:149], v[148:149], v[242:243] op_sel_hi:[1,0]\n\t"
      "v_pk_mul_f32 v[150:151], v[150:151], v[242:243] op_sel_hi:[1,0]\n\t"
      "v_pk_mul_f32 v[152:153], v[152:153], v[244:245] op_sel_hi:[1,0]\n\t"
      "v_pk_mul_f32 v[154:155], v[154:155], v[244:245] op_sel_hi:[1,0]\n\t"
      "v_pk_mul_f32 v[156:157], v[156:157], v[240:241] op_sel_hi:[1,0]\n\t"
      "v_pk_mul_f32 v[158:159], v[158:159], v[240:241] op_sel_hi:[1,0]\n\t"
      "v_pk_mul_f32 v[160:161], v[160:161], v[242:243] op_sel_hi:[1,0]\n\t"
      "v_pk_mul_f32 v[162:163], v[162:163], v[242:243] op_sel_hi:[1,0]\n\t"
      "v_pk_mul_f32 v[164:165], v[164:165], v[244:245] op_sel_hi:[1,0]\n\t"
      "v_pk_mul_f32 v[166:167], v[166:167], v[244:245] op_sel_hi:[1,0]\n\t"
      "v_pk_mul_f32 v[168:169], v[168:169], v[240:241] op_sel_hi:[1,0]\n\t"
      "v_pk_mul_f32 v[170:171], v[170:171], v[240:241] op_sel_hi:[1,0]\n\t"
      "v_pk_mul_f32 v[172:173], v[172:173], v[242:243] op_sel_hi:[1,0]\n\t"
      "v_pk_mul_f32 v[174:175], v[174:175], v[242:243] op_sel_hi:[1,0]\n\t"
      "v_pk_mul_f32 v[176:177], v[176:177], v[244:245] op_sel_hi:[1,0]\n\t"
      "v_pk_mul_f32 v[178:179], v[178:179], v[244:245] op_sel_hi:[1,0]\n\t"
      "v_pk_mul_f32 v[180:181], v[180:181], v[240:241] op_sel_hi:[1,0]\n\t"
      "v_pk_mul_f32 v[182:183], v[182:183], v[240:241] op_sel_hi:[1,0]\n\t"
      "v_pk_mul_f32 v[184:185], v[184:185], v[242:243] op_sel_hi:[1,0]\n\t"
      "v_pk_mul_f32 v[186:187], v[186:187], v[242:243] op_sel_hi:[1,0]\n\t"
      "v_pk_mul_f32 v[188:189], v[188:189], v[244:245] op_sel_hi:[1,0]\n\t"
      "v_pk_mul_f32 v[190:191], v[190:191], v[244:245] op_sel_hi:[1,0]\n\t"
      "v_cvt_pk_bf16_f32 v96, v96, v97\n\t"
      "v_cvt_pk_bf16_f32 v97, v98, v99\n\t"
      "v_cvt_pk_bf16_f32 v98, v108, v109\n\t"
      "v_cvt_pk_bf16_f32 v99, v110, v111\n\t"
      "v_cvt_pk_bf16_f32 v100, v100, v101\n\t"
      "v_cvt_pk_bf16_f32 v101, v102, v103\n\t"
      "v_cvt_pk_bf16_f32 v102, v112, v113\n\t"
      "v_cvt_pk_bf16_f32 v103, v114, v115\n\t"
      "v_cvt_pk_bf16_f32 v104, v104, v105\n\t"
      "v_cvt_pk_bf16_f32 v105, v106, v107\n\t"
      "v_cvt_pk_bf16_f32 v106, v116, v117\n\t"
      "v_cvt_pk_bf16_f32 v107, v118, v119\n\t"
      "v_cvt_pk_bf16_f32 v120, v120, v121\n\t"
      "v_cvt_pk_bf16_f32 v121, v122, v123\n\t"
      "v_cvt_pk_bf16_f32 v122, v132, v133\n\t"
      "v_cvt_pk_bf16_f32 v123, v134, v135\n\t"
      "v_cvt_pk_bf16_f32 v124, v124, v125\n\t"
      "v_cvt_pk_bf16_f32 v125, v126, v127\n\t"
      "v_cvt_pk_bf16_f32 v126, v136, v137\n\t"
      "v_cvt_pk_bf16_f32 v127, v138, v139\n\t"
      "v_cvt_pk_bf16_f32 v128, v128, v129\n\t"
      "v_cvt_pk_bf16_f32 v129, v130, v131\n\t"
      "v_cvt_pk_bf16_f32 v130, v140, v141\n\t"
      "v_cvt_pk_bf16_f32 v131, v142, v143\n\t"
      "v_cvt_pk_bf16_f32 v144, v144, v145\n\t"
      "v_cvt_pk_bf16_f32 v145, v146, v147\n\t"
      "v_cvt_pk_bf16_f32 v146, v156, v157\n\t"
      "v_cvt_pk_bf16_f32 v147, v158, v159\n\t"
      "v_cvt_pk_bf16_f32 v148, v148, v149\n\t"
      "v_cvt_pk_bf16_f32 v149, v150, v151\n\t"
      "v_cvt_pk_bf16_f32 v150, v160, v161\n\t"
      "v_cvt_pk_bf16_f32 v151, v162, v163\n\t"
      "v_cvt_pk_bf16_f32 v152, v152, v153\n\t"
      "v_cvt_pk_bf16_f32 v153, v154, v155\n\t"
      "v_cvt_pk_bf16_f32 v154, v164, v165\n\t"
      "v_cvt_pk_bf16_f32 v155, v166, v167\n\t"
      "v_cvt_pk_bf16_f32 v168, v168, v169\n\t"
      "v_cvt_pk_bf16_f32 v169, v170, v171\n\t"
      "v_cvt_pk_bf16_f32 v170, v180, v181\n\t"
      "v_cvt_pk_bf16_f32 v171, v182, v183\n\t"
      "v_cvt_pk_bf16_f32 v172, v172, v173\n\t"
      "v_cvt_pk_bf16_f32 v173, v174, v175\n\t"
      "v_cvt_pk_bf16_f32 v174, v184, v185\n\t"
      "v_cvt_pk_bf16_f32 v175, v186, v187\n\t"
      "v_cvt_pk_bf16_f32 v176, v176, v177\n\t"
      "v_cvt_pk_bf16_f32 v177, v178, v179\n\t"
      "v_cvt_pk_bf16_f32 v178, v188, v189\n\t"
      "v_cvt_pk_bf16_f32 v179, v190, v191\n\t"
      "s_cmp_lg_u32 s7, 0\n\t"
      "s_cbranch_scc1 L_ow0_%=\n\t"
      "ds_write_b128 v246, v[96:99]\n\t"
      "ds_write_b128 v246, v[100:103] offset:1024\n\t"
      "ds_write_b128 v246, v[104:107] offset:2048\n\t"
      "ds_write_b128 v246, v[120:123] offset:3072\n\t"
      "ds_write_b128 v246, v[124:127] offset:4096\n\t"
      "ds_write_b128 v246, v[128:131] offset:5120\n\t"
      "ds_write_b128 v246, v[144:147] offset:6144\n\t"
      "ds_write_b128 v246, v[148:151] offset:7168\n\t"
      "ds_write_b128 v246, v[152:155] offset:8192\n\t"
      "ds_write_b128 v246, v[168:171] offset:9216\n\t"
      "ds_write_b128 v246, v[172:175] offset:10240\n\t"
      "ds_write_b128 v246, v[176:179] offset:11264\n\t"
      "L_ow0_%=:\n\t"
      "s_waitcnt lgkmcnt(0)\n\t"
      "s_barrier\n\t"
      "ds_read_b128 a[112:115], v12\n\t"
      "ds_read_b128 a[116:119], v12 offset:1024\n\t"
      "ds_read_b128 a[120:123], v12 offset:2048\n\t"
      "ds_read_b128 a[124:127], v13\n\t"
      "ds_read_b128 a[128:131], v13 offset:1024\n\t"
      "ds_read_b128 a[132:135], v13 offset:2048\n\t"
      "ds_read_b128 a[136:139], v14\n\t"
      "ds_read_b128 a[140:143], v14 offset:1024\n\t"
      "ds_read_b128 a[144:147], v14 offset:2048\n\t"
      "ds_read_b128 a[148:151], v15\n\t"
      "ds_read_b128 a[152:155], v15 offset:1024\n\t"
      "ds_read_b128 a[156:159], v15 offset:2048\n\t"
      "ds_read_b128 a[160:163], v16\n\t"
      "ds_read_b128 a[164:167], v16 offset:1024\n\t"
      "ds_read_b128 a[168:171], v16 offset:2048\n\t"
      "ds_read_b128 a[172:175], v17\n\t"
      "ds_read_b128 a[176:179], v17 offset:1024\n\t"
      "ds_read_b128 a[180:183], v17 offset:2048\n\t"
      "ds_read_b128 a[184:187], v18\n\t"
      "ds_read_b128 a[188:191], v18 offset:1024\n\t"
      "ds_read_b128 a[192:195], v18 offset:2048\n\t"
      "ds_read_b128 a[196:199], v19\n\t"
      "ds_read_b128 a[200:203], v19 offset:1024\n\t"
      "ds_read_b128 a[204:207], v19 offset:2048\n\t"
      "s_waitcnt lgkmcnt(0)\n\t"
      "s_barrier\n\t"
      "s_cmp_lg_u32 s7, 1\n\t"
      "s_cbranch_scc1 L_ow1_%=\n\t"
      "ds_write_b128 v246, v[96:99]\n\t"
      "ds_write_b128 v246, v[100:103] offset:1024\n\t"
      "ds_write_b128 v246, v[104:107] offset:2048\n\t"
      "ds_write_b128 v246, v[120:123] offset:3072\n\t"
      "ds_write_b128 v246, v[124:127] offset:4096\n\t"
      "ds_write_b128 v246, v[128:131] offset:5120\n\t"
      "ds_write_b128 v246, v[144:147] offset:6144\n\t"
      "ds_write_b128 v246, v[148:151] offset:7168\n\t"
      "ds_write_b128 v246, v[152:155] offset:8192\n\t"
      "ds_write_b128 v246, v[168:171] offset:9216\n\t"
      "ds_write_b128 v246, v[172:175] offset:10240\n\t"
      "ds_write_b128 v246, v[176:179] offset:11264\n\t"
      "L_ow1_%=:\n\t"
      "s_waitcnt lgkmcnt(0)\n\t"
      "s_barrier\n\t"
      "ds_read_b128 v[96:99], v12\n\t"
      "ds_read_b128 v[100:103], v12 offset:1024\n\t"
      "ds_read_b128 v[104:107], v12 offset:2048\n\t"
      "ds_read_b128 v[108:111], v13\n\t"
      "ds_read_b128 v[112:115], v13 offset:1024\n\t"
      "ds_read_b128 v[116:119], v13 offset:2048\n\t"
      "ds_read_b128 v[120:123], v14\n\t"
      "ds_read_b128 v[124:127], v14 offset:1024\n\t"
      "ds_read_b128 v[128:131], v14 offset:2048\n\t"
      "ds_read_b128 v[132:135], v15\n\t"
      "ds_read_b128 v[136:139], v15 offset:1024\n\t"
      "ds_read_b128 v[140:143], v15 offset:2048\n\t"
      "ds_read_b128 v[144:147], v16\n\t"
      "ds_read_b128 v[148:151], v16 offset:1024\n\t"
      "ds_read_b128 v[152:155], v16 offset:2048\n\t"
      "ds_read_b128 v[156:159], v17\n\t"
      "ds_read_b128 v[160:163], v17 offset:1024\n\t"
      "ds_read_b128 v[164:167], v17 offset:2048\n\t"
      "ds_read_b128 v[168:171], v18\n\t"
      "ds_read_b128 v[172:175], v18 offset:1024\n\t"
      "ds_read_b128 v[176:179], v18 offset:2048\n\t"
      "ds_read_b128 v[180:183], v19\n\t"
      "ds_read_b128 v[184:187], v19 offset:1024\n\t"
      "ds_read_b128 v[188:191], v19 offset:2048\n\t"
      "s_waitcnt vmcnt(16)\n\t"
      "v_mfma_f32_16x16x32_bf16 v[0:3], a[0:3], a[112:115], 0\n\t"
      "ds_read_b64_tr_b16 a[32:33], v220 offset:8192\n\t"
      "v_mfma_f32_16x16x32_bf16 v[4:7], a[0:3], a[116:119], 0\n\t"
      "ds_read_b64_tr_b16 a[34:35], v220 offset:12288\n\t"
      "v_mfma_f32_16x16x32_bf16 v[8:11], a[0:3], a[120:123], 0\n\t"
      "ds_read_b64_tr_b16 a[36:37], v221 offset:8192\n\t"
      "v_mfma_f32_16x16x32_bf16 v[12:15], a[4:7], a[112:115], 0\n\t"
      "ds_read_b64_tr_b16 a[38:39], v221 offset:12288\n\t"
      "s_mov_b32 m0, s90\n\t"
      "v_mfma_f32_16x16x32_bf16 v[16:19], a[4:7], a[116:119], 0\n\t"
      "buffer_load_dwordx4 v208, s[44:47], s18 offen lds\n\t"
      "v_mfma_f32_16x16x32_bf16 v[20:23], a[4:7], a[120:123], 0\n\t"
      "ds_read_b64_tr_b16 a[40:41], v222 offset:8192\n\t"
      "v_mfma_f32_16x16x32_bf16 v[24:27], a[8:11], a[112:115], 0\n\t"
      "ds_read_b64_tr_b16 a[42:43], v222 offset:12288\n\t"
      "v_mfma_f32_16x16x32_bf16 v[28:31], a[8:11], a[116:119], 0\n\t"
      "ds_read_b64_tr_b16 a[44:45], v223 offset:8192\n\t"
      "v_mfma_f32_16x16x32_bf16 v[32:35], a[8:11], a[120:123], 0\n\t"
      "buffer_load_dwordx4 v209, s[44:47], s18 offen offset:1024 lds\n\t"
      "v_mfma_f32_16x16x32_bf16 v[36:39], a[12:15], a[112:115], 0\n\t"
      "ds_read_b64_tr_b16 a[46:47], v223 offset:12288\n\t"
      "v_mfma_f32_16x16x32_bf16 v[40:43], a[12:15], a[116:119], 0\n\t"
      "ds_read_b64_tr_b16 a[48:49], v224 offset:8192\n\t"
      "v_mfma_f32_16x16x32_bf16 v[44:47], a[12:15], a[120:123], 0\n\t"
      "ds_read_b64_tr_b16 a[50:51], v224 offset:12288\n\t"
      "v_mfma_f32_16x16x32_bf16 v[48:51], a[16:19], a[112:115], 0\n\t"
      "buffer_load_dwordx4 v210, s[44:47], s18 offen offset:2048 lds\n\t"
      "v_mfma_f32_16x16x32_bf16 v[52:55], a[16:19], a[116:119], 0\n\t"
      "ds_read_b64_tr_b16 a[52:53], v225 offset:8192\n\t"
      "v_mfma_f32_16x16x32_bf16 v[56:59], a[16:19], a[120:123], 0\n\t"
      "ds_read_b64_tr_b16 a[54:55], v225 offset:12288\n\t"
      "v_mfma_f32_16x16x32_bf16 v[60:63], a[20:23], a[112:115], 0\n\t"
      "ds_read_b64_tr_b16 a[56:57], v226 offset:8192\n\t"
      "v_mfma_f32_16x16x32_bf16 v[64:67], a[20:23], a[116:119], 0\n\t"
      "buffer_load_dwordx4 v211, s[44:47], s18 offen offset:3072 lds\n\t"
      "v_mfma_f32_16x16x32_bf16 v[68:71], a[20:23], a[120:123], 0\n\t"
      "ds_read_b64_tr_b16 a[58:59], v226 offset:12288\n\t"
      "v_mfma_f32_16x16x32_bf16 v[72:75], a[24:27], a[112:115], 0\n\t"
      "ds_read_b64_tr_b16 a[60:61], v227 offset:8192\n\t"
      "v_mfma_f32_16x16x32_bf16 v[76:79], a[24:27], a[116:119], 0\n\t"
      "ds_read_b64_tr_b16 a[62:63], v227 offset:12288\n\t"
      "s_add_u32 s18, s18, 0x4000\n\t"
      "s_and_b32 s18, s18, 0x3ffff\n\t"
      "s_mov_b32 m0, s91\n\t"
      "v_mfma_f32_16x16x32_bf16 v[80:83], a[24:27], a[120:123], 0\n\t"
      "buffer_load_dwordx4 v208, s[44:47], s18 offen lds\n\t"
      "v_mfma_f32_16x16x32_bf16 v[84:87], a[28:31], a[112:115], 0\n\t"
      "buffer_load_dwordx4 v209, s[44:47], s18 offen offset:1024 lds\n\t"
      "v_mfma_f32_16x16x32_bf16 v[88:91], a[28:31], a[116:119], 0\n\t"
      "buffer_load_dwordx4 v210, s[44:47], s18 offen offset:2048 lds\n\t"
      "v_mfma_f32_16x16x32_bf16 v[92:95], a[28:31], a[120:123], 0\n\t"
      "buffer_load_dwordx4 v211, s[44:47], s18 offen offset:3072 lds\n\t"
      "s_add_u32 s18, s18, 0x4000\n\t"
      "s_and_b32 s18, s18, 0x3ffff\n\t"
      "s_waitcnt lgkmcnt(0)\n\t"
      "s_waitcnt vmcnt(16)\n\t"
      "v_mfma_f32_16x16x32_bf16 v[0:3], a[32:35], a[124:127], v[0:3]\n\t"
      "ds_read_b64_tr_b16 a[0:1], v220 offset:16384\n\t"
      "v_mfma_f32_16x16x32_bf16 v[4:7], a[32:35], a[128:131], v[4:7]\n\t"
      "ds_read_b64_tr_b16 a[2:3], v220 offset:20480\n\t"
      "v_mfma_f32_16x16x32_bf16 v[8:11], a[32:35], a[132:135], v[8:11]\n\t"
      "ds_read_b64_tr_b16 a[4:5], v221 offset:16384\n\t"
      "v_mfma_f32_16x16x32_bf16 v[12:15], a[36:39], a[124:127], v[12:15]\n\t"
      "ds_read_b64_tr_b16 a[6:7], v221 offset:20480\n\t"
      "s_mov_b32 m0, s92\n\t"
      "v_mfma_f32_16x16x32_bf16 v[16:19], a[36:39], a[128:131], v[16:19]\n\t"
      "buffer_load_dwordx4 v208, s[44:47], s18 offen lds\n\t"
      "v_mfma_f32_16x16x32_bf16 v[20:23], a[36:39], a[132:135], v[20:23]\n\t"
      "ds_read_b64_tr_b16 a[8:9], v222 offset:16384\n\t"
      "v_mfma_f32_16x16x32_bf16 v[24:27], a[40:43], a[124:127], v[24:27]\n\t"
      "ds_read_b64_tr_b16 a[10:11], v222 offset:20480\n\t"
      "v_mfma_f32_16x16x32_bf16 v[28:31], a[40:43], a[128:131], v[28:31]\n\t"
      "ds_read_b64_tr_b16 a[12:13], v223 offset:16384\n\t"
      "v_mfma_f32_16x16x32_bf16 v[32:35], a[40:43], a[132:135], v[32:35]\n\t"
      "buffer_load_dwordx4 v209, s[44:47], s18 offen offset:1024 lds\n\t"
      "v_mfma_f32_16x16x32_bf16 v[36:39], a[44:47], a[124:127], v[36:39]\n\t"
      "ds_read_b64_tr_b16 a[14:15], v223 offset:20480\n\t"
      "v_mfma_f32_16x16x32_bf16 v[40:43], a[44:47], a[128:131], v[40:43]\n\t"
      "ds_read_b64_tr_b16 a[16:17], v224 offset:16384\n\t"
      "v_mfma_f32_16x16x32_bf16 v[44:47], a[44:47], a[132:135], v[44:47]\n\t"
      "ds_read_b64_tr_b16 a[18:19], v224 offset:20480\n\t"
      "v_mfma_f32_16x16x32_bf16 v[48:51], a[48:51], a[124:127], v[48:51]\n\t"
      "buffer_load_dwordx4 v210, s[44:47], s18 offen offset:2048 lds\n\t"
      "v_mfma_f32_16x16x32_bf16 v[52:55], a[48:51], a[128:131], v[52:55]\n\t"
      "ds_read_b64_tr_b16 a[20:21], v225 offset:16384\n\t"
      "v_mfma_f32_16x16x32_bf16 v[56:59], a[48:51], a[132:135], v[56:59]\n\t"
      "ds_read_b64_tr_b16 a[22:23], v225 offset:20480\n\t"
      "v_mfma_f32_16x16x32_bf16 v[60:63], a[52:55], a[124:127], v[60:63]\n\t"
      "ds_read_b64_tr_b16 a[24:25], v226 offset:16384\n\t"
      "v_mfma_f32_16x16x32_bf16 v[64:67], a[52:55], a[128:131], v[64:67]\n\t"
      "buffer_load_dwordx4 v211, s[44:47], s18 offen offset:3072 lds\n\t"
      "v_mfma_f32_16x16x32_bf16 v[68:71], a[52:55], a[132:135], v[68:71]\n\t"
      "ds_read_b64_tr_b16 a[26:27], v226 offset:20480\n\t"
      "v_mfma_f32_16x16x32_bf16 v[72:75], a[56:59], a[124:127], v[72:75]\n\t"
      "ds_read_b64_tr_b16 a[28:29], v227 offset:16384\n\t"
      "v_mfma_f32_16x16x32_bf16 v[76:79], a[56:59], a[128:131], v[76:79]\n\t"
      "ds_read_b64_tr_b16 a[30:31], v227 offset:20480\n\t"
      "s_add_u32 s18, s18, 0x4000\n\t"
      "s_and_b32 s18, s18, 0x3ffff\n\t"
      "s_mov_b32 m0, s93\n\t"
      "v_mfma_f32_16x16x32_bf16 v[80:83], a[56:59], a[132:135], v[80:83]\n\t"
      "buffer_load_dwordx4 v208, s[44:47], s18 offen lds\n\t"
      "v_mfma_f32_16x16x32_bf16 v[84:87], a[60:63], a[124:127], v[84:87]\n\t"
      "buffer_load_dwordx4 v209, s[44:47], s18 offen offset:1024 lds\n\t"
      "v_mfma_f32_16x16x32_bf16 v[88:91], a[60:63], a[128:131], v[88:91]\n\t"
      "buffer_load_dwordx4 v210, s[44:47], s18 offen offset:2048 lds\n\t"
      "v_mfma_f32_16x16x32_bf16 v[92:95], a[60:63], a[132:135], v[92:95]\n\t"
      "buffer_load_dwordx4 v211, s[44:47], s18 offen offset:3072 lds\n\t"
      "s_add_u32 s18, s18, 0x4000\n\t"
      "s_and_b32 s18, s18, 0x3ffff\n\t"
      "s_waitcnt lgkmcnt(0)\n\t"
      "s_waitcnt vmcnt(16)\n\t"
      "v_mfma_f32_16x16x32_bf16 v[0:3], a[0:3], a[136:139], v[0:3]\n\t"
      "ds_read_b64_tr_b16 a[32:33], v220 offset:24576\n\t"
      "v_mfma_f32_16x16x32_bf16 v[4:7], a[0:3], a[140:143], v[4:7]\n\t"
      "ds_read_b64_tr_b16 a[34:35], v220 offset:28672\n\t"
      "v_mfma_f32_16x16x32_bf16 v[8:11], a[0:3], a[144:147], v[8:11]\n\t"
      "ds_read_b64_tr_b16 a[36:37], v221 offset:24576\n\t"
      "v_mfma_f32_16x16x32_bf16 v[12:15], a[4:7], a[136:139], v[12:15]\n\t"
      "ds_read_b64_tr_b16 a[38:39], v221 offset:28672\n\t"
      "s_mov_b32 m0, s94\n\t"
      "v_mfma_f32_16x16x32_bf16 v[16:19], a[4:7], a[140:143], v[16:19]\n\t"
      "buffer_load_dwordx4 v208, s[44:47], s18 offen lds\n\t"
      "v_mfma_f32_16x16x32_bf16 v[20:23], a[4:7], a[144:147], v[20:23]\n\t"
      "ds_read_b64_tr_b16 a[40:41], v222 offset:24576\n\t"
      "v_mfma_f32_16x16x32_bf16 v[24:27], a[8:11], a[136:139], v[24:27]\n\t"
      "ds_read_b64_tr_b16 a[42:43], v222 offset:28672\n\t"
      "v_mfma_f32_16x16x32_bf16 v[28:31], a[8:11], a[140:143], v[28:31]\n\t"
      "ds_read_b64_tr_b16 a[44:45], v223 offset:24576\n\t"
      "v_mfma_f32_16x16x32_bf16 v[32:35], a[8:11], a[144:147], v[32:35]\n\t"
      "buffer_load_dwordx4 v209, s[44:47], s18 offen offset:1024 lds\n\t"
      "v_mfma_f32_16x16x32_bf16 v[36:39], a[12:15], a[136:139], v[36:39]\n\t"
      "ds_read_b64_tr_b16 a[46:47], v223 offset:28672\n\t"
      "v_mfma_f32_16x16x32_bf16 v[40:43], a[12:15], a[140:143], v[40:43]\n\t"
      "ds_read_b64_tr_b16 a[48:49], v224 offset:24576\n\t"
      "v_mfma_f32_16x16x32_bf16 v[44:47], a[12:15], a[144:147], v[44:47]\n\t"
      "ds_read_b64_tr_b16 a[50:51], v224 offset:28672\n\t"
      "v_mfma_f32_16x16x32_bf16 v[48:51], a[16:19], a[136:139], v[48:51]\n\t"
      "buffer_load_dwordx4 v210, s[44:47], s18 offen offset:2048 lds\n\t"
      "v_mfma_f32_16x16x32_bf16 v[52:55], a[16:19], a[140:143], v[52:55]\n\t"
      "ds_read_b64_tr_b16 a[52:53], v225 offset:24576\n\t"
      "v_mfma_f32_16x16x32_bf16 v[56:59], a[16:19], a[144:147], v[56:59]\n\t"
      "ds_read_b64_tr_b16 a[54:55], v225 offset:28672\n\t"
      "v_mfma_f32_16x16x32_bf16 v[60:63], a[20:23], a[136:139], v[60:63]\n\t"
      "ds_read_b64_tr_b16 a[56:57], v226 offset:24576\n\t"
      "v_mfma_f32_16x16x32_bf16 v[64:67], a[20:23], a[140:143], v[64:67]\n\t"
      "buffer_load_dwordx4 v211, s[44:47], s18 offen offset:3072 lds\n\t"
      "v_mfma_f32_16x16x32_bf16 v[68:71], a[20:23], a[144:147], v[68:71]\n\t"
      "ds_read_b64_tr_b16 a[58:59], v226 offset:28672\n\t"
      "v_mfma_f32_16x16x32_bf16 v[72:75], a[24:27], a[136:139], v[72:75]\n\t"
      "ds_read_b64_tr_b16 a[60:61], v227 offset:24576\n\t"
      "v_mfma_f32_16x16x32_bf16 v[76:79], a[24:27], a[140:143], v[76:79]\n\t"
      "ds_read_b64_tr_b16 a[62:63], v227 offset:28672\n\t"
      "s_add_u32 s18, s18, 0x4000\n\t"
      "s_and_b32 s18, s18, 0x3ffff\n\t"
      "s_mov_b32 m0, s95\n\t"
      "v_mfma_f32_16x16x32_bf16 v[80:83], a[24:27], a[144:147], v[80:83]\n\t"
      "buffer_load_dwordx4 v208, s[44:47], s18 offen lds\n\t"
      "v_mfma_f32_16x16x32_bf16 v[84:87], a[28:31], a[136:139], v[84:87]\n\t"
      "buffer_load_dwordx4 v209, s[44:47], s18 offen offset:1024 lds\n\t"
      "v_mfma_f32_16x16x32_bf16 v[88:91], a[28:31], a[140:143], v[88:91]\n\t"
      "buffer_load_dwordx4 v210, s[44:47], s18 offen offset:2048 lds\n\t"
      "v_mfma_f32_16x16x32_bf16 v[92:95], a[28:31], a[144:147], v[92:95]\n\t"
      "buffer_load_dwordx4 v211, s[44:47], s18 offen offset:3072 lds\n\t"
      "s_add_u32 s18, s18, 0x4000\n\t"
      "s_and_b32 s18, s18, 0x3ffff\n\t"
      "s_waitcnt lgkmcnt(0)\n\t"
      "s_waitcnt vmcnt(16)\n\t"
      "v_mfma_f32_16x16x32_bf16 v[0:3], a[32:35], a[148:151], v[0:3]\n\t"
      "ds_read_b64_tr_b16 a[0:1], v220\n\t"
      "v_mfma_f32_16x16x32_bf16 v[4:7], a[32:35], a[152:155], v[4:7]\n\t"
      "ds_read_b64_tr_b16 a[2:3], v220 offset:4096\n\t"
      "v_mfma_f32_16x16x32_bf16 v[8:11], a[32:35], a[156:159], v[8:11]\n\t"
      "ds_read_b64_tr_b16 a[4:5], v221\n\t"
      "v_mfma_f32_16x16x32_bf16 v[12:15], a[36:39], a[148:151], v[12:15]\n\t"
      "ds_read_b64_tr_b16 a[6:7], v221 offset:4096\n\t"
      "s_mov_b32 m0, s96\n\t"
      "v_mfma_f32_16x16x32_bf16 v[16:19], a[36:39], a[152:155], v[16:19]\n\t"
      "buffer_load_dwordx4 v208, s[44:47], s18 offen lds\n\t"
      "v_mfma_f32_16x16x32_bf16 v[20:23], a[36:39], a[156:159], v[20:23]\n\t"
      "ds_read_b64_tr_b16 a[8:9], v222\n\t"
      "v_mfma_f32_16x16x32_bf16 v[24:27], a[40:43], a[148:151], v[24:27]\n\t"
      "ds_read_b64_tr_b16 a[10:11], v222 offset:4096\n\t"
      "v_mfma_f32_16x16x32_bf16 v[28:31], a[40:43], a[152:155], v[28:31]\n\t"
      "ds_read_b64_tr_b16 a[12:13], v223\n\t"
      "v_mfma_f32_16x16x32_bf16 v[32:35], a[40:43], a[156:159], v[32:35]\n\t"
      "buffer_load_dwordx4 v209, s[44:47], s18 offen offset:1024 lds\n\t"
      "v_mfma_f32_16x16x32_bf16 v[36:39], a[44:47], a[148:151], v[36:39]\n\t"
      "ds_read_b64_tr_b16 a[14:15], v223 offset:4096\n\t"
      "v_mfma_f32_16x16x32_bf16 v[40:43], a[44:47], a[152:155], v[40:43]\n\t"
      "ds_read_b64_tr_b16 a[16:17], v224\n\t"
      "v_mfma_f32_16x16x32_bf16 v[44:47], a[44:47], a[156:159], v[44:47]\n\t"
      "ds_read_b64_tr_b16 a[18:19], v224 offset:4096\n\t"
      "v_mfma_f32_16x16x32_bf16 v[48:51], a[48:51], a[148:151], v[48:51]\n\t"
      "buffer_load_dwordx4 v210, s[44:47], s18 offen offset:2048 lds\n\t"
      "v_mfma_f32_16x16x32_bf16 v[52:55], a[48:51], a[152:155], v[52:55]\n\t"
      "ds_read_b64_tr_b16 a[20:21], v225\n\t"
      "v_mfma_f32_16x16x32_bf16 v[56:59], a[48:51], a[156:159], v[56:59]\n\t"
      "ds_read_b64_tr_b16 a[22:23], v225 offset:4096\n\t"
      "v_mfma_f32_16x16x32_bf16 v[60:63], a[52:55], a[148:151], v[60:63]\n\t"
      "ds_read_b64_tr_b16 a[24:25], v226\n\t"
      "v_mfma_f32_16x16x32_bf16 v[64:67], a[52:55], a[152:155], v[64:67]\n\t"
      "buffer_load_dwordx4 v211, s[44:47], s18 offen offset:3072 lds\n\t"
      "v_mfma_f32_16x16x32_bf16 v[68:71], a[52:55], a[156:159], v[68:71]\n\t"
      "ds_read_b64_tr_b16 a[26:27], v226 offset:4096\n\t"
      "v_mfma_f32_16x16x32_bf16 v[72:75], a[56:59], a[148:151], v[72:75]\n\t"
      "ds_read_b64_tr_b16 a[28:29], v227\n\t"
      "v_mfma_f32_16x16x32_bf16 v[76:79], a[56:59], a[152:155], v[76:79]\n\t"
      "ds_read_b64_tr_b16 a[30:31], v227 offset:4096\n\t"
      "s_add_u32 s18, s18, 0x4000\n\t"
      "s_and_b32 s18, s18, 0x3ffff\n\t"
      "s_mov_b32 m0, s97\n\t"
      "v_mfma_f32_16x16x32_bf16 v[80:83], a[56:59], a[156:159], v[80:83]\n\t"
      "buffer_load_dwordx4 v208, s[44:47], s18 offen lds\n\t"
      "v_mfma_f32_16x16x32_bf16 v[84:87], a[60:63], a[148:151], v[84:87]\n\t"
      "buffer_load_dwordx4 v209, s[44:47], s18 offen offset:1024 lds\n\t"
      "v_mfma_f32_16x16x32_bf16 v[88:91], a[60:63], a[152:155], v[88:91]\n\t"
      "buffer_load_dwordx4 v210, s[44:47], s18 offen offset:2048 lds\n\t"
      "v_mfma_f32_16x16x32_bf16 v[92:95], a[60:63], a[156:159], v[92:95]\n\t"
      "buffer_load_dwordx4 v211, s[44:47], s18 offen offset:3072 lds\n\t"
      "s_add_u32 s18, s18, 0x4000\n\t"
      "s_and_b32 s18, s18, 0x3ffff\n\t"
      "s_add_u32 s44, s44, 0x40000\n\t"
      "s_addc_u32 s45, s45, 0\n\t"
      "s_sub_u32 s46, s46, 0x40000\n\t"
      "s_waitcnt lgkmcnt(0)\n\t"
      "s_waitcnt vmcnt(16)\n\t"
      "v_mfma_f32_16x16x32_bf16 v[0:3], a[0:3], a[160:163], v[0:3]\n\t"
      "ds_read_b64_tr_b16 a[32:33], v220 offset:8192\n\t"
      "v_mfma_f32_16x16x32_bf16 v[4:7], a[0:3], a[164:167], v[4:7]\n\t"
      "ds_read_b64_tr_b16 a[34:35], v220 offset:12288\n\t"
      "v_mfma_f32_16x16x32_bf16 v[8:11], a[0:3], a[168:171], v[8:11]\n\t"
      "ds_read_b64_tr_b16 a[36:37], v221 offset:8192\n\t"
      "v_mfma_f32_16x16x32_bf16 v[12:15], a[4:7], a[160:163], v[12:15]\n\t"
      "ds_read_b64_tr_b16 a[38:39], v221 offset:12288\n\t"
      "s_mov_b32 m0, s90\n\t"
      "v_mfma_f32_16x16x32_bf16 v[16:19], a[4:7], a[164:167], v[16:19]\n\t"
      "buffer_load_dwordx4 v208, s[44:47], s18 offen lds\n\t"
      "v_mfma_f32_16x16x32_bf16 v[20:23], a[4:7], a[168:171], v[20:23]\n\t"
      "ds_read_b64_tr_b16 a[40:41], v222 offset:8192\n\t"
      "v_mfma_f32_16x16x32_bf16 v[24:27], a[8:11], a[160:163], v[24:27]\n\t"
      "ds_read_b64_tr_b16 a[42:43], v222 offset:12288\n\t"
      "v_mfma_f32_16x16x32_bf16 v[28:31], a[8:11], a[164:167], v[28:31]\n\t"
      "ds_read_b64_tr_b16 a[44:45], v223 offset:8192\n\t"
      "v_mfma_f32_16x16x32_bf16 v[32:35], a[8:11], a[168:171], v[32:35]\n\t"
      "buffer_load_dwordx4 v209, s[44:47], s18 offen offset:1024 lds\n\t"
      "v_mfma_f32_16x16x32_bf16 v[36:39], a[12:15], a[160:163], v[36:39]\n\t"
      "ds_read_b64_tr_b16 a[46:47], v223 offset:12288\n\t"
      "v_mfma_f32_16x16x32_bf16 v[40:43], a[12:15], a[164:167], v[40:43]\n\t"
      "ds_read_b64_tr_b16 a[48:49], v224 offset:8192\n\t"
      "v_mfma_f32_16x16x32_bf16 v[44:47], a[12:15], a[168:171], v[44:47]\n\t"
      "ds_read_b64_tr_b16 a[50:51], v224 offset:12288\n\t"
      "v_mfma_f32_16x16x32_bf16 v[48:51], a[16:19], a[160:163], v[48:51]\n\t"
      "buffer_load_dwordx4 v210, s[44:47], s18 offen offset:2048 lds\n\t"
      "v_mfma_f32_16x16x32_bf16 v[52:55], a[16:19], a[164:167], v[52:55]\n\t"
      "ds_read_b64_tr_b16 a[52:53], v225 offset:8192\n\t"
      "v_mfma_f32_16x16x32_bf16 v[56:59], a[16:19], a[168:171], v[56:59]\n\t"
      "ds_read_b64_tr_b16 a[54:55], v225 offset:12288\n\t"
      "v_mfma_f32_16x16x32_bf16 v[60:63], a[20:23], a[160:163], v[60:63]\n\t"
      "ds_read_b64_tr_b16 a[56:57], v226 offset:8192\n\t"
      "v_mfma_f32_16x16x32_bf16 v[64:67], a[20:23], a[164:167], v[64:67]\n\t"
      "buffer_load_dwordx4 v211, s[44:47], s18 offen offset:3072 lds\n\t"
      "v_mfma_f32_16x16x32_bf16 v[68:71], a[20:23], a[168:171], v[68:71]\n\t"
      "ds_read_b64_tr_b16 a[58:59], v226 offset:12288\n\t"
      "v_mfma_f32_16x16x32_bf16 v[72:75], a[24:27], a[160:163], v[72:75]\n\t"
      "ds_read_b64_tr_b16 a[60:61], v227 offset:8192\n\t"
      "v_mfma_f32_16x16x32_bf16 v[76:79], a[24:27], a[164:167], v[76:79]\n\t"
      "ds_read_b64_tr_b16 a[62:63], v227 offset:12288\n\t"
      "s_add_u32 s18, s18, 0x4000\n\t"
      "s_and_b32 s18, s18, 0x3ffff\n\t"
      "s_mov_b32 m0, s91\n\t"
      "v_mfma_f32_16x16x32_bf16 v[80:83], a[24:27], a[168:171], v[80:83]\n\t"
      "buffer_load_dwordx4 v208, s[44:47], s18 offen lds\n\t"
      "v_mfma_f32_16x16x32_bf16 v[84:87], a[28:31], a[160:163], v[84:87]\n\t"
      "buffer_load_dwordx4 v209, s[44:47], s18 offen offset:1024 lds\n\t"
      "v_mfma_f32_16x16x32_bf16 v[88:91], a[28:31], a[164:167], v[88:91]\n\t"
      "buffer_load_dwordx4 v210, s[44:47], s18 offen offset:2048 lds\n\t"
      "v_mfma_f32_16x16x32_bf16 v[92:95], a[28:31], a[168:171], v[92:95]\n\t"
      "buffer_load_dwordx4 v211, s[44:47], s18 offen offset:3072 lds\n\t"
      "s_add_u32 s18, s18, 0x4000\n\t"
      "s_and_b32 s18, s18, 0x3ffff\n\t"
      "s_waitcnt lgkmcnt(0)\n\t"
      "s_waitcnt vmcnt(16)\n\t"
      "v_mfma_f32_16x16x32_bf16 v[0:3], a[32:35], a[172:175], v[0:3]\n\t"
      "ds_read_b64_tr_b16 a[0:1], v220 offset:16384\n\t"
      "v_mfma_f32_16x16x32_bf16 v[4:7], a[32:35], a[176:179], v[4:7]\n\t"
      "ds_read_b64_tr_b16 a[2:3], v220 offset:20480\n\t"
      "v_mfma_f32_16x16x32_bf16 v[8:11], a[32:35], a[180:183], v[8:11]\n\t"
      "ds_read_b64_tr_b16 a[4:5], v221 offset:16384\n\t"
      "v_mfma_f32_16x16x32_bf16 v[12:15], a[36:39], a[172:175], v[12:15]\n\t"
      "ds_read_b64_tr_b16 a[6:7], v221 offset:20480\n\t"
      "s_mov_b32 m0, s92\n\t"
      "v_mfma_f32_16x16x32_bf16 v[16:19], a[36:39], a[176:179], v[16:19]\n\t"
      "buffer_load_dwordx4 v208, s[44:47], s18 offen lds\n\t"
      "v_mfma_f32_16x16x32_bf16 v[20:23], a[36:39], a[180:183], v[20:23]\n\t"
      "ds_read_b64_tr_b16 a[8:9], v222 offset:16384\n\t"
      "v_mfma_f32_16x16x32_bf16 v[24:27], a[40:43], a[172:175], v[24:27]\n\t"
      "ds_read_b64_tr_b16 a[10:11], v222 offset:20480\n\t"
      "v_mfma_f32_16x16x32_bf16 v[28:31], a[40:43], a[176:179], v[28:31]\n\t"
      "ds_read_b64_tr_b16 a[12:13], v223 offset:16384\n\t"
      "v_mfma_f32_16x16x32_bf16 v[32:35], a[40:43], a[180:183], v[32:35]\n\t"
      "buffer_load_dwordx4 v209, s[44:47], s18 offen offset:1024 lds\n\t"
      "v_mfma_f32_16x16x32_bf16 v[36:39], a[44:47], a[172:175], v[36:39]\n\t"
      "ds_read_b64_tr_b16 a[14:15], v223 offset:20480\n\t"
      "v_mfma_f32_16x16x32_bf16 v[40:43], a[44:47], a[176:179], v[40:43]\n\t"
      "ds_read_b64_tr_b16 a[16:17], v224 offset:16384\n\t"
      "v_mfma_f32_16x16x32_bf16 v[44:47], a[44:47], a[180:183], v[44:47]\n\t"
      "ds_read_b64_tr_b16 a[18:19], v224 offset:20480\n\t"
      "v_mfma_f32_16x16x32_bf16 v[48:51], a[48:51], a[172:175], v[48:51]\n\t"
      "buffer_load_dwordx4 v210, s[44:47], s18 offen offset:2048 lds\n\t"
      "v_mfma_f32_16x16x32_bf16 v[52:55], a[48:51], a[176:179], v[52:55]\n\t"
      "ds_read_b64_tr_b16 a[20:21], v225 offset:16384\n\t"
      "v_mfma_f32_16x16x32_bf16 v[56:59], a[48:51], a[180:183], v[56:59]\n\t"
      "ds_read_b64_tr_b16 a[22:23], v225 offset:20480\n\t"
      "v_mfma_f32_16x16x32_bf16 v[60:63], a[52:55], a[172:175], v[60:63]\n\t"
      "ds_read_b64_tr_b16 a[24:25], v226 offset:16384\n\t"
      "v_mfma_f32_16x16x32_bf16 v[64:67], a[52:55], a[176:179], v[64:67]\n\t"
      "buffer_load_dwordx4 v211, s[44:47], s18 offen offset:3072 lds\n\t"
      "v_mfma_f32_16x16x32_bf16 v[68:71], a[52:55], a[180:183], v[68:71]\n\t"
      "ds_read_b64_tr_b16 a[26:27], v226 offset:20480\n\t"
      "v_mfma_f32_16x16x32_bf16 v[72:75], a[56:59], a[172:175], v[72:75]\n\t"
      "ds_read_b64_tr_b16 a[28:29], v227 offset:16384\n\t"
      "v_mfma_f32_16x16x32_bf16 v[76:79], a[56:59], a[176:179], v[76:79]\n\t"
      "ds_read_b64_tr_b16 a[30:31], v227 offset:20480\n\t"
      "s_add_u32 s18, s18, 0x4000\n\t"
      "s_and_b32 s18, s18, 0x3ffff\n\t"
      "s_mov_b32 m0, s93\n\t"
      "v_mfma_f32_16x16x32_bf16 v[80:83], a[56:59], a[180:183], v[80:83]\n\t"
      "buffer_load_dwordx4 v208, s[44:47], s18 offen lds\n\t"
      "v_mfma_f32_16x16x32_bf16 v[84:87], a[60:63], a[172:175], v[84:87]\n\t"
      "buffer_load_dwordx4 v209, s[44:47], s18 offen offset:1024 lds\n\t"
      "v_mfma_f32_16x16x32_bf16 v[88:91], a[60:63], a[176:179], v[88:91]\n\t"
      "buffer_load_dwordx4 v210, s[44:47], s18 offen offset:2048 lds\n\t"
      "v_mfma_f32_16x16x32_bf16 v[92:95], a[60:63], a[180:183], v[92:95]\n\t"
      "buffer_load_dwordx4 v211, s[44:47], s18 offen offset:3072 lds\n\t"
      "s_add_u32 s18, s18, 0x4000\n\t"
      "s_and_b32 s18, s18, 0x3ffff\n\t"
      "s_waitcnt lgkmcnt(0)\n\t"
      "s_waitcnt vmcnt(16)\n\t"
      "v_mfma_f32_16x16x32_bf16 v[0:3], a[0:3], a[184:187], v[0:3]\n\t"
      "ds_read_b64_tr_b16 a[32:33], v220 offset:24576\n\t"
      "v_mfma_f32_16x16x32_bf16 v[4:7], a[0:3], a[188:191], v[4:7]\n\t"
      "ds_read_b64_tr_b16 a[34:35], v220 offset:28672\n\t"
      "v_mfma_f32_16x16x32_bf16 v[8:11], a[0:3], a[192:195], v[8:11]\n\t"
      "ds_read_b64_tr_b16 a[36:37], v221 offset:24576\n\t"
      "v_mfma_f32_16x16x32_bf16 v[12:15], a[4:7], a[184:187], v[12:15]\n\t"
      "ds_read_b64_tr_b16 a[38:39], v221 offset:28672\n\t"
      "s_mov_b32 m0, s94\n\t"
      "v_mfma_f32_16x16x32_bf16 v[16:19], a[4:7], a[188:191], v[16:19]\n\t"
      "buffer_load_dwordx4 v208, s[44:47], s18 offen lds\n\t"
      "v_mfma_f32_16x16x32_bf16 v[20:23], a[4:7], a[192:195], v[20:23]\n\t"
      "ds_read_b64_tr_b16 a[40:41], v222 offset:24576\n\t"
      "v_mfma_f32_16x16x32_bf16 v[24:27], a[8:11], a[184:187], v[24:27]\n\t"
      "ds_read_b64_tr_b16 a[42:43], v222 offset:28672\n\t"
      "v_mfma_f32_16x16x32_bf16 v[28:31], a[8:11], a[188:191], v[28:31]\n\t"
      "ds_read_b64_tr_b16 a[44:45], v223 offset:24576\n\t"
      "v_mfma_f32_16x16x32_bf16 v[32:35], a[8:11], a[192:195], v[32:35]\n\t"
      "buffer_load_dwordx4 v209, s[44:47], s18 offen offset:1024 lds\n\t"
      "v_mfma_f32_16x16x32_bf16 v[36:39], a[12:15], a[184:187], v[36:39]\n\t"
      "ds_read_b64_tr_b16 a[46:47], v223 offset:28672\n\t"
      "v_mfma_f32_16x16x32_bf16 v[40:43], a[12:15], a[188:191], v[40:43]\n\t"
      "ds_read_b64_tr_b16 a[48:49], v224 offset:24576\n\t"
      "v_mfma_f32_16x16x32_bf16 v[44:47], a[12:15], a[192:195], v[44:47]\n\t"
      "ds_read_b64_tr_b16 a[50:51], v224 offset:28672\n\t"
      "v_mfma_f32_16x16x32_bf16 v[48:51], a[16:19], a[184:187], v[48:51]\n\t"
      "buffer_load_dwordx4 v210, s[44:47], s18 offen offset:2048 lds\n\t"
      "v_mfma_f32_16x16x32_bf16 v[52:55], a[16:19], a[188:191], v[52:55]\n\t"
      "ds_read_b64_tr_b16 a[52:53], v225 offset:24576\n\t"
      "v_mfma_f32_16x16x32_bf16 v[56:59], a[16:19], a[192:195], v[56:59]\n\t"
      "ds_read_b64_tr_b16 a[54:55], v225 offset:28672\n\t"
      "v_mfma_f32_16x16x32_bf16 v[60:63], a[20:23], a[184:187], v[60:63]\n\t"
      "ds_read_b64_tr_b16 a[56:57], v226 offset:24576\n\t"
      "v_mfma_f32_16x16x32_bf16 v[64:67], a[20:23], a[188:191], v[64:67]\n\t"
      "buffer_load_dwordx4 v211, s[44:47], s18 offen offset:3072 lds\n\t"
      "v_mfma_f32_16x16x32_bf16 v[68:71], a[20:23], a[192:195], v[68:71]\n\t"
      "ds_read_b64_tr_b16 a[58:59], v226 offset:28672\n\t"
      "v_mfma_f32_16x16x32_bf16 v[72:75], a[24:27], a[184:187], v[72:75]\n\t"
      "ds_read_b64_tr_b16 a[60:61], v227 offset:24576\n\t"
      "v_mfma_f32_16x16x32_bf16 v[76:79], a[24:27], a[188:191], v[76:79]\n\t"
      "ds_read_b64_tr_b16 a[62:63], v227 offset:28672\n\t"
      "s_add_u32 s18, s18, 0x4000\n\t"
      "s_and_b32 s18, s18, 0x3ffff\n\t"
      "s_mov_b32 m0, s95\n\t"
      "v_mfma_f32_16x16x32_bf16 v[80:83], a[24:27], a[192:195], v[80:83]\n\t"
      "buffer_load_dwordx4 v208, s[44:47], s18 offen lds\n\t"
      "v_mfma_f32_16x16x32_bf16 v[84:87], a[28:31], a[184:187], v[84:87]\n\t"
      "buffer_load_dwordx4 v209, s[44:47], s18 offen offset:1024 lds\n\t"
      "v_mfma_f32_16x16x32_bf16 v[88:91], a[28:31], a[188:191], v[88:91]\n\t"
      "buffer_load_dwordx4 v210, s[44:47], s18 offen offset:2048 lds\n\t"
      "v_mfma_f32_16x16x32_bf16 v[92:95], a[28:31], a[192:195], v[92:95]\n\t"
      "buffer_load_dwordx4 v211, s[44:47], s18 offen offset:3072 lds\n\t"
      "s_add_u32 s18, s18, 0x4000\n\t"
      "s_and_b32 s18, s18, 0x3ffff\n\t"
      "s_waitcnt lgkmcnt(0)\n\t"
      "s_waitcnt vmcnt(16)\n\t"
      "v_mfma_f32_16x16x32_bf16 v[0:3], a[32:35], a[196:199], v[0:3]\n\t"
      "ds_read_b64_tr_b16 a[0:1], v220\n\t"
      "v_mfma_f32_16x16x32_bf16 v[4:7], a[32:35], a[200:203], v[4:7]\n\t"
      "ds_read_b64_tr_b16 a[2:3], v220 offset:4096\n\t"
      "v_mfma_f32_16x16x32_bf16 v[8:11], a[32:35], a[204:207], v[8:11]\n\t"
      "ds_read_b64_tr_b16 a[4:5], v221\n\t"
      "v_mfma_f32_16x16x32_bf16 v[12:15], a[36:39], a[196:199], v[12:15]\n\t"
      "ds_read_b64_tr_b16 a[6:7], v221 offset:4096\n\t"
      "s_mov_b32 m0, s96\n\t"
      "v_mfma_f32_16x16x32_bf16 v[16:19], a[36:39], a[200:203], v[16:19]\n\t"
      "buffer_load_dwordx4 v208, s[44:47], s18 offen lds\n\t"
      "v_mfma_f32_16x16x32_bf16 v[20:23], a[36:39], a[204:207], v[20:23]\n\t"
      "ds_read_b64_tr_b16 a[8:9], v222\n\t"
      "v_mfma_f32_16x16x32_bf16 v[24:27], a[40:43], a[196:199], v[24:27]\n\t"
      "ds_read_b64_tr_b16 a[10:11], v222 offset:4096\n\t"
      "v_mfma_f32_16x16x32_bf16 v[28:31], a[40:43], a[200:203], v[28:31]\n\t"
      "ds_read_b64_tr_b16 a[12:13], v223\n\t"
      "v_mfma_f32_16x16x32_bf16 v[32:35], a[40:43], a[204:207], v[32:35]\n\t"
      "buffer_load_dwordx4 v209, s[44:47], s18 offen offset:1024 lds\n\t"
      "v_mfma_f32_16x16x32_bf16 v[36:39], a[44:47], a[196:199], v[36:39]\n\t"
      "ds_read_b64_tr_b16 a[14:15], v223 offset:4096\n\t"
      "v_mfma_f32_16x16x32_bf16 v[40:43], a[44:47], a[200:203], v[40:43]\n\t"
      "ds_read_b64_tr_b16 a[16:17], v224\n\t"
      "v_mfma_f32_16x16x32_bf16 v[44:47], a[44:47], a[204:207], v[44:47]\n\t"
      "ds_read_b64_tr_b16 a[18:19], v224 offset:4096\n\t"
      "v_mfma_f32_16x16x32_bf16 v[48:51], a[48:51], a[196:199], v[48:51]\n\t"
      "buffer_load_dwordx4 v210, s[44:47], s18 offen offset:2048 lds\n\t"
      "v_mfma_f32_16x16x32_bf16 v[52:55], a[48:51], a[200:203], v[52:55]\n\t"
      "ds_read_b64_tr_b16 a[20:21], v225\n\t"
      "v_mfma_f32_16x16x32_bf16 v[56:59], a[48:51], a[204:207], v[56:59]\n\t"
      "ds_read_b64_tr_b16 a[22:23], v225 offset:4096\n\t"
      "v_mfma_f32_16x16x32_bf16 v[60:63], a[52:55], a[196:199], v[60:63]\n\t"
      "ds_read_b64_tr_b16 a[24:25], v226\n\t"
      "v_mfma_f32_16x16x32_bf16 v[64:67], a[52:55], a[200:203], v[64:67]\n\t"
      "buffer_load_dwordx4 v211, s[44:47], s18 offen offset:3072 lds\n\t"
      "v_mfma_f32_16x16x32_bf16 v[68:71], a[52:55], a[204:207], v[68:71]\n\t"
      "ds_read_b64_tr_b16 a[26:27], v226 offset:4096\n\t"
      "v_mfma_f32_16x16x32_bf16 v[72:75], a[56:59], a[196:199], v[72:75]\n\t"
      "ds_read_b64_tr_b16 a[28:29], v227\n\t"
      "v_mfma_f32_16x16x32_bf16 v[76:79], a[56:59], a[200:203], v[76:79]\n\t"
      "ds_read_b64_tr_b16 a[30:31], v227 offset:4096\n\t"
      "s_add_u32 s18, s18, 0x4000\n\t"
      "s_and_b32 s18, s18, 0x3ffff\n\t"
      "s_mov_b32 m0, s97\n\t"
      "v_mfma_f32_16x16x32_bf16 v[80:83], a[56:59], a[204:207], v[80:83]\n\t"
      "buffer_load_dwordx4 v208, s[44:47], s18 offen lds\n\t"
      "v_mfma_f32_16x16x32_bf16 v[84:87], a[60:63], a[196:199], v[84:87]\n\t"
      "buffer_load_dwordx4 v209, s[44:47], s18 offen offset:1024 lds\n\t"
      "v_mfma_f32_16x16x32_bf16 v[88:91], a[60:63], a[200:203], v[88:91]\n\t"
      "buffer_load_dwordx4 v210, s[44:47], s18 offen offset:2048 lds\n\t"
      "v_mfma_f32_16x16x32_bf16 v[92:95], a[60:63], a[204:207], v[92:95]\n\t"
      "buffer_load_dwordx4 v211, s[44:47], s18 offen offset:3072 lds\n\t"
      "s_add_u32 s18, s18, 0x4000\n\t"
      "s_and_b32 s18, s18, 0x3ffff\n\t"
      "s_waitcnt lgkmcnt(0)\n\t"
      "s_branch L_O_%=\n\t"
      "L_B_%=:\n\t"
      "s_lshl_b32 s18, s27, 15\n\t"
      "v_xad_u32 v220, v217, 0, s14\n\t"
      "v_xad_u32 v221, v217, 64, s14\n\t"
      "v_xor_b32 v222, 128, v217\n\t"
      "v_add_u32 v222, s14, v222\n\t"
      "v_xor_b32 v223, 192, v217\n\t"
      "v_add_u32 v223, s14, v223\n\t"
      "s_lshl_b32 s19, s8, 7\n\t"
      "v_xor_b32 v246, s19, v218\n\t"
      "v_xad_u32 v228, v246, 0, s14\n\t"
      "v_xad_u32 v229, v246, 32, s14\n\t"
      "v_xad_u32 v230, v246, 64, s14\n\t"
      "v_xor_b32 v231, 96, v246\n\t"
      "v_add_u32 v231, s14, v231\n\t"
      "v_xor_b32 v232, 128, v246\n\t"
      "v_add_u32 v232, s14, v232\n\t"
      "v_xor_b32 v233, 160, v246\n\t"
      "v_add_u32 v233, s14, v233\n\t"
      "v_xor_b32 v234, 192, v246\n\t"
      "v_add_u32 v234, s14, v234\n\t"
      "v_xor_b32 v235, 224, v246\n\t"
      "v_add_u32 v235, s14, v235\n\t"
      "ds_read_b128 a[0:3], v220\n\t"
      "ds_read_b128 a[4:7], v221\n\t"
      "ds_read_b128 a[8:11], v222\n\t"
      "ds_read_b128 a[12:15], v223\n\t"
      "ds_read_b128 a[16:19], v220 offset:4096\n\t"
      "ds_read_b128 a[20:23], v221 offset:4096\n\t"
      "ds_read_b128 a[24:27], v222 offset:4096\n\t"
      "ds_read_b128 a[28:31], v223 offset:4096\n\t"
      "s_waitcnt lgkmcnt(0)\n\t"
      "L_pass_%=:\n\t"
      "v_mov_b32 v236, 0xff800000\n\t"
      "v_mov_b32 v237, 0xff800000\n\t"
      "v_mov_b32 v238, 0xff800000\n\t"
      "s_waitcnt vmcnt(16)\n\t"
      "v_mfma_f32_16x16x32_bf16 v[0:3], a[0:3], a[64:67], 0\n\t"
      "ds_read_b128 a[32:35], v220 offset:8192\n\t"
      "s_mov_b32 m0, s90\n\t"
      "v_mfma_f32_16x16x32_bf16 v[0:3], a[4:7], a[76:79], v[0:3]\n\t"
      "buffer_load_dwordx4 v204, s[28:31], s60 offen nt lds\n\t"
      "v_mfma_f32_16x16x32_bf16 v[0:3], a[8:11], a[88:91], v[0:3]\n\t"
      "ds_read_b128 a[36:39], v221 offset:8192\n\t"
      "v_mfma_f32_16x16x32_bf16 v[0:3], a[12:15], a[100:103], v[0:3]\n\t"
      "v_mfma_f32_16x16x32_bf16 v[4:7], a[0:3], a[68:71], 0\n\t"
      "ds_read_b128 a[40:43], v222 offset:8192\n\t"
      "v_mfma_f32_16x16x32_bf16 v[4:7], a[4:7], a[80:83], v[4:7]\n\t"
      "buffer_load_dwordx4 v205, s[28:31], s60 offen offset:1024 nt lds\n\t"
      "v_mfma_f32_16x16x32_bf16 v[4:7], a[8:11], a[92:95], v[4:7]\n\t"
      "ds_read_b128 a[44:47], v223 offset:8192\n\t"
      "v_mfma_f32_16x16x32_bf16 v[4:7], a[12:15], a[104:107], v[4:7]\n\t"
      "v_mfma_f32_16x16x32_bf16 v[8:11], a[0:3], a[72:75], 0\n\t"
      "ds_read_b128 a[48:51], v220 offset:12288\n\t"
      "v_mfma_f32_16x16x32_bf16 v[8:11], a[4:7], a[84:87], v[8:11]\n\t"
      "buffer_load_dwordx4 v206, s[28:31], s60 offen offset:2048 nt lds\n\t"
      "v_mfma_f32_16x16x32_bf16 v[8:11], a[8:11], a[96:99], v[8:11]\n\t"
      "ds_read_b128 a[52:55], v221 offset:12288\n\t"
      "v_mfma_f32_16x16x32_bf16 v[8:11], a[12:15], a[108:111], v[8:11]\n\t"
      "v_mfma_f32_16x16x32_bf16 v[12:15], a[16:19], a[64:67], 0\n\t"
      "ds_read_b128 a[56:59], v222 offset:12288\n\t"
      "v_mfma_f32_16x16x32_bf16 v[12:15], a[20:23], a[76:79], v[12:15]\n\t"
      "buffer_load_dwordx4 v207, s[28:31], s60 offen offset:3072 nt lds\n\t"
      "v_mfma_f32_16x16x32_bf16 v[12:15], a[24:27], a[88:91], v[12:15]\n\t"
      "ds_read_b128 a[60:63], v223 offset:12288\n\t"
      "v_mfma_f32_16x16x32_bf16 v[12:15], a[28:31], a[100:103], v[12:15]\n\t"
      "s_mov_b32 m0, s91\n\t"
      "v_mfma_f32_16x16x32_bf16 v[16:19], a[16:19], a[68:71], 0\n\t"
      "buffer_load_dwordx4 v204, s[28:31], s61 offen nt lds\n\t"
      "v_mfma_f32_16x16x32_bf16 v[16:19], a[20:23], a[80:83], v[16:19]\n\t"
      "v_mfma_f32_16x16x32_bf16 v[16:19], a[24:27], a[92:95], v[16:19]\n\t"
      "buffer_load_dwordx4 v205, s[28:31], s61 offen offset:1024 nt lds\n\t"
      "v_mfma_f32_16x16x32_bf16 v[16:19], a[28:31], a[104:107], v[16:19]\n\t"
      "v_mfma_f32_16x16x32_bf16 v[20:23], a[16:19], a[72:75], 0\n\t"
      "buffer_load_dwordx4 v206, s[28:31], s61 offen offset:2048 nt lds\n\t"
      "v_mfma_f32_16x16x32_bf16 v[20:23], a[20:23], a[84:87], v[20:23]\n\t"
      "v_mfma_f32_16x16x32_bf16 v[20:23], a[24:27], a[96:99], v[20:23]\n\t"
      "buffer_load_dwordx4 v207, s[28:31], s61 offen offset:3072 nt lds\n\t"
      "v_mfma_f32_16x16x32_bf16 v[20:23], a[28:31], a[108:111], v[20:23]\n\t"
      "s_waitcnt lgkmcnt(0)\n\t"
      "s_waitcnt vmcnt(16)\n\t"
      "v_mfma_f32_16x16x32_bf16 v[24:27], a[32:35], a[64:67], 0\n\t"
      "ds_read_b128 a[0:3], v220 offset:16384\n\t"
      "s_mov_b32 m0, s92\n\t"
      "v_mfma_f32_16x16x32_bf16 v[24:27], a[36:39], a[76:79], v[24:27]\n\t"
      "buffer_load_dwordx4 v204, s[28:31], s62 offen nt lds\n\t"
      "v_mfma_f32_16x16x32_bf16 v[24:27], a[40:43], a[88:91], v[24:27]\n\t"
      "ds_read_b128 a[4:7], v221 offset:16384\n\t"
      "v_mfma_f32_16x16x32_bf16 v[24:27], a[44:47], a[100:103], v[24:27]\n\t"
      "v_mfma_f32_16x16x32_bf16 v[28:31], a[32:35], a[68:71], 0\n\t"
      "ds_read_b128 a[8:11], v222 offset:16384\n\t"
      "v_mfma_f32_16x16x32_bf16 v[28:31], a[36:39], a[80:83], v[28:31]\n\t"
      "buffer_load_dwordx4 v205, s[28:31], s62 offen offset:1024 nt lds\n\t"
      "v_mfma_f32_16x16x32_bf16 v[28:31], a[40:43], a[92:95], v[28:31]\n\t"
      "ds_read_b128 a[12:15], v223 offset:16384\n\t"
      "v_mfma_f32_16x16x32_bf16 v[28:31], a[44:47], a[104:107], v[28:31]\n\t"
      "v_mfma_f32_16x16x32_bf16 v[32:35], a[32:35], a[72:75], 0\n\t"
      "ds_read_b128 a[16:19], v220 offset:20480\n\t"
      "v_maximum3_f32 v236, v236, v0, v1\n\t"
      "v_mfma_f32_16x16x32_bf16 v[32:35], a[36:39], a[84:87], v[32:35]\n\t"
      "buffer_load_dwordx4 v206, s[28:31], s62 offen offset:2048 nt lds\n\t"
      "v_maximum3_f32 v237, v237, v4, v5\n\t"
      "v_mfma_f32_16x16x32_bf16 v[32:35], a[40:43], a[96:99], v[32:35]\n\t"
      "ds_read_b128 a[20:23], v221 offset:20480\n\t"
      "v_maximum3_f32 v238, v238, v8, v9\n\t"
      "v_mfma_f32_16x16x32_bf16 v[32:35], a[44:47], a[108:111], v[32:35]\n\t"
      "v_mfma_f32_16x16x32_bf16 v[36:39], a[48:51], a[64:67], 0\n\t"
      "ds_read_b128 a[24:27], v222 offset:20480\n\t"
      "v_maximum3_f32 v236, v236, v2, v3\n\t"
      "v_mfma_f32_16x16x32_bf16 v[36:39], a[52:55], a[76:79], v[36:39]\n\t"
      "buffer_load_dwordx4 v207, s[28:31], s62 offen offset:3072 nt lds\n\t"
      "v_maximum3_f32 v237, v237, v6, v7\n\t"
      "v_mfma_f32_16x16x32_bf16 v[36:39], a[56:59], a[88:91], v[36:39]\n\t"
      "ds_read_b128 a[28:31], v223 offset:20480\n\t"
      "v_maximum3_f32 v238, v238, v10, v11\n\t"
      "v_mfma_f32_16x16x32_bf16 v[36:39], a[60:63], a[100:103], v[36:39]\n\t"
      "s_mov_b32 m0, s93\n\t"
      "v_mfma_f32_16x16x32_bf16 v[40:43], a[48:51], a[68:71], 0\n\t"
      "buffer_load_dwordx4 v204, s[28:31], s63 offen nt lds\n\t"
      "v_maximum3_f32 v236, v236, v12, v13\n\t"
      "v_mfma_f32_16x16x32_bf16 v[40:43], a[52:55], a[80:83], v[40:43]\n\t"
      "v_maximum3_f32 v237, v237, v16, v17\n\t"
      "v_mfma_f32_16x16x32_bf16 v[40:43], a[56:59], a[92:95], v[40:43]\n\t"
      "buffer_load_dwordx4 v205, s[28:31], s63 offen offset:1024 nt lds\n\t"
      "v_maximum3_f32 v238, v238, v20, v21\n\t"
      "v_mfma_f32_16x16x32_bf16 v[40:43], a[60:63], a[104:107], v[40:43]\n\t"
      "v_mfma_f32_16x16x32_bf16 v[44:47], a[48:51], a[72:75], 0\n\t"
      "buffer_load_dwordx4 v206, s[28:31], s63 offen offset:2048 nt lds\n\t"
      "v_maximum3_f32 v236, v236, v14, v15\n\t"
      "v_mfma_f32_16x16x32_bf16 v[44:47], a[52:55], a[84:87], v[44:47]\n\t"
      "v_maximum3_f32 v237, v237, v18, v19\n\t"
      "v_mfma_f32_16x16x32_bf16 v[44:47], a[56:59], a[96:99], v[44:47]\n\t"
      "buffer_load_dwordx4 v207, s[28:31], s63 offen offset:3072 nt lds\n\t"
      "v_maximum3_f32 v238, v238, v22, v23\n\t"
      "v_mfma_f32_16x16x32_bf16 v[44:47], a[60:63], a[108:111], v[44:47]\n\t"
      "s_waitcnt lgkmcnt(0)\n\t"
      "s_waitcnt vmcnt(16)\n\t"
      "v_mfma_f32_16x16x32_bf16 v[48:51], a[0:3], a[64:67], 0\n\t"
      "ds_read_b128 a[32:35], v220 offset:24576\n\t"
      "s_mov_b32 m0, s94\n\t"
      "v_mfma_f32_16x16x32_bf16 v[48:51], a[4:7], a[76:79], v[48:51]\n\t"
      "buffer_load_dwordx4 v204, s[28:31], s64 offen nt lds\n\t"
      "v_mfma_f32_16x16x32_bf16 v[48:51], a[8:11], a[88:91], v[48:51]\n\t"
      "ds_read_b128 a[36:39], v221 offset:24576\n\t"
      "v_mfma_f32_16x16x32_bf16 v[48:51], a[12:15], a[100:103], v[48:51]\n\t"
      "v_mfma_f32_16x16x32_bf16 v[52:55], a[0:3], a[68:71], 0\n\t"
      "ds_read_b128 a[40:43], v222 offset:24576\n\t"
      "v_mfma_f32_16x16x32_bf16 v[52:55], a[4:7], a[80:83], v[52:55]\n\t"
      "buffer_load_dwordx4 v205, s[28:31], s64 offen offset:1024 nt lds\n\t"
      "v_mfma_f32_16x16x32_bf16 v[52:55], a[8:11], a[92:95], v[52:55]\n\t"
      "ds_read_b128 a[44:47], v223 offset:24576\n\t"
      "v_mfma_f32_16x16x32_bf16 v[52:55], a[12:15], a[104:107], v[52:55]\n\t"
      "v_mfma_f32_16x16x32_bf16 v[56:59], a[0:3], a[72:75], 0\n\t"
      "ds_read_b128 a[48:51], v220 offset:28672\n\t"
      "v_maximum3_f32 v236, v236, v24, v25\n\t"
      "v_mfma_f32_16x16x32_bf16 v[56:59], a[4:7], a[84:87], v[56:59]\n\t"
      "buffer_load_dwordx4 v206, s[28:31], s64 offen offset:2048 nt lds\n\t"
      "v_maximum3_f32 v237, v237, v28, v29\n\t"
      "v_mfma_f32_16x16x32_bf16 v[56:59], a[8:11], a[96:99], v[56:59]\n\t"
      "ds_read_b128 a[52:55], v221 offset:28672\n\t"
      "v_maximum3_f32 v238, v238, v32, v33\n\t"
      "v_mfma_f32_16x16x32_bf16 v[56:59], a[12:15], a[108:111], v[56:59]\n\t"
      "v_mfma_f32_16x16x32_bf16 v[60:63], a[16:19], a[64:67], 0\n\t"
      "ds_read_b128 a[56:59], v222 offset:28672\n\t"
      "v_maximum3_f32 v236, v236, v26, v27\n\t"
      "v_mfma_f32_16x16x32_bf16 v[60:63], a[20:23], a[76:79], v[60:63]\n\t"
      "buffer_load_dwordx4 v207, s[28:31], s64 offen offset:3072 nt lds\n\t"
      "v_maximum3_f32 v237, v237, v30, v31\n\t"
      "v_mfma_f32_16x16x32_bf16 v[60:63], a[24:27], a[88:91], v[60:63]\n\t"
      "ds_read_b128 a[60:63], v223 offset:28672\n\t"
      "v_maximum3_f32 v238, v238, v34, v35\n\t"
      "v_mfma_f32_16x16x32_bf16 v[60:63], a[28:31], a[100:103], v[60:63]\n\t"
      "s_mov_b32 m0, s95\n\t"
      "v_mfma_f32_16x16x32_bf16 v[64:67], a[16:19], a[68:71], 0\n\t"
      "buffer_load_dwordx4 v204, s[28:31], s65 offen nt lds\n\t"
      "v_maximum3_f32 v236, v236, v36, v37\n\t"
      "v_mfma_f32_16x16x32_bf16 v[64:67], a[20:23], a[80:83], v[64:67]\n\t"
      "v_maximum3_f32 v237, v237, v40, v41\n\t"
      "v_mfma_f32_16x16x32_bf16 v[64:67], a[24:27], a[92:95], v[64:67]\n\t"
      "buffer_load_dwordx4 v205, s[28:31], s65 offen offset:1024 nt lds\n\t"
      "v_maximum3_f32 v238, v238, v44, v45\n\t"
      "v_mfma_f32_16x16x32_bf16 v[64:67], a[28:31], a[104:107], v[64:67]\n\t"
      "v_mfma_f32_16x16x32_bf16 v[68:71], a[16:19], a[72:75], 0\n\t"
      "buffer_load_dwordx4 v206, s[28:31], s65 offen offset:2048 nt lds\n\t"
      "v_maximum3_f32 v236, v236, v38, v39\n\t"
      "v_mfma_f32_16x16x32_bf16 v[68:71], a[20:23], a[84:87], v[68:71]\n\t"
      "v_maximum3_f32 v237, v237, v42, v43\n\t"
      "v_mfma_f32_16x16x32_bf16 v[68:71], a[24:27], a[96:99], v[68:71]\n\t"
      "buffer_load_dwordx4 v207, s[28:31], s65 offen offset:3072 nt lds\n\t"
      "v_maximum3_f32 v238, v238, v46, v47\n\t"
      "v_mfma_f32_16x16x32_bf16 v[68:71], a[28:31], a[108:111], v[68:71]\n\t"
      "s_waitcnt lgkmcnt(0)\n\t"
      "s_waitcnt vmcnt(16)\n\t"
      "v_mfma_f32_16x16x32_bf16 v[72:75], a[32:35], a[64:67], 0\n\t"
      "ds_read_b128 a[0:3], v220\n\t"
      "s_mov_b32 m0, s96\n\t"
      "v_mfma_f32_16x16x32_bf16 v[72:75], a[36:39], a[76:79], v[72:75]\n\t"
      "buffer_load_dwordx4 v204, s[28:31], s66 offen nt lds\n\t"
      "v_mfma_f32_16x16x32_bf16 v[72:75], a[40:43], a[88:91], v[72:75]\n\t"
      "ds_read_b128 a[4:7], v221\n\t"
      "v_mfma_f32_16x16x32_bf16 v[72:75], a[44:47], a[100:103], v[72:75]\n\t"
      "v_mfma_f32_16x16x32_bf16 v[76:79], a[32:35], a[68:71], 0\n\t"
      "ds_read_b128 a[8:11], v222\n\t"
      "v_mfma_f32_16x16x32_bf16 v[76:79], a[36:39], a[80:83], v[76:79]\n\t"
      "buffer_load_dwordx4 v205, s[28:31], s66 offen offset:1024 nt lds\n\t"
      "v_mfma_f32_16x16x32_bf16 v[76:79], a[40:43], a[92:95], v[76:79]\n\t"
      "ds_read_b128 a[12:15], v223\n\t"
      "v_mfma_f32_16x16x32_bf16 v[76:79], a[44:47], a[104:107], v[76:79]\n\t"
      "v_mfma_f32_16x16x32_bf16 v[80:83], a[32:35], a[72:75], 0\n\t"
      "ds_read_b128 a[16:19], v220 offset:4096\n\t"
      "v_maximum3_f32 v236, v236, v48, v49\n\t"
      "v_mfma_f32_16x16x32_bf16 v[80:83], a[36:39], a[84:87], v[80:83]\n\t"
      "buffer_load_dwordx4 v206, s[28:31], s66 offen offset:2048 nt lds\n\t"
      "v_maximum3_f32 v237, v237, v52, v53\n\t"
      "v_mfma_f32_16x16x32_bf16 v[80:83], a[40:43], a[96:99], v[80:83]\n\t"
      "ds_read_b128 a[20:23], v221 offset:4096\n\t"
      "v_maximum3_f32 v238, v238, v56, v57\n\t"
      "v_mfma_f32_16x16x32_bf16 v[80:83], a[44:47], a[108:111], v[80:83]\n\t"
      "v_mfma_f32_16x16x32_bf16 v[84:87], a[48:51], a[64:67], 0\n\t"
      "ds_read_b128 a[24:27], v222 offset:4096\n\t"
      "v_maximum3_f32 v236, v236, v50, v51\n\t"
      "v_mfma_f32_16x16x32_bf16 v[84:87], a[52:55], a[76:79], v[84:87]\n\t"
      "buffer_load_dwordx4 v207, s[28:31], s66 offen offset:3072 nt lds\n\t"
      "v_maximum3_f32 v237, v237, v54, v55\n\t"
      "v_mfma_f32_16x16x32_bf16 v[84:87], a[56:59], a[88:91], v[84:87]\n\t"
      "ds_read_b128 a[28:31], v223 offset:4096\n\t"
      "v_maximum3_f32 v238, v238, v58, v59\n\t"
      "v_mfma_f32_16x16x32_bf16 v[84:87], a[60:63], a[100:103], v[84:87]\n\t"
      "s_mov_b32 m0, s97\n\t"
      "v_mfma_f32_16x16x32_bf16 v[88:91], a[48:51], a[68:71], 0\n\t"
      "buffer_load_dwordx4 v204, s[28:31], s67 offen nt lds\n\t"
      "v_maximum3_f32 v236, v236, v60, v61\n\t"
      "v_mfma_f32_16x16x32_bf16 v[88:91], a[52:55], a[80:83], v[88:91]\n\t"
      "v_maximum3_f32 v237, v237, v64, v65\n\t"
      "v_mfma_f32_16x16x32_bf16 v[88:91], a[56:59], a[92:95], v[88:91]\n\t"
      "buffer_load_dwordx4 v205, s[28:31], s67 offen offset:1024 nt lds\n\t"
      "v_maximum3_f32 v238, v238, v68, v69\n\t"
      "v_mfma_f32_16x16x32_bf16 v[88:91], a[60:63], a[104:107], v[88:91]\n\t"
      "v_mfma_f32_16x16x32_bf16 v[92:95], a[48:51], a[72:75], 0\n\t"
      "buffer_load_dwordx4 v206, s[28:31], s67 offen offset:2048 nt lds\n\t"
      "v_maximum3_f32 v236, v236, v62, v63\n\t"
      "v_mfma_f32_16x16x32_bf16 v[92:95], a[52:55], a[84:87], v[92:95]\n\t"
      "v_maximum3_f32 v237, v237, v66, v67\n\t"
      "v_mfma_f32_16x16x32_bf16 v[92:95], a[56:59], a[96:99], v[92:95]\n\t"
      "buffer_load_dwordx4 v207, s[28:31], s67 offen offset:3072 nt lds\n\t"
      "v_maximum3_f32 v238, v238, v70, v71\n\t"
      "v_mfma_f32_16x16x32_bf16 v[92:95], a[60:63], a[108:111], v[92:95]\n\t"
      "s_waitcnt lgkmcnt(0)\n\t"
      "s_waitcnt vmcnt(16)\n\t"
      "v_mfma_f32_16x16x32_bf16 v[96:99], a[0:3], a[64:67], 0\n\t"
      "ds_read_b128 a[32:35], v220 offset:8192\n\t"
      "s_mov_b32 m0, s90\n\t"
      "v_mfma_f32_16x16x32_bf16 v[96:99], a[4:7], a[76:79], v[96:99]\n\t"
      "buffer_load_dwordx4 v204, s[32:35], s52 offen nt lds\n\t"
      "v_mfma_f32_16x16x32_bf16 v[96:99], a[8:11], a[88:91], v[96:99]\n\t"
      "ds_read_b128 a[36:39], v221 offset:8192\n\t"
      "v_mfma_f32_16x16x32_bf16 v[96:99], a[12:15], a[100:103], v[96:99]\n\t"
      "s_add_u32 s28, s28, 0x40000\n\t"
      "s_addc_u32 s29, s29, 0\n\t"
      "s_sub_u32 s30, s30, 0x40000\n\t"
      "v_mfma_f32_16x16x32_bf16 v[100:103], a[0:3], a[68:71], 0\n\t"
      "ds_read_b128 a[40:43], v222 offset:8192\n\t"
      "v_mfma_f32_16x16x32_bf16 v[100:103], a[4:7], a[80:83], v[100:103]\n\t"
      "buffer_load_dwordx4 v205, s[32:35], s52 offen offset:1024 nt lds\n\t"
      "v_mfma_f32_16x16x32_bf16 v[100:103], a[8:11], a[92:95], v[100:103]\n\t"
      "ds_read_b128 a[44:47], v223 offset:8192\n\t"
      "v_mfma_f32_16x16x32_bf16 v[100:103], a[12:15], a[104:107], v[100:103]\n\t"
      "v_mfma_f32_16x16x32_bf16 v[104:107], a[0:3], a[72:75], 0\n\t"
      "ds_read_b128 a[48:51], v220 offset:12288\n\t"
      "v_maximum3_f32 v236, v236, v72, v73\n\t"
      "v_mfma_f32_16x16x32_bf16 v[104:107], a[4:7], a[84:87], v[104:107]\n\t"
      "buffer_load_dwordx4 v206, s[32:35], s52 offen offset:2048 nt lds\n\t"
      "v_maximum3_f32 v237, v237, v76, v77\n\t"
      "v_mfma_f32_16x16x32_bf16 v[104:107], a[8:11], a[96:99], v[104:107]\n\t"
      "ds_read_b128 a[52:55], v221 offset:12288\n\t"
      "v_maximum3_f32 v238, v238, v80, v81\n\t"
      "v_mfma_f32_16x16x32_bf16 v[104:107], a[12:15], a[108:111], v[104:107]\n\t"
      "v_mfma_f32_16x16x32_bf16 v[108:111], a[16:19], a[64:67], 0\n\t"
      "ds_read_b128 a[56:59], v222 offset:12288\n\t"
      "v_maximum3_f32 v236, v236, v74, v75\n\t"
      "v_mfma_f32_16x16x32_bf16 v[108:111], a[20:23], a[76:79], v[108:111]\n\t"
      "buffer_load_dwordx4 v207, s[32:35], s52 offen offset:3072 nt lds\n\t"
      "v_maximum3_f32 v237, v237, v78, v79\n\t"
      "v_mfma_f32_16x16x32_bf16 v[108:111], a[24:27], a[88:91], v[108:111]\n\t"
      "ds_read_b128 a[60:63], v223 offset:12288\n\t"
      "v_maximum3_f32 v238, v238, v82, v83\n\t"
      "v_mfma_f32_16x16x32_bf16 v[108:111], a[28:31], a[100:103], v[108:111]\n\t"
      "s_mov_b32 m0, s91\n\t"
      "v_mfma_f32_16x16x32_bf16 v[112:115], a[16:19], a[68:71], 0\n\t"
      "buffer_load_dwordx4 v204, s[32:35], s53 offen nt lds\n\t"
      "v_maximum3_f32 v236, v236, v84, v85\n\t"
      "v_mfma_f32_16x16x32_bf16 v[112:115], a[20:23], a[80:83], v[112:115]\n\t"
      "v_maximum3_f32 v237, v237, v88, v89\n\t"
      "v_mfma_f32_16x16x32_bf16 v[112:115], a[24:27], a[92:95], v[112:115]\n\t"
      "buffer_load_dwordx4 v205, s[32:35], s53 offen offset:1024 nt lds\n\t"
      "v_maximum3_f32 v238, v238, v92, v93\n\t"
      "v_mfma_f32_16x16x32_bf16 v[112:115], a[28:31], a[104:107], v[112:115]\n\t"
      "v_mfma_f32_16x16x32_bf16 v[116:119], a[16:19], a[72:75], 0\n\t"
      "buffer_load_dwordx4 v206, s[32:35], s53 offen offset:2048 nt lds\n\t"
      "v_maximum3_f32 v236, v236, v86, v87\n\t"
      "v_mfma_f32_16x16x32_bf16 v[116:119], a[20:23], a[84:87], v[116:119]\n\t"
      "v_maximum3_f32 v237, v237, v90, v91\n\t"
      "v_mfma_f32_16x16x32_bf16 v[116:119], a[24:27], a[96:99], v[116:119]\n\t"
      "buffer_load_dwordx4 v207, s[32:35], s53 offen offset:3072 nt lds\n\t"
      "v_maximum3_f32 v238, v238, v94, v95\n\t"
      "v_mfma_f32_16x16x32_bf16 v[116:119], a[28:31], a[108:111], v[116:119]\n\t"
      "s_waitcnt lgkmcnt(0)\n\t"
      "s_waitcnt vmcnt(16)\n\t"
      "v_mfma_f32_16x16x32_bf16 v[120:123], a[32:35], a[64:67], 0\n\t"
      "ds_read_b128 a[0:3], v220 offset:16384\n\t"
      "s_mov_b32 m0, s92\n\t"
      "v_mfma_f32_16x16x32_bf16 v[120:123], a[36:39], a[76:79], v[120:123]\n\t"
      "buffer_load_dwordx4 v204, s[32:35], s54 offen nt lds\n\t"
      "v_mfma_f32_16x16x32_bf16 v[120:123], a[40:43], a[88:91], v[120:123]\n\t"
      "ds_read_b128 a[4:7], v221 offset:16384\n\t"
      "v_mfma_f32_16x16x32_bf16 v[120:123], a[44:47], a[100:103], v[120:123]\n\t"
      "v_mfma_f32_16x16x32_bf16 v[124:127], a[32:35], a[68:71], 0\n\t"
      "ds_read_b128 a[8:11], v222 offset:16384\n\t"
      "v_mfma_f32_16x16x32_bf16 v[124:127], a[36:39], a[80:83], v[124:127]\n\t"
      "buffer_load_dwordx4 v205, s[32:35], s54 offen offset:1024 nt lds\n\t"
      "v_mfma_f32_16x16x32_bf16 v[124:127], a[40:43], a[92:95], v[124:127]\n\t"
      "ds_read_b128 a[12:15], v223 offset:16384\n\t"
      "v_mfma_f32_16x16x32_bf16 v[124:127], a[44:47], a[104:107], v[124:127]\n\t"
      "v_mfma_f32_16x16x32_bf16 v[128:131], a[32:35], a[72:75], 0\n\t"
      "ds_read_b128 a[16:19], v220 offset:20480\n\t"
      "v_maximum3_f32 v236, v236, v96, v97\n\t"
      "v_mfma_f32_16x16x32_bf16 v[128:131], a[36:39], a[84:87], v[128:131]\n\t"
      "buffer_load_dwordx4 v206, s[32:35], s54 offen offset:2048 nt lds\n\t"
      "v_maximum3_f32 v237, v237, v100, v101\n\t"
      "v_mfma_f32_16x16x32_bf16 v[128:131], a[40:43], a[96:99], v[128:131]\n\t"
      "ds_read_b128 a[20:23], v221 offset:20480\n\t"
      "v_maximum3_f32 v238, v238, v104, v105\n\t"
      "v_mfma_f32_16x16x32_bf16 v[128:131], a[44:47], a[108:111], v[128:131]\n\t"
      "v_mfma_f32_16x16x32_bf16 v[132:135], a[48:51], a[64:67], 0\n\t"
      "ds_read_b128 a[24:27], v222 offset:20480\n\t"
      "v_maximum3_f32 v236, v236, v98, v99\n\t"
      "v_mfma_f32_16x16x32_bf16 v[132:135], a[52:55], a[76:79], v[132:135]\n\t"
      "buffer_load_dwordx4 v207, s[32:35], s54 offen offset:3072 nt lds\n\t"
      "v_maximum3_f32 v237, v237, v102, v103\n\t"
      "v_mfma_f32_16x16x32_bf16 v[132:135], a[56:59], a[88:91], v[132:135]\n\t"
      "ds_read_b128 a[28:31], v223 offset:20480\n\t"
      "v_maximum3_f32 v238, v238, v106, v107\n\t"
      "v_mfma_f32_16x16x32_bf16 v[132:135], a[60:63], a[100:103], v[132:135]\n\t"
      "s_mov_b32 m0, s93\n\t"
      "v_mfma_f32_16x16x32_bf16 v[136:139], a[48:51], a[68:71], 0\n\t"
      "buffer_load_dwordx4 v204, s[32:35], s55 offen nt lds\n\t"
      "v_maximum3_f32 v236, v236, v108, v109\n\t"
      "v_mfma_f32_16x16x32_bf16 v[136:139], a[52:55], a[80:83], v[136:139]\n\t"
      "v_maximum3_f32 v237, v237, v112, v113\n\t"
      "v_mfma_f32_16x16x32_bf16 v[136:139], a[56:59], a[92:95], v[136:139]\n\t"
      "buffer_load_dwordx4 v205, s[32:35], s55 offen offset:1024 nt lds\n\t"
      "v_maximum3_f32 v238, v238, v116, v117\n\t"
      "v_mfma_f32_16x16x32_bf16 v[136:139], a[60:63], a[104:107], v[136:139]\n\t"
      "v_mfma_f32_16x16x32_bf16 v[140:143], a[48:51], a[72:75], 0\n\t"
      "buffer_load_dwordx4 v206, s[32:35], s55 offen offset:2048 nt lds\n\t"
      "v_maximum3_f32 v236, v236, v110, v111\n\t"
      "v_mfma_f32_16x16x32_bf16 v[140:143], a[52:55], a[84:87], v[140:143]\n\t"
      "v_maximum3_f32 v237, v237, v114, v115\n\t"
      "v_mfma_f32_16x16x32_bf16 v[140:143], a[56:59], a[96:99], v[140:143]\n\t"
      "buffer_load_dwordx4 v207, s[32:35], s55 offen offset:3072 nt lds\n\t"
      "v_maximum3_f32 v238, v238, v118, v119\n\t"
      "v_mfma_f32_16x16x32_bf16 v[140:143], a[60:63], a[108:111], v[140:143]\n\t"
      "s_waitcnt lgkmcnt(0)\n\t"
      "s_waitcnt vmcnt(16)\n\t"
      "v_mfma_f32_16x16x32_bf16 v[144:147], a[0:3], a[64:67], 0\n\t"
      "ds_read_b128 a[32:35], v220 offset:24576\n\t"
      "s_mov_b32 m0, s94\n\t"
      "v_mfma_f32_16x16x32_bf16 v[144:147], a[4:7], a[76:79], v[144:147]\n\t"
      "buffer_load_dwordx4 v204, s[32:35], s56 offen nt lds\n\t"
      "v_mfma_f32_16x16x32_bf16 v[144:147], a[8:11], a[88:91], v[144:147]\n\t"
      "ds_read_b128 a[36:39], v221 offset:24576\n\t"
      "v_mfma_f32_16x16x32_bf16 v[144:147], a[12:15], a[100:103], v[144:147]\n\t"
      "v_mfma_f32_16x16x32_bf16 v[148:151], a[0:3], a[68:71], 0\n\t"
      "ds_read_b128 a[40:43], v222 offset:24576\n\t"
      "v_mfma_f32_16x16x32_bf16 v[148:151], a[4:7], a[80:83], v[148:151]\n\t"
      "buffer_load_dwordx4 v205, s[32:35], s56 offen offset:1024 nt lds\n\t"
      "v_mfma_f32_16x16x32_bf16 v[148:151], a[8:11], a[92:95], v[148:151]\n\t"
      "ds_read_b128 a[44:47], v223 offset:24576\n\t"
      "v_mfma_f32_16x16x32_bf16 v[148:151], a[12:15], a[104:107], v[148:151]\n\t"
      "v_mfma_f32_16x16x32_bf16 v[152:155], a[0:3], a[72:75], 0\n\t"
      "ds_read_b128 a[48:51], v220 offset:28672\n\t"
      "v_maximum3_f32 v236, v236, v120, v121\n\t"
      "v_mfma_f32_16x16x32_bf16 v[152:155], a[4:7], a[84:87], v[152:155]\n\t"
      "buffer_load_dwordx4 v206, s[32:35], s56 offen offset:2048 nt lds\n\t"
      "v_maximum3_f32 v237, v237, v124, v125\n\t"
      "v_mfma_f32_16x16x32_bf16 v[152:155], a[8:11], a[96:99], v[152:155]\n\t"
      "ds_read_b128 a[52:55], v221 offset:28672\n\t"
      "v_maximum3_f32 v238, v238, v128, v129\n\t"
      "v_mfma_f32_16x16x32_bf16 v[152:155], a[12:15], a[108:111], v[152:155]\n\t"
      "v_mfma_f32_16x16x32_bf16 v[156:159], a[16:19], a[64:67], 0\n\t"
      "ds_read_b128 a[56:59], v222 offset:28672\n\t"
      "v_maximum3_f32 v236, v236, v122, v123\n\t"
      "v_mfma_f32_16x16x32_bf16 v[156:159], a[20:23], a[76:79], v[156:159]\n\t"
      "buffer_load_dwordx4 v207, s[32:35], s56 offen offset:3072 nt lds\n\t"
      "v_maximum3_f32 v237, v237, v126, v127\n\t"
      "v_mfma_f32_16x16x32_bf16 v[156:159], a[24:27], a[88:91], v[156:159]\n\t"
      "ds_read_b128 a[60:63], v223 offset:28672\n\t"
      "v_maximum3_f32 v238, v238, v130, v131\n\t"
      "v_mfma_f32_16x16x32_bf16 v[156:159], a[28:31], a[100:103], v[156:159]\n\t"
      "s_mov_b32 m0, s95\n\t"
      "v_mfma_f32_16x16x32_bf16 v[160:163], a[16:19], a[68:71], 0\n\t"
      "buffer_load_dwordx4 v204, s[32:35], s57 offen nt lds\n\t"
      "v_maximum3_f32 v236, v236, v132, v133\n\t"
      "v_mfma_f32_16x16x32_bf16 v[160:163], a[20:23], a[80:83], v[160:163]\n\t"
      "v_maximum3_f32 v237, v237, v136, v137\n\t"
      "v_mfma_f32_16x16x32_bf16 v[160:163], a[24:27], a[92:95], v[160:163]\n\t"
      "buffer_load_dwordx4 v205, s[32:35], s57 offen offset:1024 nt lds\n\t"
      "v_maximum3_f32 v238, v238, v140, v141\n\t"
      "v_mfma_f32_16x16x32_bf16 v[160:163], a[28:31], a[104:107], v[160:163]\n\t"
      "v_mfma_f32_16x16x32_bf16 v[164:167], a[16:19], a[72:75], 0\n\t"
      "buffer_load_dwordx4 v206, s[32:35], s57 offen offset:2048 nt lds\n\t"
      "v_maximum3_f32 v236, v236, v134, v135\n\t"
      "v_mfma_f32_16x16x32_bf16 v[164:167], a[20:23], a[84:87], v[164:167]\n\t"
      "v_maximum3_f32 v237, v237, v138, v139\n\t"
      "v_mfma_f32_16x16x32_bf16 v[164:167], a[24:27], a[96:99], v[164:167]\n\t"
      "buffer_load_dwordx4 v207, s[32:35], s57 offen offset:3072 nt lds\n\t"
      "v_maximum3_f32 v238, v238, v142, v143\n\t"
      "v_mfma_f32_16x16x32_bf16 v[164:167], a[28:31], a[108:111], v[164:167]\n\t"
      "s_waitcnt lgkmcnt(0)\n\t"
      "s_waitcnt vmcnt(16)\n\t"
      "v_mfma_f32_16x16x32_bf16 v[168:171], a[32:35], a[64:67], 0\n\t"
      "ds_read_b64_tr_b16 a[0:1], v228\n\t"
      "v_mfma_f32_16x16x32_bf16 v[168:171], a[36:39], a[76:79], v[168:171]\n\t"
      "ds_read_b64_tr_b16 a[2:3], v228 offset:4096\n\t"
      "v_mfma_f32_16x16x32_bf16 v[168:171], a[40:43], a[88:91], v[168:171]\n\t"
      "ds_read_b64_tr_b16 a[4:5], v229\n\t"
      "v_mfma_f32_16x16x32_bf16 v[168:171], a[44:47], a[100:103], v[168:171]\n\t"
      "ds_read_b64_tr_b16 a[6:7], v229 offset:4096\n\t"
      "s_mov_b32 m0, s96\n\t"
      "v_mfma_f32_16x16x32_bf16 v[172:175], a[32:35], a[68:71], 0\n\t"
      "buffer_load_dwordx4 v204, s[32:35], s58 offen nt lds\n\t"
      "v_mfma_f32_16x16x32_bf16 v[172:175], a[36:39], a[80:83], v[172:175]\n\t"
      "ds_read_b64_tr_b16 a[8:9], v230\n\t"
      "v_mfma_f32_16x16x32_bf16 v[172:175], a[40:43], a[92:95], v[172:175]\n\t"
      "ds_read_b64_tr_b16 a[10:11], v230 offset:4096\n\t"
      "v_mfma_f32_16x16x32_bf16 v[172:175], a[44:47], a[104:107], v[172:175]\n\t"
      "ds_read_b64_tr_b16 a[12:13], v231\n\t"
      "v_mfma_f32_16x16x32_bf16 v[176:179], a[32:35], a[72:75], 0\n\t"
      "buffer_load_dwordx4 v205, s[32:35], s58 offen offset:1024 nt lds\n\t"
      "v_maximum3_f32 v236, v236, v144, v145\n\t"
      "v_mfma_f32_16x16x32_bf16 v[176:179], a[36:39], a[84:87], v[176:179]\n\t"
      "ds_read_b64_tr_b16 a[14:15], v231 offset:4096\n\t"
      "v_maximum3_f32 v237, v237, v148, v149\n\t"
      "v_mfma_f32_16x16x32_bf16 v[176:179], a[40:43], a[96:99], v[176:179]\n\t"
      "ds_read_b64_tr_b16 a[16:17], v232\n\t"
      "v_maximum3_f32 v238, v238, v152, v153\n\t"
      "v_mfma_f32_16x16x32_bf16 v[176:179], a[44:47], a[108:111], v[176:179]\n\t"
      "ds_read_b64_tr_b16 a[18:19], v232 offset:4096\n\t"
      "v_mfma_f32_16x16x32_bf16 v[180:183], a[48:51], a[64:67], 0\n\t"
      "buffer_load_dwordx4 v206, s[32:35], s58 offen offset:2048 nt lds\n\t"
      "v_maximum3_f32 v236, v236, v146, v147\n\t"
      "v_mfma_f32_16x16x32_bf16 v[180:183], a[52:55], a[76:79], v[180:183]\n\t"
      "ds_read_b64_tr_b16 a[20:21], v233\n\t"
      "v_maximum3_f32 v237, v237, v150, v151\n\t"
      "v_mfma_f32_16x16x32_bf16 v[180:183], a[56:59], a[88:91], v[180:183]\n\t"
      "ds_read_b64_tr_b16 a[22:23], v233 offset:4096\n\t"
      "v_maximum3_f32 v238, v238, v154, v155\n\t"
      "v_mfma_f32_16x16x32_bf16 v[180:183], a[60:63], a[100:103], v[180:183]\n\t"
      "ds_read_b64_tr_b16 a[24:25], v234\n\t"
      "v_mfma_f32_16x16x32_bf16 v[184:187], a[48:51], a[68:71], 0\n\t"
      "buffer_load_dwordx4 v207, s[32:35], s58 offen offset:3072 nt lds\n\t"
      "v_maximum3_f32 v236, v236, v156, v157\n\t"
      "v_mfma_f32_16x16x32_bf16 v[184:187], a[52:55], a[80:83], v[184:187]\n\t"
      "ds_read_b64_tr_b16 a[26:27], v234 offset:4096\n\t"
      "v_maximum3_f32 v237, v237, v160, v161\n\t"
      "v_mfma_f32_16x16x32_bf16 v[184:187], a[56:59], a[92:95], v[184:187]\n\t"
      "ds_read_b64_tr_b16 a[28:29], v235\n\t"
      "v_maximum3_f32 v238, v238, v164, v165\n\t"
      "v_mfma_f32_16x16x32_bf16 v[184:187], a[60:63], a[104:107], v[184:187]\n\t"
      "ds_read_b64_tr_b16 a[30:31], v235 offset:4096\n\t"
      "s_mov_b32 m0, s97\n\t"
      "v_mfma_f32_16x16x32_bf16 v[188:191], a[48:51], a[72:75], 0\n\t"
      "buffer_load_dwordx4 v204, s[32:35], s59 offen nt lds\n\t"
      "v_maximum3_f32 v236, v236, v158, v159\n\t"
      "v_mfma_f32_16x16x32_bf16 v[188:191], a[52:55], a[84:87], v[188:191]\n\t"
      "buffer_load_dwordx4 v205, s[32:35], s59 offen offset:1024 nt lds\n\t"
      "v_maximum3_f32 v237, v237, v162, v163\n\t"
      "v_mfma_f32_16x16x32_bf16 v[188:191], a[56:59], a[96:99], v[188:191]\n\t"
      "buffer_load_dwordx4 v206, s[32:35], s59 offen offset:2048 nt lds\n\t"
      "v_maximum3_f32 v238, v238, v166, v167\n\t"
      "v_mfma_f32_16x16x32_bf16 v[188:191], a[60:63], a[108:111], v[188:191]\n\t"
      "buffer_load_dwordx4 v207, s[32:35], s59 offen offset:3072 nt lds\n\t"
      "s_waitcnt lgkmcnt(0)\n\t"
      "s_waitcnt vmcnt(16)\n\t"
      "s_barrier\n\t"
      "ds_read_b64_tr_b16 a[32:33], v228 offset:8192\n\t"
      "ds_read_b64_tr_b16 a[34:35], v228 offset:12288\n\t"
      "ds_read_b64_tr_b16 a[36:37], v229 offset:8192\n\t"
      "ds_read_b64_tr_b16 a[38:39], v229 offset:12288\n\t"
      "ds_read_b64_tr_b16 a[40:41], v230 offset:8192\n\t"
      "v_maximum3_f32 v236, v236, v168, v169\n\t"
      "v_maximum3_f32 v237, v237, v172, v173\n\t"
      "v_maximum3_f32 v238, v238, v176, v177\n\t"
      "v_maximum3_f32 v236, v236, v170, v171\n\t"
      "v_maximum3_f32 v237, v237, v174, v175\n\t"
      "v_maximum3_f32 v238, v238, v178, v179\n\t"
      "v_maximum3_f32 v236, v236, v180, v181\n\t"
      "v_maximum3_f32 v237, v237, v184, v185\n\t"
      "v_maximum3_f32 v238, v238, v188, v189\n\t"
      "v_maximum3_f32 v236, v236, v182, v183\n\t"
      "v_maximum3_f32 v237, v237, v186, v187\n\t"
      "v_maximum3_f32 v238, v238, v190, v191\n\t"
      "v_mov_b32 v246, v236\n\t"
      "v_mov_b32 v247, v237\n\t"
      "v_mov_b32 v248, v238\n\t"
      "v_permlane16_swap_b32 v246, v236\n\t"
      "v_permlane16_swap_b32 v247, v237\n\t"
      "v_permlane16_swap_b32 v248, v238\n\t"
      "v_maximum3_f32 v236, v236, v246, v246\n\t"
      "v_maximum3_f32 v237, v237, v247, v247\n\t"
      "v_maximum3_f32 v238, v238, v248, v248\n\t"
      "v_mov_b32 v246, v236\n\t"
      "v_mov_b32 v247, v237\n\t"
      "v_mov_b32 v248, v238\n\t"
      "v_permlane32_swap_b32 v246, v236\n\t"
      "v_permlane32_swap_b32 v247, v237\n\t"
      "v_permlane32_swap_b32 v248, v238\n\t"
      "v_maximum3_f32 v236, v236, v246, v246\n\t"
      "v_maximum3_f32 v237, v237, v247, v247\n\t"
      "v_maximum3_f32 v238, v238, v248, v248\n\t"
      "v_mul_f32 v240, 0xbe0293ee, v236\n\t"
      "v_mul_f32 v242, 0xbe0293ee, v237\n\t"
      "v_mul_f32 v244, 0xbe0293ee, v238\n\t"
      "v_pk_fma_f32 v[0:1], v[0:1], s[26:27], v[240:241] op_sel_hi:[1,0,0]\n\t"
      "v_pk_fma_f32 v[2:3], v[2:3], s[26:27], v[240:241] op_sel_hi:[1,0,0]\n\t"
      "v_pk_fma_f32 v[12:13], v[12:13], s[26:27], v[240:241] op_sel_hi:[1,0,0]\n\t"
      "v_pk_fma_f32 v[14:15], v[14:15], s[26:27], v[240:241] op_sel_hi:[1,0,0]\n\t"
      "v_exp_f32 v0, v0\n\t"
      "v_exp_f32 v1, v1\n\t"
      "v_exp_f32 v2, v2\n\t"
      "v_exp_f32 v3, v3\n\t"
      "v_exp_f32 v12, v12\n\t"
      "v_exp_f32 v13, v13\n\t"
      "v_exp_f32 v14, v14\n\t"
      "v_exp_f32 v15, v15\n\t"
      "v_pk_fma_f32 v[4:5], v[4:5], s[26:27], v[242:243] op_sel_hi:[1,0,0]\n\t"
      "v_pk_fma_f32 v[6:7], v[6:7], s[26:27], v[242:243] op_sel_hi:[1,0,0]\n\t"
      "v_pk_fma_f32 v[16:17], v[16:17], s[26:27], v[242:243] op_sel_hi:[1,0,0]\n\t"
      "v_pk_fma_f32 v[18:19], v[18:19], s[26:27], v[242:243] op_sel_hi:[1,0,0]\n\t"
      "ds_read_b64_tr_b16 a[42:43], v230 offset:12288\n\t"
      "ds_read_b64_tr_b16 a[44:45], v231 offset:8192\n\t"
      "ds_read_b64_tr_b16 a[46:47], v231 offset:12288\n\t"
      "ds_read_b64_tr_b16 a[48:49], v232 offset:8192\n\t"
      "v_cvt_pk_bf16_f32 v0, v0, v1\n\t"
      "v_cvt_pk_bf16_f32 v1, v2, v3\n\t"
      "v_cvt_pk_bf16_f32 v2, v12, v13\n\t"
      "v_cvt_pk_bf16_f32 v3, v14, v15\n\t"
      "v_exp_f32 v4, v4\n\t"
      "v_exp_f32 v5, v5\n\t"
      "v_exp_f32 v6, v6\n\t"
      "v_exp_f32 v7, v7\n\t"
      "v_exp_f32 v16, v16\n\t"
      "v_exp_f32 v17, v17\n\t"
      "v_exp_f32 v18, v18\n\t"
      "v_exp_f32 v19, v19\n\t"
      "v_pk_fma_f32 v[8:9], v[8:9], s[26:27], v[244:245] op_sel_hi:[1,0,0]\n\t"
      "v_pk_fma_f32 v[10:11], v[10:11], s[26:27], v[244:245] op_sel_hi:[1,0,0]\n\t"
      "v_pk_fma_f32 v[20:21], v[20:21], s[26:27], v[244:245] op_sel_hi:[1,0,0]\n\t"
      "v_pk_fma_f32 v[22:23], v[22:23], s[26:27], v[244:245] op_sel_hi:[1,0,0]\n\t"
      "ds_read_b64_tr_b16 a[50:51], v232 offset:12288\n\t"
      "ds_read_b64_tr_b16 a[52:53], v233 offset:8192\n\t"
      "ds_read_b64_tr_b16 a[54:55], v233 offset:12288\n\t"
      "ds_read_b64_tr_b16 a[56:57], v234 offset:8192\n\t"
      "v_cvt_pk_bf16_f32 v4, v4, v5\n\t"
      "v_cvt_pk_bf16_f32 v5, v6, v7\n\t"
      "v_cvt_pk_bf16_f32 v6, v16, v17\n\t"
      "v_cvt_pk_bf16_f32 v7, v18, v19\n\t"
      "v_exp_f32 v8, v8\n\t"
      "v_exp_f32 v9, v9\n\t"
      "v_exp_f32 v10, v10\n\t"
      "v_exp_f32 v11, v11\n\t"
      "v_exp_f32 v20, v20\n\t"
      "v_exp_f32 v21, v21\n\t"
      "v_exp_f32 v22, v22\n\t"
      "v_exp_f32 v23, v23\n\t"
      "v_pk_fma_f32 v[24:25], v[24:25], s[26:27], v[240:241] op_sel_hi:[1,0,0]\n\t"
      "v_pk_fma_f32 v[26:27], v[26:27], s[26:27], v[240:241] op_sel_hi:[1,0,0]\n\t"
      "v_pk_fma_f32 v[36:37], v[36:37], s[26:27], v[240:241] op_sel_hi:[1,0,0]\n\t"
      "v_pk_fma_f32 v[38:39], v[38:39], s[26:27], v[240:241] op_sel_hi:[1,0,0]\n\t"
      "ds_read_b64_tr_b16 a[58:59], v234 offset:12288\n\t"
      "ds_read_b64_tr_b16 a[60:61], v235 offset:8192\n\t"
      "ds_read_b64_tr_b16 a[62:63], v235 offset:12288\n\t"
      "s_mov_b32 m0, s90\n\t"
      "v_cvt_pk_bf16_f32 v8, v8, v9\n\t"
      "v_cvt_pk_bf16_f32 v9, v10, v11\n\t"
      "v_cvt_pk_bf16_f32 v10, v20, v21\n\t"
      "v_cvt_pk_bf16_f32 v11, v22, v23\n\t"
      "v_exp_f32 v24, v24\n\t"
      "v_exp_f32 v25, v25\n\t"
      "v_exp_f32 v26, v26\n\t"
      "v_exp_f32 v27, v27\n\t"
      "v_exp_f32 v36, v36\n\t"
      "v_exp_f32 v37, v37\n\t"
      "v_exp_f32 v38, v38\n\t"
      "v_exp_f32 v39, v39\n\t"
      "v_pk_fma_f32 v[28:29], v[28:29], s[26:27], v[242:243] op_sel_hi:[1,0,0]\n\t"
      "v_pk_fma_f32 v[30:31], v[30:31], s[26:27], v[242:243] op_sel_hi:[1,0,0]\n\t"
      "v_pk_fma_f32 v[40:41], v[40:41], s[26:27], v[242:243] op_sel_hi:[1,0,0]\n\t"
      "v_pk_fma_f32 v[42:43], v[42:43], s[26:27], v[242:243] op_sel_hi:[1,0,0]\n\t"
      "buffer_load_dwordx4 v204, s[32:35], s60 offen nt lds\n\t"
      "buffer_load_dwordx4 v205, s[32:35], s60 offen offset:1024 nt lds\n\t"
      "buffer_load_dwordx4 v206, s[32:35], s60 offen offset:2048 nt lds\n\t"
      "buffer_load_dwordx4 v207, s[32:35], s60 offen offset:3072 nt lds\n\t"
      "v_cvt_pk_bf16_f32 v24, v24, v25\n\t"
      "v_cvt_pk_bf16_f32 v25, v26, v27\n\t"
      "v_cvt_pk_bf16_f32 v26, v36, v37\n\t"
      "v_cvt_pk_bf16_f32 v27, v38, v39\n\t"
      "v_exp_f32 v28, v28\n\t"
      "v_exp_f32 v29, v29\n\t"
      "v_exp_f32 v30, v30\n\t"
      "v_exp_f32 v31, v31\n\t"
      "v_exp_f32 v40, v40\n\t"
      "v_exp_f32 v41, v41\n\t"
      "v_exp_f32 v42, v42\n\t"
      "v_exp_f32 v43, v43\n\t"
      "v_pk_fma_f32 v[32:33], v[32:33], s[26:27], v[244:245] op_sel_hi:[1,0,0]\n\t"
      "v_pk_fma_f32 v[34:35], v[34:35], s[26:27], v[244:245] op_sel_hi:[1,0,0]\n\t"
      "v_pk_fma_f32 v[44:45], v[44:45], s[26:27], v[244:245] op_sel_hi:[1,0,0]\n\t"
      "v_pk_fma_f32 v[46:47], v[46:47], s[26:27], v[244:245] op_sel_hi:[1,0,0]\n\t"
      "s_mov_b32 m0, s91\n\t"
      "s_nop 0\n\t"
      "buffer_load_dwordx4 v204, s[32:35], s61 offen nt lds\n\t"
      "buffer_load_dwordx4 v205, s[32:35], s61 offen offset:1024 nt lds\n\t"
      "buffer_load_dwordx4 v206, s[32:35], s61 offen offset:2048 nt lds\n\t"
      "v_cvt_pk_bf16_f32 v28, v28, v29\n\t"
      "v_cvt_pk_bf16_f32 v29, v30, v31\n\t"
      "v_cvt_pk_bf16_f32 v30, v40, v41\n\t"
      "v_cvt_pk_bf16_f32 v31, v42, v43\n\t"
      "v_exp_f32 v32, v32\n\t"
      "v_exp_f32 v33, v33\n\t"
      "v_exp_f32 v34, v34\n\t"
      "v_exp_f32 v35, v35\n\t"
      "v_exp_f32 v44, v44\n\t"
      "v_exp_f32 v45, v45\n\t"
      "v_exp_f32 v46, v46\n\t"
      "v_exp_f32 v47, v47\n\t"
      "buffer_load_dwordx4 v207, s[32:35], s61 offen offset:3072 nt lds\n\t"
      "v_cvt_pk_bf16_f32 v32, v32, v33\n\t"
      "v_cvt_pk_bf16_f32 v33, v34, v35\n\t"
      "v_cvt_pk_bf16_f32 v34, v44, v45\n\t"
      "v_cvt_pk_bf16_f32 v35, v46, v47\n\t"
      "s_waitcnt lgkmcnt(0)\n\t"
      "s_mov_b32 m0, s92\n\t"
      "v_mfma_f32_16x16x32_bf16 v[192:195], a[208:211], v[0:3], 0\n\t"
      "buffer_load_dwordx4 v204, s[32:35], s62 offen nt lds\n\t"
      "v_mfma_f32_16x16x32_bf16 v[196:199], a[208:211], v[4:7], 0\n\t"
      "v_mfma_f32_16x16x32_bf16 v[200:203], a[208:211], v[8:11], 0\n\t"
      "v_mfma_f32_16x16x32_bf16 v[12:15], a[0:3], v[0:3], 0\n\t"
      "v_mfma_f32_16x16x32_bf16 v[16:19], a[0:3], v[4:7], 0\n\t"
      "v_mfma_f32_16x16x32_bf16 v[20:23], a[0:3], v[8:11], 0\n\t"
      "v_mfma_f32_16x16x32_bf16 v[36:39], a[4:7], v[0:3], 0\n\t"
      "buffer_load_dwordx4 v205, s[32:35], s62 offen offset:1024 nt lds\n\t"
      "v_mfma_f32_16x16x32_bf16 v[40:43], a[4:7], v[4:7], 0\n\t"
      "v_mfma_f32_16x16x32_bf16 v[44:47], a[4:7], v[8:11], 0\n\t"
      "v_mfma_f32_16x16x32_bf16 a[224:227], a[8:11], v[0:3], 0\n\t"
      "v_mfma_f32_16x16x32_bf16 a[228:231], a[8:11], v[4:7], 0\n\t"
      "v_mfma_f32_16x16x32_bf16 a[232:235], a[8:11], v[8:11], 0\n\t"
      "v_mfma_f32_16x16x32_bf16 a[236:239], a[12:15], v[0:3], 0\n\t"
      "v_mfma_f32_16x16x32_bf16 a[240:243], a[12:15], v[4:7], 0\n\t"
      "buffer_load_dwordx4 v206, s[32:35], s62 offen offset:2048 nt lds\n\t"
      "v_mfma_f32_16x16x32_bf16 a[244:247], a[12:15], v[8:11], 0\n\t"
      "v_mfma_f32_16x16x32_bf16 a[64:67], a[16:19], v[0:3], 0\n\t"
      "v_mfma_f32_16x16x32_bf16 a[68:71], a[16:19], v[4:7], 0\n\t"
      "v_mfma_f32_16x16x32_bf16 a[72:75], a[16:19], v[8:11], 0\n\t"
      "v_mfma_f32_16x16x32_bf16 a[76:79], a[20:23], v[0:3], 0\n\t"
      "v_mfma_f32_16x16x32_bf16 a[80:83], a[20:23], v[4:7], 0\n\t"
      "v_mfma_f32_16x16x32_bf16 a[84:87], a[20:23], v[8:11], 0\n\t"
      "buffer_load_dwordx4 v207, s[32:35], s62 offen offset:3072 nt lds\n\t"
      "v_mfma_f32_16x16x32_bf16 a[88:91], a[24:27], v[0:3], 0\n\t"
      "v_mfma_f32_16x16x32_bf16 a[92:95], a[24:27], v[4:7], 0\n\t"
      "v_mfma_f32_16x16x32_bf16 a[96:99], a[24:27], v[8:11], 0\n\t"
      "v_mfma_f32_16x16x32_bf16 a[100:103], a[28:31], v[0:3], 0\n\t"
      "v_mfma_f32_16x16x32_bf16 a[104:107], a[28:31], v[4:7], 0\n\t"
      "v_mfma_f32_16x16x32_bf16 a[108:111], a[28:31], v[8:11], 0\n\t"
      "s_waitcnt vmcnt(20)\n\t"
      "v_mfma_f32_16x16x32_bf16 v[192:195], a[208:211], v[24:27], v[192:195]\n\t"
      "ds_read_b64_tr_b16 a[0:1], v228 offset:16384\n\t"
      "v_mfma_f32_16x16x32_bf16 v[196:199], a[208:211], v[28:31], v[196:199]\n\t"
      "ds_read_b64_tr_b16 a[2:3], v228 offset:20480\n\t"
      "v_mfma_f32_16x16x32_bf16 v[200:203], a[208:211], v[32:35], v[200:203]\n\t"
      "ds_read_b64_tr_b16 a[4:5], v229 offset:16384\n\t"
      "s_mov_b32 m0, s93\n\t"
      "v_mfma_f32_16x16x32_bf16 v[12:15], a[32:35], v[24:27], v[12:15]\n\t"
      "buffer_load_dwordx4 v204, s[32:35], s63 offen nt lds\n\t"
      "v_mfma_f32_16x16x32_bf16 v[16:19], a[32:35], v[28:31], v[16:19]\n\t"
      "ds_read_b64_tr_b16 a[6:7], v229 offset:20480\n\t"
      "v_mfma_f32_16x16x32_bf16 v[20:23], a[32:35], v[32:35], v[20:23]\n\t"
      "ds_read_b64_tr_b16 a[8:9], v230 offset:16384\n\t"
      "v_mfma_f32_16x16x32_bf16 v[36:39], a[36:39], v[24:27], v[36:39]\n\t"
      "v_mfma_f32_16x16x32_bf16 v[40:43], a[36:39], v[28:31], v[40:43]\n\t"
      "ds_read_b64_tr_b16 a[10:11], v230 offset:20480\n\t"
      "v_mfma_f32_16x16x32_bf16 v[44:47], a[36:39], v[32:35], v[44:47]\n\t"
      "ds_read_b64_tr_b16 a[12:13], v231 offset:16384\n\t"
      "v_mfma_f32_16x16x32_bf16 a[224:227], a[40:43], v[24:27], a[224:227]\n\t"
      "buffer_load_dwordx4 v205, s[32:35], s63 offen offset:1024 nt lds\n\t"
      "v_mfma_f32_16x16x32_bf16 a[228:231], a[40:43], v[28:31], a[228:231]\n\t"
      "ds_read_b64_tr_b16 a[14:15], v231 offset:20480\n\t"
      "v_mfma_f32_16x16x32_bf16 a[232:235], a[40:43], v[32:35], a[232:235]\n\t"
      "ds_read_b64_tr_b16 a[16:17], v232 offset:16384\n\t"
      "v_mfma_f32_16x16x32_bf16 a[236:239], a[44:47], v[24:27], a[236:239]\n\t"
      "ds_read_b64_tr_b16 a[18:19], v232 offset:20480\n\t"
      "v_mfma_f32_16x16x32_bf16 a[240:243], a[44:47], v[28:31], a[240:243]\n\t"
      "v_mfma_f32_16x16x32_bf16 a[244:247], a[44:47], v[32:35], a[244:247]\n\t"
      "ds_read_b64_tr_b16 a[20:21], v233 offset:16384\n\t"
      "v_mfma_f32_16x16x32_bf16 a[64:67], a[48:51], v[24:27], a[64:67]\n\t"
      "ds_read_b64_tr_b16 a[22:23], v233 offset:20480\n\t"
      "v_mfma_f32_16x16x32_bf16 a[68:71], a[48:51], v[28:31], a[68:71]\n\t"
      "v_mfma_f32_16x16x32_bf16 a[72:75], a[48:51], v[32:35], a[72:75]\n\t"
      "ds_read_b64_tr_b16 a[24:25], v234 offset:16384\n\t"
      "v_mfma_f32_16x16x32_bf16 a[76:79], a[52:55], v[24:27], a[76:79]\n\t"
      "ds_read_b64_tr_b16 a[26:27], v234 offset:20480\n\t"
      "v_mfma_f32_16x16x32_bf16 a[80:83], a[52:55], v[28:31], a[80:83]\n\t"
      "buffer_load_dwordx4 v206, s[32:35], s63 offen offset:2048 nt lds\n\t"
      "v_mfma_f32_16x16x32_bf16 a[84:87], a[52:55], v[32:35], a[84:87]\n\t"
      "ds_read_b64_tr_b16 a[28:29], v235 offset:16384\n\t"
      "v_mfma_f32_16x16x32_bf16 a[88:91], a[56:59], v[24:27], a[88:91]\n\t"
      "ds_read_b64_tr_b16 a[30:31], v235 offset:20480\n\t"
      "v_mfma_f32_16x16x32_bf16 a[92:95], a[56:59], v[28:31], a[92:95]\n\t"
      "v_mfma_f32_16x16x32_bf16 a[96:99], a[56:59], v[32:35], a[96:99]\n\t"
      "v_mfma_f32_16x16x32_bf16 a[100:103], a[60:63], v[24:27], a[100:103]\n\t"
      "buffer_load_dwordx4 v207, s[32:35], s63 offen offset:3072 nt lds\n\t"
      "v_mfma_f32_16x16x32_bf16 a[104:107], a[60:63], v[28:31], a[104:107]\n\t"
      "v_mfma_f32_16x16x32_bf16 a[108:111], a[60:63], v[32:35], a[108:111]\n\t"
      "s_waitcnt lgkmcnt(0)\n\t"
      "s_waitcnt vmcnt(16)\n\t"
      "v_pk_fma_f32 v[48:49], v[48:49], s[26:27], v[240:241] op_sel_hi:[1,0,0]\n\t"
      "v_pk_fma_f32 v[50:51], v[50:51], s[26:27], v[240:241] op_sel_hi:[1,0,0]\n\t"
      "v_pk_fma_f32 v[60:61], v[60:61], s[26:27], v[240:241] op_sel_hi:[1,0,0]\n\t"
      "v_pk_fma_f32 v[62:63], v[62:63], s[26:27], v[240:241] op_sel_hi:[1,0,0]\n\t"
      "v_exp_f32 v48, v48\n\t"
      "v_exp_f32 v49, v49\n\t"
      "v_exp_f32 v50, v50\n\t"
      "v_exp_f32 v51, v51\n\t"
      "v_exp_f32 v60, v60\n\t"
      "v_exp_f32 v61, v61\n\t"
      "v_exp_f32 v62, v62\n\t"
      "v_exp_f32 v63, v63\n\t"
      "v_pk_fma_f32 v[52:53], v[52:53], s[26:27], v[242:243] op_sel_hi:[1,0,0]\n\t"
      "v_pk_fma_f32 v[54:55], v[54:55], s[26:27], v[242:243] op_sel_hi:[1,0,0]\n\t"
      "v_pk_fma_f32 v[64:65], v[64:65], s[26:27], v[242:243] op_sel_hi:[1,0,0]\n\t"
      "v_pk_fma_f32 v[66:67], v[66:67], s[26:27], v[242:243] op_sel_hi:[1,0,0]\n\t"
      "ds_read_b64_tr_b16 a[32:33], v228 offset:24576\n\t"
      "ds_read_b64_tr_b16 a[34:35], v228 offset:28672\n\t"
      "ds_read_b64_tr_b16 a[36:37], v229 offset:24576\n\t"
      "ds_read_b64_tr_b16 a[38:39], v229 offset:28672\n\t"
      "v_cvt_pk_bf16_f32 v48, v48, v49\n\t"
      "v_cvt_pk_bf16_f32 v49, v50, v51\n\t"
      "v_cvt_pk_bf16_f32 v50, v60, v61\n\t"
      "v_cvt_pk_bf16_f32 v51, v62, v63\n\t"
      "v_exp_f32 v52, v52\n\t"
      "v_exp_f32 v53, v53\n\t"
      "v_exp_f32 v54, v54\n\t"
      "v_exp_f32 v55, v55\n\t"
      "v_exp_f32 v64, v64\n\t"
      "v_exp_f32 v65, v65\n\t"
      "v_exp_f32 v66, v66\n\t"
      "v_exp_f32 v67, v67\n\t"
      "v_pk_fma_f32 v[56:57], v[56:57], s[26:27], v[244:245] op_sel_hi:[1,0,0]\n\t"
      "v_pk_fma_f32 v[58:59], v[58:59], s[26:27], v[244:245] op_sel_hi:[1,0,0]\n\t"
      "v_pk_fma_f32 v[68:69], v[68:69], s[26:27], v[244:245] op_sel_hi:[1,0,0]\n\t"
      "v_pk_fma_f32 v[70:71], v[70:71], s[26:27], v[244:245] op_sel_hi:[1,0,0]\n\t"
      "ds_read_b64_tr_b16 a[40:41], v230 offset:24576\n\t"
      "ds_read_b64_tr_b16 a[42:43], v230 offset:28672\n\t"
      "ds_read_b64_tr_b16 a[44:45], v231 offset:24576\n\t"
      "ds_read_b64_tr_b16 a[46:47], v231 offset:28672\n\t"
      "v_cvt_pk_bf16_f32 v52, v52, v53\n\t"
      "v_cvt_pk_bf16_f32 v53, v54, v55\n\t"
      "v_cvt_pk_bf16_f32 v54, v64, v65\n\t"
      "v_cvt_pk_bf16_f32 v55, v66, v67\n\t"
      "v_exp_f32 v56, v56\n\t"
      "v_exp_f32 v57, v57\n\t"
      "v_exp_f32 v58, v58\n\t"
      "v_exp_f32 v59, v59\n\t"
      "v_exp_f32 v68, v68\n\t"
      "v_exp_f32 v69, v69\n\t"
      "v_exp_f32 v70, v70\n\t"
      "v_exp_f32 v71, v71\n\t"
      "v_pk_fma_f32 v[72:73], v[72:73], s[26:27], v[240:241] op_sel_hi:[1,0,0]\n\t"
      "v_pk_fma_f32 v[74:75], v[74:75], s[26:27], v[240:241] op_sel_hi:[1,0,0]\n\t"
      "v_pk_fma_f32 v[84:85], v[84:85], s[26:27], v[240:241] op_sel_hi:[1,0,0]\n\t"
      "v_pk_fma_f32 v[86:87], v[86:87], s[26:27], v[240:241] op_sel_hi:[1,0,0]\n\t"
      "ds_read_b64_tr_b16 a[48:49], v232 offset:24576\n\t"
      "ds_read_b64_tr_b16 a[50:51], v232 offset:28672\n\t"
      "ds_read_b64_tr_b16 a[52:53], v233 offset:24576\n\t"
      "ds_read_b64_tr_b16 a[54:55], v233 offset:28672\n\t"
      "v_cvt_pk_bf16_f32 v56, v56, v57\n\t"
      "v_cvt_pk_bf16_f32 v57, v58, v59\n\t"
      "v_cvt_pk_bf16_f32 v58, v68, v69\n\t"
      "v_cvt_pk_bf16_f32 v59, v70, v71\n\t"
      "v_exp_f32 v72, v72\n\t"
      "v_exp_f32 v73, v73\n\t"
      "v_exp_f32 v74, v74\n\t"
      "v_exp_f32 v75, v75\n\t"
      "v_exp_f32 v84, v84\n\t"
      "v_exp_f32 v85, v85\n\t"
      "v_exp_f32 v86, v86\n\t"
      "v_exp_f32 v87, v87\n\t"
      "v_pk_fma_f32 v[76:77], v[76:77], s[26:27], v[242:243] op_sel_hi:[1,0,0]\n\t"
      "v_pk_fma_f32 v[78:79], v[78:79], s[26:27], v[242:243] op_sel_hi:[1,0,0]\n\t"
      "v_pk_fma_f32 v[88:89], v[88:89], s[26:27], v[242:243] op_sel_hi:[1,0,0]\n\t"
      "v_pk_fma_f32 v[90:91], v[90:91], s[26:27], v[242:243] op_sel_hi:[1,0,0]\n\t"
      "ds_read_b64_tr_b16 a[56:57], v234 offset:24576\n\t"
      "ds_read_b64_tr_b16 a[58:59], v234 offset:28672\n\t"
      "ds_read_b64_tr_b16 a[60:61], v235 offset:24576\n\t"
      "ds_read_b64_tr_b16 a[62:63], v235 offset:28672\n\t"
      "v_cvt_pk_bf16_f32 v72, v72, v73\n\t"
      "v_cvt_pk_bf16_f32 v73, v74, v75\n\t"
      "v_cvt_pk_bf16_f32 v74, v84, v85\n\t"
      "v_cvt_pk_bf16_f32 v75, v86, v87\n\t"
      "v_exp_f32 v76, v76\n\t"
      "v_exp_f32 v77, v77\n\t"
      "v_exp_f32 v78, v78\n\t"
      "v_exp_f32 v79, v79\n\t"
      "v_exp_f32 v88, v88\n\t"
      "v_exp_f32 v89, v89\n\t"
      "v_exp_f32 v90, v90\n\t"
      "v_exp_f32 v91, v91\n\t"
      "v_pk_fma_f32 v[80:81], v[80:81], s[26:27], v[244:245] op_sel_hi:[1,0,0]\n\t"
      "v_pk_fma_f32 v[82:83], v[82:83], s[26:27], v[244:245] op_sel_hi:[1,0,0]\n\t"
      "v_pk_fma_f32 v[92:93], v[92:93], s[26:27], v[244:245] op_sel_hi:[1,0,0]\n\t"
      "v_pk_fma_f32 v[94:95], v[94:95], s[26:27], v[244:245] op_sel_hi:[1,0,0]\n\t"
      "s_mov_b32 m0, s94\n\t"
      "s_nop 0\n\t"
      "buffer_load_dwordx4 v204, s[32:35], s64 offen nt lds\n\t"
      "buffer_load_dwordx4 v205, s[32:35], s64 offen offset:1024 nt lds\n\t"
      "buffer_load_dwordx4 v206, s[32:35], s64 offen offset:2048 nt lds\n\t"
      "v_cvt_pk_bf16_f32 v76, v76, v77\n\t"
      "v_cvt_pk_bf16_f32 v77, v78, v79\n\t"
      "v_cvt_pk_bf16_f32 v78, v88, v89\n\t"
      "v_cvt_pk_bf16_f32 v79, v90, v91\n\t"
      "v_exp_f32 v80, v80\n\t"
      "v_exp_f32 v81, v81\n\t"
      "v_exp_f32 v82, v82\n\t"
      "v_exp_f32 v83, v83\n\t"
      "v_exp_f32 v92, v92\n\t"
      "v_exp_f32 v93, v93\n\t"
      "v_exp_f32 v94, v94\n\t"
      "v_exp_f32 v95, v95\n\t"
      "buffer_load_dwordx4 v207, s[32:35], s64 offen offset:3072 nt lds\n\t"
      "v_cvt_pk_bf16_f32 v80, v80, v81\n\t"
      "v_cvt_pk_bf16_f32 v81, v82, v83\n\t"
      "v_cvt_pk_bf16_f32 v82, v92, v93\n\t"
      "v_cvt_pk_bf16_f32 v83, v94, v95\n\t"
      "s_waitcnt lgkmcnt(0)\n\t"
      "s_mov_b32 m0, s95\n\t"
      "v_mfma_f32_16x16x32_bf16 v[192:195], a[208:211], v[48:51], v[192:195]\n\t"
      "buffer_load_dwordx4 v204, s[32:35], s65 offen nt lds\n\t"
      "v_mfma_f32_16x16x32_bf16 v[196:199], a[208:211], v[52:55], v[196:199]\n\t"
      "v_mfma_f32_16x16x32_bf16 v[200:203], a[208:211], v[56:59], v[200:203]\n\t"
      "v_mfma_f32_16x16x32_bf16 v[12:15], a[0:3], v[48:51], v[12:15]\n\t"
      "v_mfma_f32_16x16x32_bf16 v[16:19], a[0:3], v[52:55], v[16:19]\n\t"
      "v_mfma_f32_16x16x32_bf16 v[20:23], a[0:3], v[56:59], v[20:23]\n\t"
      "v_mfma_f32_16x16x32_bf16 v[36:39], a[4:7], v[48:51], v[36:39]\n\t"
      "buffer_load_dwordx4 v205, s[32:35], s65 offen offset:1024 nt lds\n\t"
      "v_mfma_f32_16x16x32_bf16 v[40:43], a[4:7], v[52:55], v[40:43]\n\t"
      "v_mfma_f32_16x16x32_bf16 v[44:47], a[4:7], v[56:59], v[44:47]\n\t"
      "v_mfma_f32_16x16x32_bf16 a[224:227], a[8:11], v[48:51], a[224:227]\n\t"
      "v_mfma_f32_16x16x32_bf16 a[228:231], a[8:11], v[52:55], a[228:231]\n\t"
      "v_mfma_f32_16x16x32_bf16 a[232:235], a[8:11], v[56:59], a[232:235]\n\t"
      "v_mfma_f32_16x16x32_bf16 a[236:239], a[12:15], v[48:51], a[236:239]\n\t"
      "v_mfma_f32_16x16x32_bf16 a[240:243], a[12:15], v[52:55], a[240:243]\n\t"
      "buffer_load_dwordx4 v206, s[32:35], s65 offen offset:2048 nt lds\n\t"
      "v_mfma_f32_16x16x32_bf16 a[244:247], a[12:15], v[56:59], a[244:247]\n\t"
      "v_mfma_f32_16x16x32_bf16 a[64:67], a[16:19], v[48:51], a[64:67]\n\t"
      "v_mfma_f32_16x16x32_bf16 a[68:71], a[16:19], v[52:55], a[68:71]\n\t"
      "v_mfma_f32_16x16x32_bf16 a[72:75], a[16:19], v[56:59], a[72:75]\n\t"
      "v_mfma_f32_16x16x32_bf16 a[76:79], a[20:23], v[48:51], a[76:79]\n\t"
      "v_mfma_f32_16x16x32_bf16 a[80:83], a[20:23], v[52:55], a[80:83]\n\t"
      "v_mfma_f32_16x16x32_bf16 a[84:87], a[20:23], v[56:59], a[84:87]\n\t"
      "buffer_load_dwordx4 v207, s[32:35], s65 offen offset:3072 nt lds\n\t"
      "v_mfma_f32_16x16x32_bf16 a[88:91], a[24:27], v[48:51], a[88:91]\n\t"
      "v_mfma_f32_16x16x32_bf16 a[92:95], a[24:27], v[52:55], a[92:95]\n\t"
      "v_mfma_f32_16x16x32_bf16 a[96:99], a[24:27], v[56:59], a[96:99]\n\t"
      "v_mfma_f32_16x16x32_bf16 a[100:103], a[28:31], v[48:51], a[100:103]\n\t"
      "v_mfma_f32_16x16x32_bf16 a[104:107], a[28:31], v[52:55], a[104:107]\n\t"
      "v_mfma_f32_16x16x32_bf16 a[108:111], a[28:31], v[56:59], a[108:111]\n\t"
      "s_waitcnt vmcnt(16)\n\t"
      "v_mfma_f32_16x16x32_bf16 v[192:195], a[208:211], v[72:75], v[192:195]\n\t"
      "ds_read_b64_tr_b16 a[0:1], v228\n\t"
      "v_mfma_f32_16x16x32_bf16 v[196:199], a[208:211], v[76:79], v[196:199]\n\t"
      "ds_read_b64_tr_b16 a[2:3], v228 offset:4096\n\t"
      "v_mfma_f32_16x16x32_bf16 v[200:203], a[208:211], v[80:83], v[200:203]\n\t"
      "ds_read_b64_tr_b16 a[4:5], v229\n\t"
      "s_mov_b32 m0, s96\n\t"
      "v_mfma_f32_16x16x32_bf16 v[12:15], a[32:35], v[72:75], v[12:15]\n\t"
      "buffer_load_dwordx4 v204, s[32:35], s66 offen nt lds\n\t"
      "v_mfma_f32_16x16x32_bf16 v[16:19], a[32:35], v[76:79], v[16:19]\n\t"
      "ds_read_b64_tr_b16 a[6:7], v229 offset:4096\n\t"
      "v_mfma_f32_16x16x32_bf16 v[20:23], a[32:35], v[80:83], v[20:23]\n\t"
      "ds_read_b64_tr_b16 a[8:9], v230\n\t"
      "v_mfma_f32_16x16x32_bf16 v[36:39], a[36:39], v[72:75], v[36:39]\n\t"
      "v_mfma_f32_16x16x32_bf16 v[40:43], a[36:39], v[76:79], v[40:43]\n\t"
      "ds_read_b64_tr_b16 a[10:11], v230 offset:4096\n\t"
      "v_mfma_f32_16x16x32_bf16 v[44:47], a[36:39], v[80:83], v[44:47]\n\t"
      "ds_read_b64_tr_b16 a[12:13], v231\n\t"
      "v_mfma_f32_16x16x32_bf16 a[224:227], a[40:43], v[72:75], a[224:227]\n\t"
      "buffer_load_dwordx4 v205, s[32:35], s66 offen offset:1024 nt lds\n\t"
      "v_mfma_f32_16x16x32_bf16 a[228:231], a[40:43], v[76:79], a[228:231]\n\t"
      "ds_read_b64_tr_b16 a[14:15], v231 offset:4096\n\t"
      "v_mfma_f32_16x16x32_bf16 a[232:235], a[40:43], v[80:83], a[232:235]\n\t"
      "ds_read_b64_tr_b16 a[16:17], v232\n\t"
      "v_mfma_f32_16x16x32_bf16 a[236:239], a[44:47], v[72:75], a[236:239]\n\t"
      "ds_read_b64_tr_b16 a[18:19], v232 offset:4096\n\t"
      "v_mfma_f32_16x16x32_bf16 a[240:243], a[44:47], v[76:79], a[240:243]\n\t"
      "v_mfma_f32_16x16x32_bf16 a[244:247], a[44:47], v[80:83], a[244:247]\n\t"
      "ds_read_b64_tr_b16 a[20:21], v233\n\t"
      "v_mfma_f32_16x16x32_bf16 a[64:67], a[48:51], v[72:75], a[64:67]\n\t"
      "ds_read_b64_tr_b16 a[22:23], v233 offset:4096\n\t"
      "v_mfma_f32_16x16x32_bf16 a[68:71], a[48:51], v[76:79], a[68:71]\n\t"
      "v_mfma_f32_16x16x32_bf16 a[72:75], a[48:51], v[80:83], a[72:75]\n\t"
      "ds_read_b64_tr_b16 a[24:25], v234\n\t"
      "v_mfma_f32_16x16x32_bf16 a[76:79], a[52:55], v[72:75], a[76:79]\n\t"
      "ds_read_b64_tr_b16 a[26:27], v234 offset:4096\n\t"
      "v_mfma_f32_16x16x32_bf16 a[80:83], a[52:55], v[76:79], a[80:83]\n\t"
      "buffer_load_dwordx4 v206, s[32:35], s66 offen offset:2048 nt lds\n\t"
      "v_mfma_f32_16x16x32_bf16 a[84:87], a[52:55], v[80:83], a[84:87]\n\t"
      "ds_read_b64_tr_b16 a[28:29], v235\n\t"
      "v_mfma_f32_16x16x32_bf16 a[88:91], a[56:59], v[72:75], a[88:91]\n\t"
      "ds_read_b64_tr_b16 a[30:31], v235 offset:4096\n\t"
      "v_mfma_f32_16x16x32_bf16 a[92:95], a[56:59], v[76:79], a[92:95]\n\t"
      "v_mfma_f32_16x16x32_bf16 a[96:99], a[56:59], v[80:83], a[96:99]\n\t"
      "v_mfma_f32_16x16x32_bf16 a[100:103], a[60:63], v[72:75], a[100:103]\n\t"
      "buffer_load_dwordx4 v207, s[32:35], s66 offen offset:3072 nt lds\n\t"
      "v_mfma_f32_16x16x32_bf16 a[104:107], a[60:63], v[76:79], a[104:107]\n\t"
      "v_mfma_f32_16x16x32_bf16 a[108:111], a[60:63], v[80:83], a[108:111]\n\t"
      "v_pk_fma_f32 v[96:97], v[96:97], s[26:27], v[240:241] op_sel_hi:[1,0,0]\n\t"
      "v_pk_fma_f32 v[98:99], v[98:99], s[26:27], v[240:241] op_sel_hi:[1,0,0]\n\t"
      "v_pk_fma_f32 v[108:109], v[108:109], s[26:27], v[240:241] op_sel_hi:[1,0,0]\n\t"
      "v_pk_fma_f32 v[110:111], v[110:111], s[26:27], v[240:241] op_sel_hi:[1,0,0]\n\t"
      "v_exp_f32 v96, v96\n\t"
      "v_exp_f32 v97, v97\n\t"
      "v_exp_f32 v98, v98\n\t"
      "v_exp_f32 v99, v99\n\t"
      "v_exp_f32 v108, v108\n\t"
      "v_exp_f32 v109, v109\n\t"
      "v_exp_f32 v110, v110\n\t"
      "v_exp_f32 v111, v111\n\t"
      "v_pk_fma_f32 v[100:101], v[100:101], s[26:27], v[242:243] op_sel_hi:[1,0,0]\n\t"
      "v_pk_fma_f32 v[102:103], v[102:103], s[26:27], v[242:243] op_sel_hi:[1,0,0]\n\t"
      "v_pk_fma_f32 v[112:113], v[112:113], s[26:27], v[242:243] op_sel_hi:[1,0,0]\n\t"
      "v_pk_fma_f32 v[114:115], v[114:115], s[26:27], v[242:243] op_sel_hi:[1,0,0]\n\t"
      "s_mov_b32 m0, s97\n\t"
      "v_cvt_pk_bf16_f32 v96, v96, v97\n\t"
      "v_cvt_pk_bf16_f32 v97, v98, v99\n\t"
      "v_cvt_pk_bf16_f32 v98, v108, v109\n\t"
      "v_cvt_pk_bf16_f32 v99, v110, v111\n\t"
      "v_exp_f32 v100, v100\n\t"
      "v_exp_f32 v101, v101\n\t"
      "v_exp_f32 v102, v102\n\t"
      "v_exp_f32 v103, v103\n\t"
      "v_exp_f32 v112, v112\n\t"
      "v_exp_f32 v113, v113\n\t"
      "v_exp_f32 v114, v114\n\t"
      "v_exp_f32 v115, v115\n\t"
      "v_pk_fma_f32 v[104:105], v[104:105], s[26:27], v[244:245] op_sel_hi:[1,0,0]\n\t"
      "v_pk_fma_f32 v[106:107], v[106:107], s[26:27], v[244:245] op_sel_hi:[1,0,0]\n\t"
      "v_pk_fma_f32 v[116:117], v[116:117], s[26:27], v[244:245] op_sel_hi:[1,0,0]\n\t"
      "v_pk_fma_f32 v[118:119], v[118:119], s[26:27], v[244:245] op_sel_hi:[1,0,0]\n\t"
      "buffer_load_dwordx4 v204, s[32:35], s67 offen nt lds\n\t"
      "v_cvt_pk_bf16_f32 v100, v100, v101\n\t"
      "v_cvt_pk_bf16_f32 v101, v102, v103\n\t"
      "v_cvt_pk_bf16_f32 v102, v112, v113\n\t"
      "v_cvt_pk_bf16_f32 v103, v114, v115\n\t"
      "v_exp_f32 v104, v104\n\t"
      "v_exp_f32 v105, v105\n\t"
      "v_exp_f32 v106, v106\n\t"
      "v_exp_f32 v107, v107\n\t"
      "v_exp_f32 v116, v116\n\t"
      "v_exp_f32 v117, v117\n\t"
      "v_exp_f32 v118, v118\n\t"
      "v_exp_f32 v119, v119\n\t"
      "v_pk_fma_f32 v[120:121], v[120:121], s[26:27], v[240:241] op_sel_hi:[1,0,0]\n\t"
      "v_pk_fma_f32 v[122:123], v[122:123], s[26:27], v[240:241] op_sel_hi:[1,0,0]\n\t"
      "v_pk_fma_f32 v[132:133], v[132:133], s[26:27], v[240:241] op_sel_hi:[1,0,0]\n\t"
      "v_pk_fma_f32 v[134:135], v[134:135], s[26:27], v[240:241] op_sel_hi:[1,0,0]\n\t"
      "buffer_load_dwordx4 v205, s[32:35], s67 offen offset:1024 nt lds\n\t"
      "v_cvt_pk_bf16_f32 v104, v104, v105\n\t"
      "v_cvt_pk_bf16_f32 v105, v106, v107\n\t"
      "v_cvt_pk_bf16_f32 v106, v116, v117\n\t"
      "v_cvt_pk_bf16_f32 v107, v118, v119\n\t"
      "v_exp_f32 v120, v120\n\t"
      "v_exp_f32 v121, v121\n\t"
      "v_exp_f32 v122, v122\n\t"
      "v_exp_f32 v123, v123\n\t"
      "v_exp_f32 v132, v132\n\t"
      "v_exp_f32 v133, v133\n\t"
      "v_exp_f32 v134, v134\n\t"
      "v_exp_f32 v135, v135\n\t"
      "v_pk_fma_f32 v[124:125], v[124:125], s[26:27], v[242:243] op_sel_hi:[1,0,0]\n\t"
      "v_pk_fma_f32 v[126:127], v[126:127], s[26:27], v[242:243] op_sel_hi:[1,0,0]\n\t"
      "v_pk_fma_f32 v[136:137], v[136:137], s[26:27], v[242:243] op_sel_hi:[1,0,0]\n\t"
      "v_pk_fma_f32 v[138:139], v[138:139], s[26:27], v[242:243] op_sel_hi:[1,0,0]\n\t"
      "buffer_load_dwordx4 v206, s[32:35], s67 offen offset:2048 nt lds\n\t"
      "v_cvt_pk_bf16_f32 v120, v120, v121\n\t"
      "v_cvt_pk_bf16_f32 v121, v122, v123\n\t"
      "v_cvt_pk_bf16_f32 v122, v132, v133\n\t"
      "v_cvt_pk_bf16_f32 v123, v134, v135\n\t"
      "v_exp_f32 v124, v124\n\t"
      "v_exp_f32 v125, v125\n\t"
      "v_exp_f32 v126, v126\n\t"
      "v_exp_f32 v127, v127\n\t"
      "v_exp_f32 v136, v136\n\t"
      "v_exp_f32 v137, v137\n\t"
      "v_exp_f32 v138, v138\n\t"
      "v_exp_f32 v139, v139\n\t"
      "v_pk_fma_f32 v[128:129], v[128:129], s[26:27], v[244:245] op_sel_hi:[1,0,0]\n\t"
      "v_pk_fma_f32 v[130:131], v[130:131], s[26:27], v[244:245] op_sel_hi:[1,0,0]\n\t"
      "v_pk_fma_f32 v[140:141], v[140:141], s[26:27], v[244:245] op_sel_hi:[1,0,0]\n\t"
      "v_pk_fma_f32 v[142:143], v[142:143], s[26:27], v[244:245] op_sel_hi:[1,0,0]\n\t"
      "buffer_load_dwordx4 v207, s[32:35], s67 offen offset:3072 nt lds\n\t"
      "v_cvt_pk_bf16_f32 v124, v124, v125\n\t"
      "v_cvt_pk_bf16_f32 v125, v126, v127\n\t"
      "v_cvt_pk_bf16_f32 v126, v136, v137\n\t"
      "v_cvt_pk_bf16_f32 v127, v138, v139\n\t"
      "v_exp_f32 v128, v128\n\t"
      "v_exp_f32 v129, v129\n\t"
      "v_exp_f32 v130, v130\n\t"
      "v_exp_f32 v131, v131\n\t"
      "v_exp_f32 v140, v140\n\t"
      "v_exp_f32 v141, v141\n\t"
      "v_exp_f32 v142, v142\n\t"
      "v_exp_f32 v143, v143\n\t"
      "v_pk_fma_f32 v[144:145], v[144:145], s[26:27], v[240:241] op_sel_hi:[1,0,0]\n\t"
      "v_pk_fma_f32 v[146:147], v[146:147], s[26:27], v[240:241] op_sel_hi:[1,0,0]\n\t"
      "v_pk_fma_f32 v[156:157], v[156:157], s[26:27], v[240:241] op_sel_hi:[1,0,0]\n\t"
      "v_pk_fma_f32 v[158:159], v[158:159], s[26:27], v[240:241] op_sel_hi:[1,0,0]\n\t"
      "v_cvt_pk_bf16_f32 v128, v128, v129\n\t"
      "v_cvt_pk_bf16_f32 v129, v130, v131\n\t"
      "v_cvt_pk_bf16_f32 v130, v140, v141\n\t"
      "v_cvt_pk_bf16_f32 v131, v142, v143\n\t"
      "v_exp_f32 v144, v144\n\t"
      "v_exp_f32 v145, v145\n\t"
      "v_exp_f32 v146, v146\n\t"
      "v_exp_f32 v147, v147\n\t"
      "v_exp_f32 v156, v156\n\t"
      "v_exp_f32 v157, v157\n\t"
      "v_exp_f32 v158, v158\n\t"
      "v_exp_f32 v159, v159\n\t"
      "v_pk_fma_f32 v[148:149], v[148:149], s[26:27], v[242:243] op_sel_hi:[1,0,0]\n\t"
      "v_pk_fma_f32 v[150:151], v[150:151], s[26:27], v[242:243] op_sel_hi:[1,0,0]\n\t"
      "v_pk_fma_f32 v[160:161], v[160:161], s[26:27], v[242:243] op_sel_hi:[1,0,0]\n\t"
      "v_pk_fma_f32 v[162:163], v[162:163], s[26:27], v[242:243] op_sel_hi:[1,0,0]\n\t"
      "v_cvt_pk_bf16_f32 v144, v144, v145\n\t"
      "v_cvt_pk_bf16_f32 v145, v146, v147\n\t"
      "v_cvt_pk_bf16_f32 v146, v156, v157\n\t"
      "v_cvt_pk_bf16_f32 v147, v158, v159\n\t"
      "v_exp_f32 v148, v148\n\t"
      "v_exp_f32 v149, v149\n\t"
      "v_exp_f32 v150, v150\n\t"
      "v_exp_f32 v151, v151\n\t"
      "v_exp_f32 v160, v160\n\t"
      "v_exp_f32 v161, v161\n\t"
      "v_exp_f32 v162, v162\n\t"
      "v_exp_f32 v163, v163\n\t"
      "v_pk_fma_f32 v[152:153], v[152:153], s[26:27], v[244:245] op_sel_hi:[1,0,0]\n\t"
      "v_pk_fma_f32 v[154:155], v[154:155], s[26:27], v[244:245] op_sel_hi:[1,0,0]\n\t"
      "v_pk_fma_f32 v[164:165], v[164:165], s[26:27], v[244:245] op_sel_hi:[1,0,0]\n\t"
      "v_pk_fma_f32 v[166:167], v[166:167], s[26:27], v[244:245] op_sel_hi:[1,0,0]\n\t"
      "v_cvt_pk_bf16_f32 v148, v148, v149\n\t"
      "v_cvt_pk_bf16_f32 v149, v150, v151\n\t"
      "v_cvt_pk_bf16_f32 v150, v160, v161\n\t"
      "v_cvt_pk_bf16_f32 v151, v162, v163\n\t"
      "v_exp_f32 v152, v152\n\t"
      "v_exp_f32 v153, v153\n\t"
      "v_exp_f32 v154, v154\n\t"
      "v_exp_f32 v155, v155\n\t"
      "v_exp_f32 v164, v164\n\t"
      "v_exp_f32 v165, v165\n\t"
      "v_exp_f32 v166, v166\n\t"
      "v_exp_f32 v167, v167\n\t"
      "v_pk_fma_f32 v[168:169], v[168:169], s[26:27], v[240:241] op_sel_hi:[1,0,0]\n\t"
      "v_pk_fma_f32 v[170:171], v[170:171], s[26:27], v[240:241] op_sel_hi:[1,0,0]\n\t"
      "v_pk_fma_f32 v[180:181], v[180:181], s[26:27], v[240:241] op_sel_hi:[1,0,0]\n\t"
      "v_pk_fma_f32 v[182:183], v[182:183], s[26:27], v[240:241] op_sel_hi:[1,0,0]\n\t"
      "v_cvt_pk_bf16_f32 v152, v152, v153\n\t"
      "v_cvt_pk_bf16_f32 v153, v154, v155\n\t"
      "v_cvt_pk_bf16_f32 v154, v164, v165\n\t"
      "v_cvt_pk_bf16_f32 v155, v166, v167\n\t"
      "v_exp_f32 v168, v168\n\t"
      "v_exp_f32 v169, v169\n\t"
      "v_exp_f32 v170, v170\n\t"
      "v_exp_f32 v171, v171\n\t"
      "v_exp_f32 v180, v180\n\t"
      "v_exp_f32 v181, v181\n\t"
      "v_exp_f32 v182, v182\n\t"
      "v_exp_f32 v183, v183\n\t"
      "v_pk_fma_f32 v[172:173], v[172:173], s[26:27], v[242:243] op_sel_hi:[1,0,0]\n\t"
      "v_pk_fma_f32 v[174:175], v[174:175], s[26:27], v[242:243] op_sel_hi:[1,0,0]\n\t"
      "v_pk_fma_f32 v[184:185], v[184:185], s[26:27], v[242:243] op_sel_hi:[1,0,0]\n\t"
      "v_pk_fma_f32 v[186:187], v[186:187], s[26:27], v[242:243] op_sel_hi:[1,0,0]\n\t"
      "v_cvt_pk_bf16_f32 v168, v168, v169\n\t"
      "v_cvt_pk_bf16_f32 v169, v170, v171\n\t"
      "v_cvt_pk_bf16_f32 v170, v180, v181\n\t"
      "v_cvt_pk_bf16_f32 v171, v182, v183\n\t"
      "v_exp_f32 v172, v172\n\t"
      "v_exp_f32 v173, v173\n\t"
      "v_exp_f32 v174, v174\n\t"
      "v_exp_f32 v175, v175\n\t"
      "v_exp_f32 v184, v184\n\t"
      "v_exp_f32 v185, v185\n\t"
      "v_exp_f32 v186, v186\n\t"
      "v_exp_f32 v187, v187\n\t"
      "v_pk_fma_f32 v[176:177], v[176:177], s[26:27], v[244:245] op_sel_hi:[1,0,0]\n\t"
      "v_pk_fma_f32 v[178:179], v[178:179], s[26:27], v[244:245] op_sel_hi:[1,0,0]\n\t"
      "v_pk_fma_f32 v[188:189], v[188:189], s[26:27], v[244:245] op_sel_hi:[1,0,0]\n\t"
      "v_pk_fma_f32 v[190:191], v[190:191], s[26:27], v[244:245] op_sel_hi:[1,0,0]\n\t"
      "v_cvt_pk_bf16_f32 v172, v172, v173\n\t"
      "v_cvt_pk_bf16_f32 v173, v174, v175\n\t"
      "v_cvt_pk_bf16_f32 v174, v184, v185\n\t"
      "v_cvt_pk_bf16_f32 v175, v186, v187\n\t"
      "v_exp_f32 v176, v176\n\t"
      "v_exp_f32 v177, v177\n\t"
      "v_exp_f32 v178, v178\n\t"
      "v_exp_f32 v179, v179\n\t"
      "v_exp_f32 v188, v188\n\t"
      "v_exp_f32 v189, v189\n\t"
      "v_exp_f32 v190, v190\n\t"
      "v_exp_f32 v191, v191\n\t"
      "v_cvt_pk_bf16_f32 v176, v176, v177\n\t"
      "v_cvt_pk_bf16_f32 v177, v178, v179\n\t"
      "v_cvt_pk_bf16_f32 v178, v188, v189\n\t"
      "v_cvt_pk_bf16_f32 v179, v190, v191\n\t"
      "v_mfma_f32_16x16x32_bf16 v[192:195], a[208:211], v[96:99], v[192:195]\n\t"
      "v_accvgpr_read_b32 v240, a212\n\t"
      "v_mfma_f32_16x16x32_bf16 v[196:199], a[208:211], v[100:103], v[196:199]\n\t"
      "v_accvgpr_read_b32 v241, a213\n\t"
      "v_mfma_f32_16x16x32_bf16 v[200:203], a[208:211], v[104:107], v[200:203]\n\t"
      "v_accvgpr_read_b32 v242, a214\n\t"
      "v_mfma_f32_16x16x32_bf16 v[192:195], a[208:211], v[120:123], v[192:195]\n\t"
      "v_accvgpr_read_b32 v243, a215\n\t"
      "v_mfma_f32_16x16x32_bf16 v[196:199], a[208:211], v[124:127], v[196:199]\n\t"
      "v_xad_u32 v220, v218, 0, s14\n\t"
      "v_mfma_f32_16x16x32_bf16 v[200:203], a[208:211], v[128:131], v[200:203]\n\t"
      "v_xad_u32 v221, v218, 32, s14\n\t"
      "v_mfma_f32_16x16x32_bf16 v[192:195], a[208:211], v[144:147], v[192:195]\n\t"
      "v_xad_u32 v222, v218, 64, s14\n\t"
      "v_mfma_f32_16x16x32_bf16 v[196:199], a[208:211], v[148:151], v[196:199]\n\t"
      "v_xor_b32 v223, 96, v218\n\t"
      "v_add_u32 v223, s14, v223\n\t"
      "v_mfma_f32_16x16x32_bf16 v[200:203], a[208:211], v[152:155], v[200:203]\n\t"
      "v_xor_b32 v224, 128, v218\n\t"
      "v_add_u32 v224, s14, v224\n\t"
      "v_mfma_f32_16x16x32_bf16 v[192:195], a[208:211], v[168:171], v[192:195]\n\t"
      "v_xor_b32 v225, 160, v218\n\t"
      "v_add_u32 v225, s14, v225\n\t"
      "v_mfma_f32_16x16x32_bf16 v[196:199], a[208:211], v[172:175], v[196:199]\n\t"
      "v_xor_b32 v226, 192, v218\n\t"
      "v_add_u32 v226, s14, v226\n\t"
      "v_mfma_f32_16x16x32_bf16 v[200:203], a[208:211], v[176:179], v[200:203]\n\t"
      "v_xor_b32 v227, 224, v218\n\t"
      "v_add_u32 v227, s14, v227\n\t"
      "s_waitcnt lgkmcnt(0)\n\t"
      "s_waitcnt vmcnt(16)\n\t"
      "v_mfma_f32_16x16x32_bf16 a[224:227], a[8:11], v[96:99], a[224:227]\n\t"
      "ds_read_b64_tr_b16 a[32:33], v228 offset:8192\n\t"
      "v_mfma_f32_16x16x32_bf16 a[228:231], a[8:11], v[100:103], a[228:231]\n\t"
      "ds_read_b64_tr_b16 a[34:35], v228 offset:12288\n\t"
      "v_mov_b32 v193, v236\n\t"
      "v_mfma_f32_16x16x32_bf16 a[232:235], a[8:11], v[104:107], a[232:235]\n\t"
      "ds_write_b64 v242, v[192:193]\n\t"
      "ds_read_b64_tr_b16 a[36:37], v229 offset:8192\n\t"
      "v_mfma_f32_16x16x32_bf16 a[236:239], a[12:15], v[96:99], a[236:239]\n\t"
      "ds_read_b64_tr_b16 a[38:39], v229 offset:12288\n\t"
      "s_mov_b32 m0, s90\n\t"
      "v_mov_b32 v197, v237\n\t"
      "v_mfma_f32_16x16x32_bf16 a[240:243], a[12:15], v[100:103], a[240:243]\n\t"
      "ds_write_b64 v242, v[196:197] offset:128\n\t"
      "buffer_load_dwordx4 v208, s[44:47], s18 offen lds\n\t"
      "v_mfma_f32_16x16x32_bf16 a[244:247], a[12:15], v[104:107], a[244:247]\n\t"
      "ds_read_b64_tr_b16 a[40:41], v230 offset:8192\n\t"
      "v_mov_b32 v201, v238\n\t"
      "v_mfma_f32_16x16x32_bf16 v[12:15], a[0:3], v[96:99], v[12:15]\n\t"
      "ds_write_b64 v242, v[200:201] offset:256\n\t"
      "ds_read_b64_tr_b16 a[42:43], v230 offset:12288\n\t"
      "v_mfma_f32_16x16x32_bf16 v[16:19], a[0:3], v[100:103], v[16:19]\n\t"
      "ds_read_b64_tr_b16 a[44:45], v231 offset:8192\n\t"
      "v_mfma_f32_16x16x32_bf16 v[20:23], a[0:3], v[104:107], v[20:23]\n\t"
      "ds_read_b64_tr_b16 a[46:47], v231 offset:12288\n\t"
      "v_accvgpr_read_b32 v60, a224\n\t"
      "v_accvgpr_read_b32 v61, a225\n\t"
      "v_mfma_f32_16x16x32_bf16 v[36:39], a[4:7], v[96:99], v[36:39]\n\t"
      "v_accvgpr_read_b32 v62, a226\n\t"
      "v_mfma_f32_16x16x32_bf16 v[40:43], a[4:7], v[100:103], v[40:43]\n\t"
      "ds_read_b64_tr_b16 a[48:49], v232 offset:8192\n\t"
      "v_accvgpr_read_b32 v63, a227\n\t"
      "v_accvgpr_read_b32 v64, a228\n\t"
      "v_mfma_f32_16x16x32_bf16 v[44:47], a[4:7], v[104:107], v[44:47]\n\t"
      "ds_read_b64_tr_b16 a[50:51], v232 offset:12288\n\t"
      "v_accvgpr_read_b32 v65, a229\n\t"
      "v_mfma_f32_16x16x32_bf16 a[64:67], a[16:19], v[96:99], a[64:67]\n\t"
      "ds_read_b64_tr_b16 a[52:53], v233 offset:8192\n\t"
      "v_accvgpr_read_b32 v66, a230\n\t"
      "v_accvgpr_read_b32 v67, a231\n\t"
      "v_mfma_f32_16x16x32_bf16 a[68:71], a[16:19], v[100:103], a[68:71]\n\t"
      "ds_read_b64_tr_b16 a[54:55], v233 offset:12288\n\t"
      "v_accvgpr_read_b32 v68, a232\n\t"
      "v_mfma_f32_16x16x32_bf16 a[72:75], a[16:19], v[104:107], a[72:75]\n\t"
      "buffer_load_dwordx4 v209, s[44:47], s18 offen offset:1024 lds\n\t"
      "v_accvgpr_read_b32 v69, a233\n\t"
      "v_accvgpr_read_b32 v70, a234\n\t"
      "v_mfma_f32_16x16x32_bf16 a[76:79], a[20:23], v[96:99], a[76:79]\n\t"
      "ds_read_b64_tr_b16 a[56:57], v234 offset:8192\n\t"
      "v_accvgpr_read_b32 v71, a235\n\t"
      "v_mfma_f32_16x16x32_bf16 a[80:83], a[20:23], v[100:103], a[80:83]\n\t"
      "ds_read_b64_tr_b16 a[58:59], v234 offset:12288\n\t"
      "v_accvgpr_read_b32 v84, a236\n\t"
      "v_accvgpr_read_b32 v85, a237\n\t"
      "v_mfma_f32_16x16x32_bf16 a[84:87], a[20:23], v[104:107], a[84:87]\n\t"
      "ds_read_b64_tr_b16 a[60:61], v235 offset:8192\n\t"
      "v_accvgpr_read_b32 v86, a238\n\t"
      "v_mfma_f32_16x16x32_bf16 a[88:91], a[24:27], v[96:99], a[88:91]\n\t"
      "ds_read_b64_tr_b16 a[62:63], v235 offset:12288\n\t"
      "v_accvgpr_read_b32 v87, a239\n\t"
      "v_accvgpr_read_b32 v88, a240\n\t"
      "v_mfma_f32_16x16x32_bf16 a[92:95], a[24:27], v[100:103], a[92:95]\n\t"
      "v_accvgpr_read_b32 v89, a241\n\t"
      "v_mfma_f32_16x16x32_bf16 a[96:99], a[24:27], v[104:107], a[96:99]\n\t"
      "buffer_load_dwordx4 v210, s[44:47], s18 offen offset:2048 lds\n\t"
      "v_accvgpr_read_b32 v90, a242\n\t"
      "v_accvgpr_read_b32 v91, a243\n\t"
      "v_mfma_f32_16x16x32_bf16 a[100:103], a[28:31], v[96:99], a[100:103]\n\t"
      "v_accvgpr_read_b32 v92, a244\n\t"
      "v_mfma_f32_16x16x32_bf16 a[104:107], a[28:31], v[100:103], a[104:107]\n\t"
      "buffer_load_dwordx4 v211, s[44:47], s18 offen offset:3072 lds\n\t"
      "s_add_u32 s18, s18, 0x4000\n\t"
      "s_and_b32 s18, s18, 0x3ffff\n\t"
      "v_accvgpr_read_b32 v93, a245\n\t"
      "v_accvgpr_read_b32 v94, a246\n\t"
      "v_mfma_f32_16x16x32_bf16 a[108:111], a[28:31], v[104:107], a[108:111]\n\t"
      "v_accvgpr_read_b32 v95, a247\n\t"
      "s_waitcnt lgkmcnt(0)\n\t"
      "s_waitcnt vmcnt(12)\n\t"
      "s_barrier\n\t"
      "ds_read_b64 v[194:195], v243\n\t"
      "ds_read_b64 v[198:199], v243 offset:128\n\t"
      "ds_read_b64 v[202:203], v243 offset:256\n\t"
      "v_mfma_f32_16x16x32_bf16 a[64:67], a[48:51], v[120:123], a[64:67]\n\t"
      "ds_read_b64_tr_b16 a[0:1], v228 offset:16384\n\t"
      "v_accvgpr_read_b32 v48, a216\n\t"
      "v_mfma_f32_16x16x32_bf16 a[68:71], a[48:51], v[124:127], a[68:71]\n\t"
      "ds_read_b64_tr_b16 a[2:3], v228 offset:20480\n\t"
      "v_accvgpr_read_b32 v49, a217\n\t"
      "v_mfma_f32_16x16x32_bf16 a[72:75], a[48:51], v[128:131], a[72:75]\n\t"
      "ds_read_b64_tr_b16 a[4:5], v229 offset:16384\n\t"
      "v_accvgpr_read_b32 v50, a218\n\t"
      "v_mfma_f32_16x16x32_bf16 a[76:79], a[52:55], v[120:123], a[76:79]\n\t"
      "ds_read_b64_tr_b16 a[6:7], v229 offset:20480\n\t"
      "s_mov_b32 m0, s91\n\t"
      "v_accvgpr_read_b32 v51, a219\n\t"
      "v_mfma_f32_16x16x32_bf16 a[80:83], a[52:55], v[124:127], a[80:83]\n\t"
      "buffer_load_dwordx4 v208, s[44:47], s18 offen lds\n\t"
      "v_accvgpr_read_b32 v52, a220\n\t"
      "v_mfma_f32_16x16x32_bf16 a[84:87], a[52:55], v[128:131], a[84:87]\n\t"
      "ds_read_b64_tr_b16 a[8:9], v230 offset:16384\n\t"
      "v_accvgpr_read_b32 v53, a221\n\t"
      "v_mfma_f32_16x16x32_bf16 v[12:15], a[32:35], v[120:123], v[12:15]\n\t"
      "ds_read_b64_tr_b16 a[10:11], v230 offset:20480\n\t"
      "v_accvgpr_read_b32 v54, a222\n\t"
      "v_mfma_f32_16x16x32_bf16 v[16:19], a[32:35], v[124:127], v[16:19]\n\t"
      "ds_read_b64_tr_b16 a[12:13], v231 offset:16384\n\t"
      "v_accvgpr_read_b32 v55, a223\n\t"
      "v_mfma_f32_16x16x32_bf16 v[20:23], a[32:35], v[128:131], v[20:23]\n\t"
      "ds_read_b64_tr_b16 a[14:15], v231 offset:20480\n\t"
      "v_accvgpr_read_b32 v108, a64\n\t"
      "v_accvgpr_read_b32 v109, a65\n\t"
      "v_mfma_f32_16x16x32_bf16 v[36:39], a[36:39], v[120:123], v[36:39]\n\t"
      "v_accvgpr_read_b32 v110, a66\n\t"
      "v_mfma_f32_16x16x32_bf16 v[40:43], a[36:39], v[124:127], v[40:43]\n\t"
      "ds_read_b64_tr_b16 a[16:17], v232 offset:16384\n\t"
      "v_accvgpr_read_b32 v111, a67\n\t"
      "v_accvgpr_read_b32 v112, a68\n\t"
      "v_mfma_f32_16x16x32_bf16 v[44:47], a[36:39], v[128:131], v[44:47]\n\t"
      "ds_read_b64_tr_b16 a[18:19], v232 offset:20480\n\t"
      "v_accvgpr_read_b32 v113, a69\n\t"
      "v_mfma_f32_16x16x32_bf16 v[60:63], a[40:43], v[120:123], v[60:63]\n\t"
      "ds_read_b64_tr_b16 a[20:21], v233 offset:16384\n\t"
      "v_accvgpr_read_b32 v114, a70\n\t"
      "v_accvgpr_read_b32 v115, a71\n\t"
      "v_mfma_f32_16x16x32_bf16 v[64:67], a[40:43], v[124:127], v[64:67]\n\t"
      "ds_read_b64_tr_b16 a[22:23], v233 offset:20480\n\t"
      "v_accvgpr_read_b32 v116, a72\n\t"
      "v_mfma_f32_16x16x32_bf16 v[68:71], a[40:43], v[128:131], v[68:71]\n\t"
      "buffer_load_dwordx4 v209, s[44:47], s18 offen offset:1024 lds\n\t"
      "v_accvgpr_read_b32 v117, a73\n\t"
      "v_accvgpr_read_b32 v118, a74\n\t"
      "v_mfma_f32_16x16x32_bf16 v[84:87], a[44:47], v[120:123], v[84:87]\n\t"
      "ds_read_b64_tr_b16 a[24:25], v234 offset:16384\n\t"
      "v_accvgpr_read_b32 v119, a75\n\t"
      "v_mfma_f32_16x16x32_bf16 v[88:91], a[44:47], v[124:127], v[88:91]\n\t"
      "ds_read_b64_tr_b16 a[26:27], v234 offset:20480\n\t"
      "v_accvgpr_read_b32 v132, a76\n\t"
      "v_accvgpr_read_b32 v133, a77\n\t"
      "v_mfma_f32_16x16x32_bf16 v[92:95], a[44:47], v[128:131], v[92:95]\n\t"
      "ds_read_b64_tr_b16 a[28:29], v235 offset:16384\n\t"
      "v_accvgpr_read_b32 v134, a78\n\t"
      "v_mfma_f32_16x16x32_bf16 a[88:91], a[56:59], v[120:123], a[88:91]\n\t"
      "ds_read_b64_tr_b16 a[30:31], v235 offset:20480\n\t"
      "v_accvgpr_read_b32 v135, a79\n\t"
      "v_accvgpr_read_b32 v136, a80\n\t"
      "v_mfma_f32_16x16x32_bf16 a[92:95], a[56:59], v[124:127], a[92:95]\n\t"
      "v_accvgpr_read_b32 v137, a81\n\t"
      "v_mfma_f32_16x16x32_bf16 a[96:99], a[56:59], v[128:131], a[96:99]\n\t"
      "buffer_load_dwordx4 v210, s[44:47], s18 offen offset:2048 lds\n\t"
      "v_accvgpr_read_b32 v138, a82\n\t"
      "v_accvgpr_read_b32 v139, a83\n\t"
      "v_mfma_f32_16x16x32_bf16 a[100:103], a[60:63], v[120:123], a[100:103]\n\t"
      "v_accvgpr_read_b32 v140, a84\n\t"
      "v_mfma_f32_16x16x32_bf16 a[104:107], a[60:63], v[124:127], a[104:107]\n\t"
      "buffer_load_dwordx4 v211, s[44:47], s18 offen offset:3072 lds\n\t"
      "s_add_u32 s18, s18, 0x4000\n\t"
      "s_and_b32 s18, s18, 0x3ffff\n\t"
      "v_accvgpr_read_b32 v141, a85\n\t"
      "v_accvgpr_read_b32 v142, a86\n\t"
      "v_mfma_f32_16x16x32_bf16 a[108:111], a[60:63], v[128:131], a[108:111]\n\t"
      "s_add_u32 s32, s32, 0x40000\n\t"
      "s_addc_u32 s33, s33, 0\n\t"
      "s_sub_u32 s34, s34, 0x40000\n\t"
      "v_accvgpr_read_b32 v143, a87\n\t"
      "s_waitcnt lgkmcnt(0)\n\t"
      "s_waitcnt vmcnt(8)\n\t"
      "v_mfma_f32_16x16x32_bf16 a[88:91], a[24:27], v[144:147], a[88:91]\n\t"
      "ds_read_b64_tr_b16 a[32:33], v228 offset:24576\n\t"
      "v_maximum3_f32 v246, v236, v195, v195\n\t"
      "v_maximum3_f32 v247, v237, v199, v199\n\t"
      "v_mfma_f32_16x16x32_bf16 a[92:95], a[24:27], v[148:151], a[92:95]\n\t"
      "ds_read_b64_tr_b16 a[34:35], v228 offset:28672\n\t"
      "v_maximum3_f32 v248, v238, v203, v203\n\t"
      "v_mfma_f32_16x16x32_bf16 a[96:99], a[24:27], v[152:155], a[96:99]\n\t"
      "ds_read_b64_tr_b16 a[36:37], v229 offset:24576\n\t"
      "v_sub_f32 v72, v236, v246\n\t"
      "v_sub_f32 v74, v195, v246\n\t"
      "v_sub_f32 v76, v237, v247\n\t"
      "v_sub_f32 v78, v199, v247\n\t"
      "v_mfma_f32_16x16x32_bf16 a[100:103], a[28:31], v[144:147], a[100:103]\n\t"
      "ds_read_b64_tr_b16 a[38:39], v229 offset:28672\n\t"
      "s_mov_b32 m0, s92\n\t"
      "v_sub_f32 v80, v238, v248\n\t"
      "v_sub_f32 v82, v203, v248\n\t"
      "v_mfma_f32_16x16x32_bf16 a[104:107], a[28:31], v[148:151], a[104:107]\n\t"
      "buffer_load_dwordx4 v208, s[44:47], s18 offen lds\n\t"
      "v_mul_f32 v72, s26, v72\n\t"
      "v_mul_f32 v74, s26, v74\n\t"
      "v_mul_f32 v76, s26, v76\n\t"
      "v_mul_f32 v78, s26, v78\n\t"
      "v_mfma_f32_16x16x32_bf16 a[108:111], a[28:31], v[152:155], a[108:111]\n\t"
      "ds_read_b64_tr_b16 a[40:41], v230 offset:24576\n\t"
      "v_mul_f32 v80, s26, v80\n\t"
      "v_mul_f32 v82, s26, v82\n\t"
      "v_mfma_f32_16x16x32_bf16 v[12:15], a[0:3], v[144:147], v[12:15]\n\t"
      "ds_read_b64_tr_b16 a[42:43], v230 offset:28672\n\t"
      "v_exp_f32 v72, v72\n\t"
      "v_exp_f32 v76, v76\n\t"
      "v_mfma_f32_16x16x32_bf16 v[16:19], a[0:3], v[148:151], v[16:19]\n\t"
      "ds_read_b64_tr_b16 a[44:45], v231 offset:24576\n\t"
      "v_exp_f32 v80, v80\n\t"
      "v_mfma_f32_16x16x32_bf16 v[20:23], a[0:3], v[152:155], v[20:23]\n\t"
      "ds_read_b64_tr_b16 a[46:47], v231 offset:28672\n\t"
      "v_accvgpr_read_b32 v156, a88\n\t"
      "v_accvgpr_read_b32 v157, a89\n\t"
      "v_mfma_f32_16x16x32_bf16 v[36:39], a[4:7], v[144:147], v[36:39]\n\t"
      "buffer_load_dwordx4 v209, s[44:47], s18 offen offset:1024 lds\n\t"
      "v_accvgpr_read_b32 v158, a90\n\t"
      "v_mfma_f32_16x16x32_bf16 v[40:43], a[4:7], v[148:151], v[40:43]\n\t"
      "ds_read_b64_tr_b16 a[48:49], v232 offset:24576\n\t"
      "v_accvgpr_read_b32 v159, a91\n\t"
      "v_accvgpr_read_b32 v160, a92\n\t"
      "v_mfma_f32_16x16x32_bf16 v[44:47], a[4:7], v[152:155], v[44:47]\n\t"
      "ds_read_b64_tr_b16 a[50:51], v232 offset:28672\n\t"
      "v_accvgpr_read_b32 v161, a93\n\t"
      "v_mfma_f32_16x16x32_bf16 v[60:63], a[8:11], v[144:147], v[60:63]\n\t"
      "ds_read_b64_tr_b16 a[52:53], v233 offset:24576\n\t"
      "v_accvgpr_read_b32 v162, a94\n\t"
      "v_accvgpr_read_b32 v163, a95\n\t"
      "v_mfma_f32_16x16x32_bf16 v[64:67], a[8:11], v[148:151], v[64:67]\n\t"
      "ds_read_b64_tr_b16 a[54:55], v233 offset:28672\n\t"
      "v_accvgpr_read_b32 v164, a96\n\t"
      "v_mfma_f32_16x16x32_bf16 v[68:71], a[8:11], v[152:155], v[68:71]\n\t"
      "buffer_load_dwordx4 v210, s[44:47], s18 offen offset:2048 lds\n\t"
      "v_accvgpr_read_b32 v165, a97\n\t"
      "v_accvgpr_read_b32 v166, a98\n\t"
      "v_mfma_f32_16x16x32_bf16 v[84:87], a[12:15], v[144:147], v[84:87]\n\t"
      "ds_read_b64_tr_b16 a[56:57], v234 offset:24576\n\t"
      "v_accvgpr_read_b32 v167, a99\n\t"
      "v_mfma_f32_16x16x32_bf16 v[88:91], a[12:15], v[148:151], v[88:91]\n\t"
      "ds_read_b64_tr_b16 a[58:59], v234 offset:28672\n\t"
      "v_accvgpr_read_b32 v180, a100\n\t"
      "v_accvgpr_read_b32 v181, a101\n\t"
      "v_mfma_f32_16x16x32_bf16 v[92:95], a[12:15], v[152:155], v[92:95]\n\t"
      "ds_read_b64_tr_b16 a[60:61], v235 offset:24576\n\t"
      "v_accvgpr_read_b32 v182, a102\n\t"
      "v_mfma_f32_16x16x32_bf16 v[108:111], a[16:19], v[144:147], v[108:111]\n\t"
      "ds_read_b64_tr_b16 a[62:63], v235 offset:28672\n\t"
      "v_accvgpr_read_b32 v183, a103\n\t"
      "v_accvgpr_read_b32 v184, a104\n\t"
      "v_mfma_f32_16x16x32_bf16 v[112:115], a[16:19], v[148:151], v[112:115]\n\t"
      "buffer_load_dwordx4 v211, s[44:47], s18 offen offset:3072 lds\n\t"
      "s_add_u32 s18, s18, 0x4000\n\t"
      "s_and_b32 s18, s18, 0x3ffff\n\t"
      "s_mov_b32 m0, s93\n\t"
      "v_accvgpr_read_b32 v185, a105\n\t"
      "v_mfma_f32_16x16x32_bf16 v[116:119], a[16:19], v[152:155], v[116:119]\n\t"
      "buffer_load_dwordx4 v208, s[44:47], s18 offen lds\n\t"
      "v_accvgpr_read_b32 v186, a106\n\t"
      "v_accvgpr_read_b32 v187, a107\n\t"
      "v_mfma_f32_16x16x32_bf16 v[132:135], a[20:23], v[144:147], v[132:135]\n\t"
      "buffer_load_dwordx4 v209, s[44:47], s18 offen offset:1024 lds\n\t"
      "v_accvgpr_read_b32 v188, a108\n\t"
      "v_mfma_f32_16x16x32_bf16 v[136:139], a[20:23], v[148:151], v[136:139]\n\t"
      "buffer_load_dwordx4 v210, s[44:47], s18 offen offset:2048 lds\n\t"
      "v_accvgpr_read_b32 v189, a109\n\t"
      "v_accvgpr_read_b32 v190, a110\n\t"
      "v_mfma_f32_16x16x32_bf16 v[140:143], a[20:23], v[152:155], v[140:143]\n\t"
      "buffer_load_dwordx4 v211, s[44:47], s18 offen offset:3072 lds\n\t"
      "s_add_u32 s18, s18, 0x4000\n\t"
      "s_and_b32 s18, s18, 0x3ffff\n\t"
      "v_accvgpr_read_b32 v191, a111\n\t"
      "s_waitcnt lgkmcnt(0)\n\t"
      "s_mov_b32 m0, s94\n\t"
      "v_mfma_f32_16x16x32_bf16 v[108:111], a[48:51], v[168:171], v[108:111]\n\t"
      "buffer_load_dwordx4 v208, s[44:47], s18 offen lds\n\t"
      "v_exp_f32 v74, v74\n\t"
      "v_exp_f32 v78, v78\n\t"
      "v_mfma_f32_16x16x32_bf16 v[112:115], a[48:51], v[172:175], v[112:115]\n\t"
      "v_exp_f32 v82, v82\n\t"
      "v_mul_f32 v192, v72, v192\n\t"
      "v_mfma_f32_16x16x32_bf16 v[116:119], a[48:51], v[176:179], v[116:119]\n\t"
      "v_mul_f32 v196, v76, v196\n\t"
      "v_mfma_f32_16x16x32_bf16 v[132:135], a[52:55], v[168:171], v[132:135]\n\t"
      "buffer_load_dwordx4 v209, s[44:47], s18 offen offset:1024 lds\n\t"
      "v_mul_f32 v200, v80, v200\n\t"
      "v_fmac_f32 v192, v74, v194\n\t"
      "v_mfma_f32_16x16x32_bf16 v[136:139], a[52:55], v[172:175], v[136:139]\n\t"
      "v_fmac_f32 v196, v78, v198\n\t"
      "v_fmac_f32 v200, v82, v202\n\t"
      "v_mfma_f32_16x16x32_bf16 v[140:143], a[52:55], v[176:179], v[140:143]\n\t"
      "v_rcp_f32 v192, v192\n\t"
      "v_mfma_f32_16x16x32_bf16 v[156:159], a[56:59], v[168:171], v[156:159]\n\t"
      "buffer_load_dwordx4 v210, s[44:47], s18 offen offset:2048 lds\n\t"
      "v_rcp_f32 v196, v196\n\t"
      "v_rcp_f32 v200, v200\n\t"
      "v_mfma_f32_16x16x32_bf16 v[160:163], a[56:59], v[172:175], v[160:163]\n\t"
      "v_mul_f32 v72, v72, v192\n\t"
      "v_mul_f32 v74, v74, v192\n\t"
      "v_mul_f32 v76, v76, v196\n\t"
      "v_mul_f32 v78, v78, v196\n\t"
      "v_mfma_f32_16x16x32_bf16 v[164:167], a[56:59], v[176:179], v[164:167]\n\t"
      "v_mul_f32 v80, v80, v200\n\t"
      "v_mul_f32 v82, v82, v200\n\t"
      "v_mfma_f32_16x16x32_bf16 v[180:183], a[60:63], v[168:171], v[180:183]\n\t"
      "buffer_load_dwordx4 v211, s[44:47], s18 offen offset:3072 lds\n\t"
      "v_cvt_pk_bf16_f32 v108, v108, v109\n\t"
      "v_cvt_pk_bf16_f32 v109, v110, v111\n\t"
      "v_cvt_pk_bf16_f32 v110, v132, v133\n\t"
      "v_mfma_f32_16x16x32_bf16 v[184:187], a[60:63], v[172:175], v[184:187]\n\t"
      "v_cvt_pk_bf16_f32 v111, v134, v135\n\t"
      "v_mfma_f32_16x16x32_bf16 v[188:191], a[60:63], v[176:179], v[188:191]\n\t"
      "ds_write_b128 v240, v[108:111]\n\t"
      "s_add_u32 s18, s18, 0x4000\n\t"
      "s_and_b32 s18, s18, 0x3ffff\n\t"
      "s_mov_b32 m0, s95\n\t"
      "v_cvt_pk_bf16_f32 v112, v112, v113\n\t"
      "v_cvt_pk_bf16_f32 v113, v114, v115\n\t"
      "v_cvt_pk_bf16_f32 v114, v136, v137\n\t"
      "v_mfma_f32_16x16x32_bf16 v[12:15], a[32:35], v[168:171], v[12:15]\n\t"
      "buffer_load_dwordx4 v208, s[44:47], s18 offen lds\n\t"
      "v_cvt_pk_bf16_f32 v115, v138, v139\n\t"
      "v_mfma_f32_16x16x32_bf16 v[16:19], a[32:35], v[172:175], v[16:19]\n\t"
      "ds_write_b128 v240, v[112:115] offset:1024\n\t"
      "v_cvt_pk_bf16_f32 v116, v116, v117\n\t"
      "v_cvt_pk_bf16_f32 v117, v118, v119\n\t"
      "v_cvt_pk_bf16_f32 v118, v140, v141\n\t"
      "v_mfma_f32_16x16x32_bf16 v[20:23], a[32:35], v[176:179], v[20:23]\n\t"
      "v_cvt_pk_bf16_f32 v119, v142, v143\n\t"
      "v_mfma_f32_16x16x32_bf16 v[36:39], a[36:39], v[168:171], v[36:39]\n\t"
      "ds_write_b128 v240, v[116:119] offset:2048\n\t"
      "buffer_load_dwordx4 v209, s[44:47], s18 offen offset:1024 lds\n\t"
      "v_cvt_pk_bf16_f32 v156, v156, v157\n\t"
      "v_cvt_pk_bf16_f32 v157, v158, v159\n\t"
      "v_mfma_f32_16x16x32_bf16 v[40:43], a[36:39], v[172:175], v[40:43]\n\t"
      "v_cvt_pk_bf16_f32 v158, v180, v181\n\t"
      "v_cvt_pk_bf16_f32 v159, v182, v183\n\t"
      "v_mfma_f32_16x16x32_bf16 v[44:47], a[36:39], v[176:179], v[44:47]\n\t"
      "v_cvt_pk_bf16_f32 v160, v160, v161\n\t"
      "v_mfma_f32_16x16x32_bf16 v[60:63], a[40:43], v[168:171], v[60:63]\n\t"
      "ds_write_b128 v240, v[156:159] offset:3072\n\t"
      "buffer_load_dwordx4 v210, s[44:47], s18 offen offset:2048 lds\n\t"
      "v_cvt_pk_bf16_f32 v161, v162, v163\n\t"
      "v_cvt_pk_bf16_f32 v162, v184, v185\n\t"
      "v_mfma_f32_16x16x32_bf16 v[64:67], a[40:43], v[172:175], v[64:67]\n\t"
      "v_cvt_pk_bf16_f32 v163, v186, v187\n\t"
      "v_mfma_f32_16x16x32_bf16 v[68:71], a[40:43], v[176:179], v[68:71]\n\t"
      "ds_write_b128 v240, v[160:163] offset:4096\n\t"
      "v_cvt_pk_bf16_f32 v164, v164, v165\n\t"
      "v_cvt_pk_bf16_f32 v165, v166, v167\n\t"
      "v_mfma_f32_16x16x32_bf16 v[84:87], a[44:47], v[168:171], v[84:87]\n\t"
      "buffer_load_dwordx4 v211, s[44:47], s18 offen offset:3072 lds\n\t"
      "s_add_u32 s18, s18, 0x4000\n\t"
      "s_and_b32 s18, s18, 0x3ffff\n\t"
      "v_cvt_pk_bf16_f32 v166, v188, v189\n\t"
      "v_cvt_pk_bf16_f32 v167, v190, v191\n\t"
      "v_mfma_f32_16x16x32_bf16 v[88:91], a[44:47], v[172:175], v[88:91]\n\t"
      "v_mfma_f32_16x16x32_bf16 v[92:95], a[44:47], v[176:179], v[92:95]\n\t"
      "ds_write_b128 v240, v[164:167] offset:5120\n\t"
      "s_waitcnt lgkmcnt(0)\n\t"
      "s_barrier\n\t"
      "ds_read_b128 v[0:3], v241\n\t"
      "ds_read_b128 v[24:27], v241 offset:3072\n\t"
      "ds_read_b128 v[4:7], v241 offset:1024\n\t"
      "ds_read_b128 v[28:31], v241 offset:4096\n\t"
      "ds_read_b128 v[8:11], v241 offset:2048\n\t"
      "ds_read_b128 v[32:35], v241 offset:5120\n\t"
      "s_mov_b32 m0, s96\n\t"
      "s_nop 0\n\t"
      "buffer_load_dwordx4 v208, s[44:47], s18 offen lds\n\t"
      "s_waitcnt lgkmcnt(4)\n\t"
      "v_lshlrev_b32 v96, 16, v0\n\t"
      "v_and_b32 v97, 0xffff0000, v0\n\t"
      "v_lshlrev_b32 v98, 16, v1\n\t"
      "v_and_b32 v99, 0xffff0000, v1\n\t"
      "v_lshlrev_b32 v120, 16, v2\n\t"
      "v_and_b32 v121, 0xffff0000, v2\n\t"
      "v_lshlrev_b32 v122, 16, v3\n\t"
      "v_and_b32 v123, 0xffff0000, v3\n\t"
      "buffer_load_dwordx4 v209, s[44:47], s18 offen offset:1024 lds\n\t"
      "buffer_load_dwordx4 v210, s[44:47], s18 offen offset:2048 lds\n\t"
      "v_pk_mul_f32 v[12:13], v[12:13], v[72:73] op_sel_hi:[1,0]\n\t"
      "v_pk_fma_f32 v[12:13], v[96:97], v[74:75], v[12:13] op_sel_hi:[1,0,1]\n\t"
      "v_pk_mul_f32 v[14:15], v[14:15], v[72:73] op_sel_hi:[1,0]\n\t"
      "v_pk_fma_f32 v[14:15], v[98:99], v[74:75], v[14:15] op_sel_hi:[1,0,1]\n\t"
      "v_pk_mul_f32 v[36:37], v[36:37], v[72:73] op_sel_hi:[1,0]\n\t"
      "v_pk_fma_f32 v[36:37], v[120:121], v[74:75], v[36:37] op_sel_hi:[1,0,1]\n\t"
      "v_pk_mul_f32 v[38:39], v[38:39], v[72:73] op_sel_hi:[1,0]\n\t"
      "v_pk_fma_f32 v[38:39], v[122:123], v[74:75], v[38:39] op_sel_hi:[1,0,1]\n\t"
      "buffer_load_dwordx4 v211, s[44:47], s18 offen offset:3072 lds\n\t"
      "s_add_u32 s18, s18, 0x4000\n\t"
      "s_and_b32 s18, s18, 0x3ffff\n\t"
      "v_cvt_pk_bf16_f32 v0, v12, v13\n\t"
      "v_cvt_pk_bf16_f32 v1, v14, v15\n\t"
      "v_cvt_pk_bf16_f32 v2, v36, v37\n\t"
      "v_cvt_pk_bf16_f32 v3, v38, v39\n\t"
      "ds_write_b128 v241, v[0:3]\n\t"
      "v_lshlrev_b32 v96, 16, v24\n\t"
      "v_and_b32 v97, 0xffff0000, v24\n\t"
      "v_lshlrev_b32 v98, 16, v25\n\t"
      "v_and_b32 v99, 0xffff0000, v25\n\t"
      "v_lshlrev_b32 v120, 16, v26\n\t"
      "v_and_b32 v121, 0xffff0000, v26\n\t"
      "v_lshlrev_b32 v122, 16, v27\n\t"
      "v_and_b32 v123, 0xffff0000, v27\n\t"
      "s_mov_b32 m0, s97\n\t"
      "s_nop 0\n\t"
      "buffer_load_dwordx4 v208, s[44:47], s18 offen lds\n\t"
      "v_pk_mul_f32 v[60:61], v[60:61], v[72:73] op_sel_hi:[1,0]\n\t"
      "v_pk_fma_f32 v[60:61], v[96:97], v[74:75], v[60:61] op_sel_hi:[1,0,1]\n\t"
      "v_pk_mul_f32 v[62:63], v[62:63], v[72:73] op_sel_hi:[1,0]\n\t"
      "v_pk_fma_f32 v[62:63], v[98:99], v[74:75], v[62:63] op_sel_hi:[1,0,1]\n\t"
      "v_pk_mul_f32 v[84:85], v[84:85], v[72:73] op_sel_hi:[1,0]\n\t"
      "v_pk_fma_f32 v[84:85], v[120:121], v[74:75], v[84:85] op_sel_hi:[1,0,1]\n\t"
      "v_pk_mul_f32 v[86:87], v[86:87], v[72:73] op_sel_hi:[1,0]\n\t"
      "v_pk_fma_f32 v[86:87], v[122:123], v[74:75], v[86:87] op_sel_hi:[1,0,1]\n\t"
      "buffer_load_dwordx4 v209, s[44:47], s18 offen offset:1024 lds\n\t"
      "buffer_load_dwordx4 v210, s[44:47], s18 offen offset:2048 lds\n\t"
      "v_cvt_pk_bf16_f32 v24, v60, v61\n\t"
      "v_cvt_pk_bf16_f32 v25, v62, v63\n\t"
      "v_cvt_pk_bf16_f32 v26, v84, v85\n\t"
      "v_cvt_pk_bf16_f32 v27, v86, v87\n\t"
      "ds_write_b128 v241, v[24:27] offset:3072\n\t"
      "s_waitcnt lgkmcnt(4)\n\t"
      "v_lshlrev_b32 v100, 16, v4\n\t"
      "v_and_b32 v101, 0xffff0000, v4\n\t"
      "v_lshlrev_b32 v102, 16, v5\n\t"
      "v_and_b32 v103, 0xffff0000, v5\n\t"
      "v_lshlrev_b32 v124, 16, v6\n\t"
      "v_and_b32 v125, 0xffff0000, v6\n\t"
      "v_lshlrev_b32 v126, 16, v7\n\t"
      "v_and_b32 v127, 0xffff0000, v7\n\t"
      "buffer_load_dwordx4 v211, s[44:47], s18 offen offset:3072 lds\n\t"
      "s_add_u32 s18, s18, 0x4000\n\t"
      "s_and_b32 s18, s18, 0x3ffff\n\t"
      "v_pk_mul_f32 v[16:17], v[16:17], v[76:77] op_sel_hi:[1,0]\n\t"
      "v_pk_fma_f32 v[16:17], v[100:101], v[78:79], v[16:17] op_sel_hi:[1,0,1]\n\t"
      "v_pk_mul_f32 v[18:19], v[18:19], v[76:77] op_sel_hi:[1,0]\n\t"
      "v_pk_fma_f32 v[18:19], v[102:103], v[78:79], v[18:19] op_sel_hi:[1,0,1]\n\t"
      "v_pk_mul_f32 v[40:41], v[40:41], v[76:77] op_sel_hi:[1,0]\n\t"
      "v_pk_fma_f32 v[40:41], v[124:125], v[78:79], v[40:41] op_sel_hi:[1,0,1]\n\t"
      "v_pk_mul_f32 v[42:43], v[42:43], v[76:77] op_sel_hi:[1,0]\n\t"
      "v_pk_fma_f32 v[42:43], v[126:127], v[78:79], v[42:43] op_sel_hi:[1,0,1]\n\t"
      "v_cvt_pk_bf16_f32 v4, v16, v17\n\t"
      "v_cvt_pk_bf16_f32 v5, v18, v19\n\t"
      "v_cvt_pk_bf16_f32 v6, v40, v41\n\t"
      "v_cvt_pk_bf16_f32 v7, v42, v43\n\t"
      "ds_write_b128 v241, v[4:7] offset:1024\n\t"
      "v_lshlrev_b32 v100, 16, v28\n\t"
      "v_and_b32 v101, 0xffff0000, v28\n\t"
      "v_lshlrev_b32 v102, 16, v29\n\t"
      "v_and_b32 v103, 0xffff0000, v29\n\t"
      "v_lshlrev_b32 v124, 16, v30\n\t"
      "v_and_b32 v125, 0xffff0000, v30\n\t"
      "v_lshlrev_b32 v126, 16, v31\n\t"
      "v_and_b32 v127, 0xffff0000, v31\n\t"
      "v_pk_mul_f32 v[64:65], v[64:65], v[76:77] op_sel_hi:[1,0]\n\t"
      "v_pk_fma_f32 v[64:65], v[100:101], v[78:79], v[64:65] op_sel_hi:[1,0,1]\n\t"
      "v_pk_mul_f32 v[66:67], v[66:67], v[76:77] op_sel_hi:[1,0]\n\t"
      "v_pk_fma_f32 v[66:67], v[102:103], v[78:79], v[66:67] op_sel_hi:[1,0,1]\n\t"
      "v_pk_mul_f32 v[88:89], v[88:89], v[76:77] op_sel_hi:[1,0]\n\t"
      "v_pk_fma_f32 v[88:89], v[124:125], v[78:79], v[88:89] op_sel_hi:[1,0,1]\n\t"
      "v_pk_mul_f32 v[90:91], v[90:91], v[76:77] op_sel_hi:[1,0]\n\t"
      "v_pk_fma_f32 v[90:91], v[126:127], v[78:79], v[90:91] op_sel_hi:[1,0,1]\n\t"
      "v_cvt_pk_bf16_f32 v28, v64, v65\n\t"
      "v_cvt_pk_bf16_f32 v29, v66, v67\n\t"
      "v_cvt_pk_bf16_f32 v30, v88, v89\n\t"
      "v_cvt_pk_bf16_f32 v31, v90, v91\n\t"
      "ds_write_b128 v241, v[28:31] offset:4096\n\t"
      "s_waitcnt lgkmcnt(4)\n\t"
      "s_waitcnt vmcnt(24)\n\t"
      "ds_read_b64_tr_b16 a[0:1], v220\n\t"
      "ds_read_b64_tr_b16 a[2:3], v220 offset:4096\n\t"
      "ds_read_b64_tr_b16 a[4:5], v221\n\t"
      "ds_read_b64_tr_b16 a[6:7], v221 offset:4096\n\t"
      "ds_read_b64_tr_b16 a[8:9], v222\n\t"
      "ds_read_b64_tr_b16 a[10:11], v222 offset:4096\n\t"
      "ds_read_b64_tr_b16 a[12:13], v223\n\t"
      "ds_read_b64_tr_b16 a[14:15], v223 offset:4096\n\t"
      "ds_read_b64_tr_b16 a[16:17], v224\n\t"
      "ds_read_b64_tr_b16 a[18:19], v224 offset:4096\n\t"
      "ds_read_b64_tr_b16 a[20:21], v225\n\t"
      "ds_read_b64_tr_b16 a[22:23], v225 offset:4096\n\t"
      "ds_read_b64_tr_b16 a[24:25], v226\n\t"
      "ds_read_b64_tr_b16 a[26:27], v226 offset:4096\n\t"
      "ds_read_b64_tr_b16 a[28:29], v227\n\t"
      "ds_read_b64_tr_b16 a[30:31], v227 offset:4096\n\t"
      "v_lshlrev_b32 v104, 16, v8\n\t"
      "v_and_b32 v105, 0xffff0000, v8\n\t"
      "v_lshlrev_b32 v106, 16, v9\n\t"
      "v_and_b32 v107, 0xffff0000, v9\n\t"
      "v_lshlrev_b32 v128, 16, v10\n\t"
      "v_and_b32 v129, 0xffff0000, v10\n\t"
      "v_lshlrev_b32 v130, 16, v11\n\t"
      "v_and_b32 v131, 0xffff0000, v11\n\t"
      "v_pk_mul_f32 v[20:21], v[20:21], v[80:81] op_sel_hi:[1,0]\n\t"
      "v_pk_fma_f32 v[20:21], v[104:105], v[82:83], v[20:21] op_sel_hi:[1,0,1]\n\t"
      "v_pk_mul_f32 v[22:23], v[22:23], v[80:81] op_sel_hi:[1,0]\n\t"
      "v_pk_fma_f32 v[22:23], v[106:107], v[82:83], v[22:23] op_sel_hi:[1,0,1]\n\t"
      "v_pk_mul_f32 v[44:45], v[44:45], v[80:81] op_sel_hi:[1,0]\n\t"
      "v_pk_fma_f32 v[44:45], v[128:129], v[82:83], v[44:45] op_sel_hi:[1,0,1]\n\t"
      "v_pk_mul_f32 v[46:47], v[46:47], v[80:81] op_sel_hi:[1,0]\n\t"
      "v_pk_fma_f32 v[46:47], v[130:131], v[82:83], v[46:47] op_sel_hi:[1,0,1]\n\t"
      "v_cvt_pk_bf16_f32 v8, v20, v21\n\t"
      "v_cvt_pk_bf16_f32 v9, v22, v23\n\t"
      "v_cvt_pk_bf16_f32 v10, v44, v45\n\t"
      "v_cvt_pk_bf16_f32 v11, v46, v47\n\t"
      "ds_write_b128 v241, v[8:11] offset:2048\n\t"
      "v_lshlrev_b32 v104, 16, v32\n\t"
      "v_and_b32 v105, 0xffff0000, v32\n\t"
      "v_lshlrev_b32 v106, 16, v33\n\t"
      "v_and_b32 v107, 0xffff0000, v33\n\t"
      "v_lshlrev_b32 v128, 16, v34\n\t"
      "v_and_b32 v129, 0xffff0000, v34\n\t"
      "v_lshlrev_b32 v130, 16, v35\n\t"
      "v_and_b32 v131, 0xffff0000, v35\n\t"
      "v_pk_mul_f32 v[68:69], v[68:69], v[80:81] op_sel_hi:[1,0]\n\t"
      "v_pk_fma_f32 v[68:69], v[104:105], v[82:83], v[68:69] op_sel_hi:[1,0,1]\n\t"
      "v_pk_mul_f32 v[70:71], v[70:71], v[80:81] op_sel_hi:[1,0]\n\t"
      "v_pk_fma_f32 v[70:71], v[106:107], v[82:83], v[70:71] op_sel_hi:[1,0,1]\n\t"
      "v_pk_mul_f32 v[92:93], v[92:93], v[80:81] op_sel_hi:[1,0]\n\t"
      "v_pk_fma_f32 v[92:93], v[128:129], v[82:83], v[92:93] op_sel_hi:[1,0,1]\n\t"
      "v_pk_mul_f32 v[94:95], v[94:95], v[80:81] op_sel_hi:[1,0]\n\t"
      "v_pk_fma_f32 v[94:95], v[130:131], v[82:83], v[94:95] op_sel_hi:[1,0,1]\n\t"
      "v_cvt_pk_bf16_f32 v32, v68, v69\n\t"
      "v_cvt_pk_bf16_f32 v33, v70, v71\n\t"
      "v_cvt_pk_bf16_f32 v34, v92, v93\n\t"
      "v_cvt_pk_bf16_f32 v35, v94, v95\n\t"
      "ds_write_b128 v241, v[32:35] offset:5120\n\t"
      "s_waitcnt lgkmcnt(0)\n\t"
      "s_barrier\n\t"
      "s_cmp_lg_u32 s13, 0\n\t"
      "s_cbranch_scc1 L_Blast_%=\n\t"
      "ds_read_b128 v[96:99], v48\n\t"
      "ds_read_b128 v[100:103], v48 offset:1024\n\t"
      "ds_read_b128 v[104:107], v48 offset:2048\n\t"
      "ds_read_b128 v[108:111], v49\n\t"
      "ds_read_b128 v[112:115], v49 offset:1024\n\t"
      "ds_read_b128 v[116:119], v49 offset:2048\n\t"
      "ds_read_b128 v[120:123], v50\n\t"
      "ds_read_b128 v[124:127], v50 offset:1024\n\t"
      "ds_read_b128 v[128:131], v50 offset:2048\n\t"
      "ds_read_b128 v[132:135], v51\n\t"
      "ds_read_b128 v[136:139], v51 offset:1024\n\t"
      "ds_read_b128 v[140:143], v51 offset:2048\n\t"
      "ds_read_b128 v[144:147], v52\n\t"
      "ds_read_b128 v[148:151], v52 offset:1024\n\t"
      "ds_read_b128 v[152:155], v52 offset:2048\n\t"
      "ds_read_b128 v[156:159], v53\n\t"
      "ds_read_b128 v[160:163], v53 offset:1024\n\t"
      "ds_read_b128 v[164:167], v53 offset:2048\n\t"
      "ds_read_b128 v[168:171], v54\n\t"
      "ds_read_b128 v[172:175], v54 offset:1024\n\t"
      "ds_read_b128 v[176:179], v54 offset:2048\n\t"
      "ds_read_b128 v[180:183], v55\n\t"
      "ds_read_b128 v[184:187], v55 offset:1024\n\t"
      "ds_read_b128 v[188:191], v55 offset:2048\n\t"
      "s_waitcnt lgkmcnt(15)\n\t"
      "s_waitcnt vmcnt(16)\n\t"
      "v_mfma_f32_16x16x32_bf16 v[0:3], a[0:3], v[96:99], 0\n\t"
      "ds_read_b64_tr_b16 a[32:33], v220 offset:8192\n\t"
      "v_mfma_f32_16x16x32_bf16 v[4:7], a[0:3], v[100:103], 0\n\t"
      "ds_read_b64_tr_b16 a[34:35], v220 offset:12288\n\t"
      "v_mfma_f32_16x16x32_bf16 v[8:11], a[0:3], v[104:107], 0\n\t"
      "ds_read_b64_tr_b16 a[36:37], v221 offset:8192\n\t"
      "v_mfma_f32_16x16x32_bf16 v[12:15], a[4:7], v[96:99], 0\n\t"
      "ds_read_b64_tr_b16 a[38:39], v221 offset:12288\n\t"
      "s_mov_b32 m0, s90\n\t"
      "v_mfma_f32_16x16x32_bf16 v[16:19], a[4:7], v[100:103], 0\n\t"
      "buffer_load_dwordx4 v208, s[44:47], s18 offen lds\n\t"
      "v_mfma_f32_16x16x32_bf16 v[20:23], a[4:7], v[104:107], 0\n\t"
      "ds_read_b64_tr_b16 a[40:41], v222 offset:8192\n\t"
      "v_mfma_f32_16x16x32_bf16 v[24:27], a[8:11], v[96:99], 0\n\t"
      "ds_read_b64_tr_b16 a[42:43], v222 offset:12288\n\t"
      "v_mfma_f32_16x16x32_bf16 v[28:31], a[8:11], v[100:103], 0\n\t"
      "ds_read_b64_tr_b16 a[44:45], v223 offset:8192\n\t"
      "v_mfma_f32_16x16x32_bf16 v[32:35], a[8:11], v[104:107], 0\n\t"
      "buffer_load_dwordx4 v209, s[44:47], s18 offen offset:1024 lds\n\t"
      "v_mfma_f32_16x16x32_bf16 v[36:39], a[12:15], v[96:99], 0\n\t"
      "ds_read_b64_tr_b16 a[46:47], v223 offset:12288\n\t"
      "v_mfma_f32_16x16x32_bf16 v[40:43], a[12:15], v[100:103], 0\n\t"
      "ds_read_b64_tr_b16 a[48:49], v224 offset:8192\n\t"
      "v_mfma_f32_16x16x32_bf16 v[44:47], a[12:15], v[104:107], 0\n\t"
      "ds_read_b64_tr_b16 a[50:51], v224 offset:12288\n\t"
      "v_mfma_f32_16x16x32_bf16 v[48:51], a[16:19], v[96:99], 0\n\t"
      "buffer_load_dwordx4 v210, s[44:47], s18 offen offset:2048 lds\n\t"
      "v_mfma_f32_16x16x32_bf16 v[52:55], a[16:19], v[100:103], 0\n\t"
      "ds_read_b64_tr_b16 a[52:53], v225 offset:8192\n\t"
      "v_mfma_f32_16x16x32_bf16 v[56:59], a[16:19], v[104:107], 0\n\t"
      "ds_read_b64_tr_b16 a[54:55], v225 offset:12288\n\t"
      "v_mfma_f32_16x16x32_bf16 v[60:63], a[20:23], v[96:99], 0\n\t"
      "ds_read_b64_tr_b16 a[56:57], v226 offset:8192\n\t"
      "v_mfma_f32_16x16x32_bf16 v[64:67], a[20:23], v[100:103], 0\n\t"
      "buffer_load_dwordx4 v211, s[44:47], s18 offen offset:3072 lds\n\t"
      "v_mfma_f32_16x16x32_bf16 v[68:71], a[20:23], v[104:107], 0\n\t"
      "ds_read_b64_tr_b16 a[58:59], v226 offset:12288\n\t"
      "v_mfma_f32_16x16x32_bf16 v[72:75], a[24:27], v[96:99], 0\n\t"
      "ds_read_b64_tr_b16 a[60:61], v227 offset:8192\n\t"
      "v_mfma_f32_16x16x32_bf16 v[76:79], a[24:27], v[100:103], 0\n\t"
      "ds_read_b64_tr_b16 a[62:63], v227 offset:12288\n\t"
      "s_add_u32 s18, s18, 0x4000\n\t"
      "s_and_b32 s18, s18, 0x3ffff\n\t"
      "s_mov_b32 m0, s91\n\t"
      "v_mfma_f32_16x16x32_bf16 v[80:83], a[24:27], v[104:107], 0\n\t"
      "buffer_load_dwordx4 v208, s[44:47], s18 offen lds\n\t"
      "v_mfma_f32_16x16x32_bf16 v[84:87], a[28:31], v[96:99], 0\n\t"
      "buffer_load_dwordx4 v209, s[44:47], s18 offen offset:1024 lds\n\t"
      "v_mfma_f32_16x16x32_bf16 v[88:91], a[28:31], v[100:103], 0\n\t"
      "buffer_load_dwordx4 v210, s[44:47], s18 offen offset:2048 lds\n\t"
      "v_mfma_f32_16x16x32_bf16 v[92:95], a[28:31], v[104:107], 0\n\t"
      "buffer_load_dwordx4 v211, s[44:47], s18 offen offset:3072 lds\n\t"
      "s_add_u32 s18, s18, 0x4000\n\t"
      "s_and_b32 s18, s18, 0x3ffff\n\t"
      "s_waitcnt lgkmcnt(0)\n\t"
      "s_waitcnt vmcnt(16)\n\t"
      "v_mfma_f32_16x16x32_bf16 v[0:3], a[32:35], v[108:111], v[0:3]\n\t"
      "ds_read_b64_tr_b16 a[0:1], v220 offset:16384\n\t"
      "v_accvgpr_mov_b32 a64, a112\n\t"
      "v_mfma_f32_16x16x32_bf16 v[4:7], a[32:35], v[112:115], v[4:7]\n\t"
      "ds_read_b64_tr_b16 a[2:3], v220 offset:20480\n\t"
      "v_accvgpr_mov_b32 a65, a113\n\t"
      "v_mfma_f32_16x16x32_bf16 v[8:11], a[32:35], v[116:119], v[8:11]\n\t"
      "ds_read_b64_tr_b16 a[4:5], v221 offset:16384\n\t"
      "v_accvgpr_mov_b32 a66, a114\n\t"
      "v_mfma_f32_16x16x32_bf16 v[12:15], a[36:39], v[108:111], v[12:15]\n\t"
      "ds_read_b64_tr_b16 a[6:7], v221 offset:20480\n\t"
      "s_mov_b32 m0, s92\n\t"
      "v_accvgpr_mov_b32 a67, a115\n\t"
      "v_mfma_f32_16x16x32_bf16 v[16:19], a[36:39], v[112:115], v[16:19]\n\t"
      "buffer_load_dwordx4 v208, s[44:47], s18 offen lds\n\t"
      "v_accvgpr_mov_b32 a68, a116\n\t"
      "v_mfma_f32_16x16x32_bf16 v[20:23], a[36:39], v[116:119], v[20:23]\n\t"
      "ds_read_b64_tr_b16 a[8:9], v222 offset:16384\n\t"
      "v_accvgpr_mov_b32 a69, a117\n\t"
      "v_mfma_f32_16x16x32_bf16 v[24:27], a[40:43], v[108:111], v[24:27]\n\t"
      "ds_read_b64_tr_b16 a[10:11], v222 offset:20480\n\t"
      "v_accvgpr_mov_b32 a70, a118\n\t"
      "v_mfma_f32_16x16x32_bf16 v[28:31], a[40:43], v[112:115], v[28:31]\n\t"
      "ds_read_b64_tr_b16 a[12:13], v223 offset:16384\n\t"
      "v_accvgpr_mov_b32 a71, a119\n\t"
      "v_mfma_f32_16x16x32_bf16 v[32:35], a[40:43], v[116:119], v[32:35]\n\t"
      "buffer_load_dwordx4 v209, s[44:47], s18 offen offset:1024 lds\n\t"
      "v_accvgpr_mov_b32 a72, a120\n\t"
      "v_mfma_f32_16x16x32_bf16 v[36:39], a[44:47], v[108:111], v[36:39]\n\t"
      "ds_read_b64_tr_b16 a[14:15], v223 offset:20480\n\t"
      "v_accvgpr_mov_b32 a73, a121\n\t"
      "v_mfma_f32_16x16x32_bf16 v[40:43], a[44:47], v[112:115], v[40:43]\n\t"
      "ds_read_b64_tr_b16 a[16:17], v224 offset:16384\n\t"
      "v_accvgpr_mov_b32 a74, a122\n\t"
      "v_mfma_f32_16x16x32_bf16 v[44:47], a[44:47], v[116:119], v[44:47]\n\t"
      "ds_read_b64_tr_b16 a[18:19], v224 offset:20480\n\t"
      "v_accvgpr_mov_b32 a75, a123\n\t"
      "v_mfma_f32_16x16x32_bf16 v[48:51], a[48:51], v[108:111], v[48:51]\n\t"
      "buffer_load_dwordx4 v210, s[44:47], s18 offen offset:2048 lds\n\t"
      "v_accvgpr_mov_b32 a76, a124\n\t"
      "v_mfma_f32_16x16x32_bf16 v[52:55], a[48:51], v[112:115], v[52:55]\n\t"
      "ds_read_b64_tr_b16 a[20:21], v225 offset:16384\n\t"
      "v_accvgpr_mov_b32 a77, a125\n\t"
      "v_mfma_f32_16x16x32_bf16 v[56:59], a[48:51], v[116:119], v[56:59]\n\t"
      "ds_read_b64_tr_b16 a[22:23], v225 offset:20480\n\t"
      "v_accvgpr_mov_b32 a78, a126\n\t"
      "v_mfma_f32_16x16x32_bf16 v[60:63], a[52:55], v[108:111], v[60:63]\n\t"
      "ds_read_b64_tr_b16 a[24:25], v226 offset:16384\n\t"
      "v_accvgpr_mov_b32 a79, a127\n\t"
      "v_mfma_f32_16x16x32_bf16 v[64:67], a[52:55], v[112:115], v[64:67]\n\t"
      "buffer_load_dwordx4 v211, s[44:47], s18 offen offset:3072 lds\n\t"
      "v_accvgpr_mov_b32 a80, a128\n\t"
      "v_mfma_f32_16x16x32_bf16 v[68:71], a[52:55], v[116:119], v[68:71]\n\t"
      "ds_read_b64_tr_b16 a[26:27], v226 offset:20480\n\t"
      "v_accvgpr_mov_b32 a81, a129\n\t"
      "v_mfma_f32_16x16x32_bf16 v[72:75], a[56:59], v[108:111], v[72:75]\n\t"
      "ds_read_b64_tr_b16 a[28:29], v227 offset:16384\n\t"
      "v_accvgpr_mov_b32 a82, a130\n\t"
      "v_mfma_f32_16x16x32_bf16 v[76:79], a[56:59], v[112:115], v[76:79]\n\t"
      "ds_read_b64_tr_b16 a[30:31], v227 offset:20480\n\t"
      "s_add_u32 s18, s18, 0x4000\n\t"
      "s_and_b32 s18, s18, 0x3ffff\n\t"
      "s_mov_b32 m0, s93\n\t"
      "v_accvgpr_mov_b32 a83, a131\n\t"
      "v_mfma_f32_16x16x32_bf16 v[80:83], a[56:59], v[116:119], v[80:83]\n\t"
      "buffer_load_dwordx4 v208, s[44:47], s18 offen lds\n\t"
      "v_accvgpr_mov_b32 a84, a132\n\t"
      "v_mfma_f32_16x16x32_bf16 v[84:87], a[60:63], v[108:111], v[84:87]\n\t"
      "buffer_load_dwordx4 v209, s[44:47], s18 offen offset:1024 lds\n\t"
      "v_accvgpr_mov_b32 a85, a133\n\t"
      "v_mfma_f32_16x16x32_bf16 v[88:91], a[60:63], v[112:115], v[88:91]\n\t"
      "buffer_load_dwordx4 v210, s[44:47], s18 offen offset:2048 lds\n\t"
      "v_accvgpr_mov_b32 a86, a134\n\t"
      "v_mfma_f32_16x16x32_bf16 v[92:95], a[60:63], v[116:119], v[92:95]\n\t"
      "buffer_load_dwordx4 v211, s[44:47], s18 offen offset:3072 lds\n\t"
      "s_add_u32 s18, s18, 0x4000\n\t"
      "s_and_b32 s18, s18, 0x3ffff\n\t"
      "v_accvgpr_mov_b32 a87, a135\n\t"
      "s_waitcnt lgkmcnt(0)\n\t"
      "s_waitcnt vmcnt(16)\n\t"
      "v_mfma_f32_16x16x32_bf16 v[0:3], a[0:3], v[120:123], v[0:3]\n\t"
      "ds_read_b64_tr_b16 a[32:33], v220 offset:24576\n\t"
      "v_accvgpr_mov_b32 a88, a136\n\t"
      "v_mfma_f32_16x16x32_bf16 v[4:7], a[0:3], v[124:127], v[4:7]\n\t"
      "ds_read_b64_tr_b16 a[34:35], v220 offset:28672\n\t"
      "v_accvgpr_mov_b32 a89, a137\n\t"
      "v_mfma_f32_16x16x32_bf16 v[8:11], a[0:3], v[128:131], v[8:11]\n\t"
      "ds_read_b64_tr_b16 a[36:37], v221 offset:24576\n\t"
      "v_accvgpr_mov_b32 a90, a138\n\t"
      "v_mfma_f32_16x16x32_bf16 v[12:15], a[4:7], v[120:123], v[12:15]\n\t"
      "ds_read_b64_tr_b16 a[38:39], v221 offset:28672\n\t"
      "s_mov_b32 m0, s94\n\t"
      "v_accvgpr_mov_b32 a91, a139\n\t"
      "v_mfma_f32_16x16x32_bf16 v[16:19], a[4:7], v[124:127], v[16:19]\n\t"
      "buffer_load_dwordx4 v208, s[44:47], s18 offen lds\n\t"
      "v_accvgpr_mov_b32 a92, a140\n\t"
      "v_mfma_f32_16x16x32_bf16 v[20:23], a[4:7], v[128:131], v[20:23]\n\t"
      "ds_read_b64_tr_b16 a[40:41], v222 offset:24576\n\t"
      "v_accvgpr_mov_b32 a93, a141\n\t"
      "v_mfma_f32_16x16x32_bf16 v[24:27], a[8:11], v[120:123], v[24:27]\n\t"
      "ds_read_b64_tr_b16 a[42:43], v222 offset:28672\n\t"
      "v_accvgpr_mov_b32 a94, a142\n\t"
      "v_mfma_f32_16x16x32_bf16 v[28:31], a[8:11], v[124:127], v[28:31]\n\t"
      "ds_read_b64_tr_b16 a[44:45], v223 offset:24576\n\t"
      "v_accvgpr_mov_b32 a95, a143\n\t"
      "v_mfma_f32_16x16x32_bf16 v[32:35], a[8:11], v[128:131], v[32:35]\n\t"
      "buffer_load_dwordx4 v209, s[44:47], s18 offen offset:1024 lds\n\t"
      "v_accvgpr_mov_b32 a96, a144\n\t"
      "v_mfma_f32_16x16x32_bf16 v[36:39], a[12:15], v[120:123], v[36:39]\n\t"
      "ds_read_b64_tr_b16 a[46:47], v223 offset:28672\n\t"
      "v_accvgpr_mov_b32 a97, a145\n\t"
      "v_mfma_f32_16x16x32_bf16 v[40:43], a[12:15], v[124:127], v[40:43]\n\t"
      "ds_read_b64_tr_b16 a[48:49], v224 offset:24576\n\t"
      "v_accvgpr_mov_b32 a98, a146\n\t"
      "v_mfma_f32_16x16x32_bf16 v[44:47], a[12:15], v[128:131], v[44:47]\n\t"
      "ds_read_b64_tr_b16 a[50:51], v224 offset:28672\n\t"
      "v_accvgpr_mov_b32 a99, a147\n\t"
      "v_mfma_f32_16x16x32_bf16 v[48:51], a[16:19], v[120:123], v[48:51]\n\t"
      "buffer_load_dwordx4 v210, s[44:47], s18 offen offset:2048 lds\n\t"
      "v_accvgpr_mov_b32 a100, a148\n\t"
      "v_mfma_f32_16x16x32_bf16 v[52:55], a[16:19], v[124:127], v[52:55]\n\t"
      "ds_read_b64_tr_b16 a[52:53], v225 offset:24576\n\t"
      "v_accvgpr_mov_b32 a101, a149\n\t"
      "v_mfma_f32_16x16x32_bf16 v[56:59], a[16:19], v[128:131], v[56:59]\n\t"
      "ds_read_b64_tr_b16 a[54:55], v225 offset:28672\n\t"
      "v_accvgpr_mov_b32 a102, a150\n\t"
      "v_mfma_f32_16x16x32_bf16 v[60:63], a[20:23], v[120:123], v[60:63]\n\t"
      "ds_read_b64_tr_b16 a[56:57], v226 offset:24576\n\t"
      "v_accvgpr_mov_b32 a103, a151\n\t"
      "v_mfma_f32_16x16x32_bf16 v[64:67], a[20:23], v[124:127], v[64:67]\n\t"
      "buffer_load_dwordx4 v211, s[44:47], s18 offen offset:3072 lds\n\t"
      "v_accvgpr_mov_b32 a104, a152\n\t"
      "v_mfma_f32_16x16x32_bf16 v[68:71], a[20:23], v[128:131], v[68:71]\n\t"
      "ds_read_b64_tr_b16 a[58:59], v226 offset:28672\n\t"
      "v_accvgpr_mov_b32 a105, a153\n\t"
      "v_mfma_f32_16x16x32_bf16 v[72:75], a[24:27], v[120:123], v[72:75]\n\t"
      "ds_read_b64_tr_b16 a[60:61], v227 offset:24576\n\t"
      "v_accvgpr_mov_b32 a106, a154\n\t"
      "v_mfma_f32_16x16x32_bf16 v[76:79], a[24:27], v[124:127], v[76:79]\n\t"
      "ds_read_b64_tr_b16 a[62:63], v227 offset:28672\n\t"
      "s_add_u32 s18, s18, 0x4000\n\t"
      "s_and_b32 s18, s18, 0x3ffff\n\t"
      "s_mov_b32 m0, s95\n\t"
      "v_accvgpr_mov_b32 a107, a155\n\t"
      "v_mfma_f32_16x16x32_bf16 v[80:83], a[24:27], v[128:131], v[80:83]\n\t"
      "buffer_load_dwordx4 v208, s[44:47], s18 offen lds\n\t"
      "v_accvgpr_mov_b32 a108, a156\n\t"
      "v_mfma_f32_16x16x32_bf16 v[84:87], a[28:31], v[120:123], v[84:87]\n\t"
      "buffer_load_dwordx4 v209, s[44:47], s18 offen offset:1024 lds\n\t"
      "v_accvgpr_mov_b32 a109, a157\n\t"
      "v_mfma_f32_16x16x32_bf16 v[88:91], a[28:31], v[124:127], v[88:91]\n\t"
      "buffer_load_dwordx4 v210, s[44:47], s18 offen offset:2048 lds\n\t"
      "v_accvgpr_mov_b32 a110, a158\n\t"
      "v_mfma_f32_16x16x32_bf16 v[92:95], a[28:31], v[128:131], v[92:95]\n\t"
      "buffer_load_dwordx4 v211, s[44:47], s18 offen offset:3072 lds\n\t"
      "s_add_u32 s18, s18, 0x4000\n\t"
      "s_and_b32 s18, s18, 0x3ffff\n\t"
      "v_accvgpr_mov_b32 a111, a159\n\t"
      "s_waitcnt lgkmcnt(0)\n\t"
      "s_waitcnt vmcnt(16)\n\t"
      "v_mfma_f32_16x16x32_bf16 v[0:3], a[32:35], v[132:135], v[0:3]\n\t"
      "ds_read_b64_tr_b16 a[0:1], v220\n\t"
      "v_mfma_f32_16x16x32_bf16 v[4:7], a[32:35], v[136:139], v[4:7]\n\t"
      "ds_read_b64_tr_b16 a[2:3], v220 offset:4096\n\t"
      "v_mfma_f32_16x16x32_bf16 v[8:11], a[32:35], v[140:143], v[8:11]\n\t"
      "ds_read_b64_tr_b16 a[4:5], v221\n\t"
      "v_mfma_f32_16x16x32_bf16 v[12:15], a[36:39], v[132:135], v[12:15]\n\t"
      "ds_read_b64_tr_b16 a[6:7], v221 offset:4096\n\t"
      "s_mov_b32 m0, s96\n\t"
      "v_mfma_f32_16x16x32_bf16 v[16:19], a[36:39], v[136:139], v[16:19]\n\t"
      "buffer_load_dwordx4 v208, s[44:47], s18 offen lds\n\t"
      "v_mfma_f32_16x16x32_bf16 v[20:23], a[36:39], v[140:143], v[20:23]\n\t"
      "ds_read_b64_tr_b16 a[8:9], v222\n\t"
      "v_mfma_f32_16x16x32_bf16 v[24:27], a[40:43], v[132:135], v[24:27]\n\t"
      "ds_read_b64_tr_b16 a[10:11], v222 offset:4096\n\t"
      "v_mfma_f32_16x16x32_bf16 v[28:31], a[40:43], v[136:139], v[28:31]\n\t"
      "ds_read_b64_tr_b16 a[12:13], v223\n\t"
      "v_mfma_f32_16x16x32_bf16 v[32:35], a[40:43], v[140:143], v[32:35]\n\t"
      "buffer_load_dwordx4 v209, s[44:47], s18 offen offset:1024 lds\n\t"
      "v_mfma_f32_16x16x32_bf16 v[36:39], a[44:47], v[132:135], v[36:39]\n\t"
      "ds_read_b64_tr_b16 a[14:15], v223 offset:4096\n\t"
      "v_mfma_f32_16x16x32_bf16 v[40:43], a[44:47], v[136:139], v[40:43]\n\t"
      "ds_read_b64_tr_b16 a[16:17], v224\n\t"
      "v_mfma_f32_16x16x32_bf16 v[44:47], a[44:47], v[140:143], v[44:47]\n\t"
      "ds_read_b64_tr_b16 a[18:19], v224 offset:4096\n\t"
      "v_mfma_f32_16x16x32_bf16 v[48:51], a[48:51], v[132:135], v[48:51]\n\t"
      "buffer_load_dwordx4 v210, s[44:47], s18 offen offset:2048 lds\n\t"
      "v_mfma_f32_16x16x32_bf16 v[52:55], a[48:51], v[136:139], v[52:55]\n\t"
      "ds_read_b64_tr_b16 a[20:21], v225\n\t"
      "v_mfma_f32_16x16x32_bf16 v[56:59], a[48:51], v[140:143], v[56:59]\n\t"
      "ds_read_b64_tr_b16 a[22:23], v225 offset:4096\n\t"
      "v_mfma_f32_16x16x32_bf16 v[60:63], a[52:55], v[132:135], v[60:63]\n\t"
      "ds_read_b64_tr_b16 a[24:25], v226\n\t"
      "v_mfma_f32_16x16x32_bf16 v[64:67], a[52:55], v[136:139], v[64:67]\n\t"
      "buffer_load_dwordx4 v211, s[44:47], s18 offen offset:3072 lds\n\t"
      "v_mfma_f32_16x16x32_bf16 v[68:71], a[52:55], v[140:143], v[68:71]\n\t"
      "ds_read_b64_tr_b16 a[26:27], v226 offset:4096\n\t"
      "v_mfma_f32_16x16x32_bf16 v[72:75], a[56:59], v[132:135], v[72:75]\n\t"
      "ds_read_b64_tr_b16 a[28:29], v227\n\t"
      "v_mfma_f32_16x16x32_bf16 v[76:79], a[56:59], v[136:139], v[76:79]\n\t"
      "ds_read_b64_tr_b16 a[30:31], v227 offset:4096\n\t"
      "s_add_u32 s18, s18, 0x4000\n\t"
      "s_and_b32 s18, s18, 0x3ffff\n\t"
      "s_mov_b32 m0, s97\n\t"
      "v_mfma_f32_16x16x32_bf16 v[80:83], a[56:59], v[140:143], v[80:83]\n\t"
      "buffer_load_dwordx4 v208, s[44:47], s18 offen lds\n\t"
      "v_mfma_f32_16x16x32_bf16 v[84:87], a[60:63], v[132:135], v[84:87]\n\t"
      "buffer_load_dwordx4 v209, s[44:47], s18 offen offset:1024 lds\n\t"
      "v_mfma_f32_16x16x32_bf16 v[88:91], a[60:63], v[136:139], v[88:91]\n\t"
      "buffer_load_dwordx4 v210, s[44:47], s18 offen offset:2048 lds\n\t"
      "v_mfma_f32_16x16x32_bf16 v[92:95], a[60:63], v[140:143], v[92:95]\n\t"
      "buffer_load_dwordx4 v211, s[44:47], s18 offen offset:3072 lds\n\t"
      "s_add_u32 s18, s18, 0x4000\n\t"
      "s_and_b32 s18, s18, 0x3ffff\n\t"
      "s_waitcnt lgkmcnt(0)\n\t"
      "s_waitcnt vmcnt(16)\n\t"
      "v_mfma_f32_16x16x32_bf16 v[0:3], a[0:3], v[144:147], v[0:3]\n\t"
      "ds_read_b64_tr_b16 a[32:33], v220 offset:8192\n\t"
      "v_mfma_f32_16x16x32_bf16 v[4:7], a[0:3], v[148:151], v[4:7]\n\t"
      "ds_read_b64_tr_b16 a[34:35], v220 offset:12288\n\t"
      "v_mfma_f32_16x16x32_bf16 v[8:11], a[0:3], v[152:155], v[8:11]\n\t"
      "ds_read_b64_tr_b16 a[36:37], v221 offset:8192\n\t"
      "v_mfma_f32_16x16x32_bf16 v[12:15], a[4:7], v[144:147], v[12:15]\n\t"
      "ds_read_b64_tr_b16 a[38:39], v221 offset:12288\n\t"
      "s_mov_b32 m0, s90\n\t"
      "v_mfma_f32_16x16x32_bf16 v[16:19], a[4:7], v[148:151], v[16:19]\n\t"
      "buffer_load_dwordx4 v204, s[28:31], s52 offen nt lds\n\t"
      "v_mfma_f32_16x16x32_bf16 v[20:23], a[4:7], v[152:155], v[20:23]\n\t"
      "ds_read_b64_tr_b16 a[40:41], v222 offset:8192\n\t"
      "v_mfma_f32_16x16x32_bf16 v[24:27], a[8:11], v[144:147], v[24:27]\n\t"
      "ds_read_b64_tr_b16 a[42:43], v222 offset:12288\n\t"
      "v_mfma_f32_16x16x32_bf16 v[28:31], a[8:11], v[148:151], v[28:31]\n\t"
      "ds_read_b64_tr_b16 a[44:45], v223 offset:8192\n\t"
      "v_mfma_f32_16x16x32_bf16 v[32:35], a[8:11], v[152:155], v[32:35]\n\t"
      "buffer_load_dwordx4 v205, s[28:31], s52 offen offset:1024 nt lds\n\t"
      "v_mfma_f32_16x16x32_bf16 v[36:39], a[12:15], v[144:147], v[36:39]\n\t"
      "ds_read_b64_tr_b16 a[46:47], v223 offset:12288\n\t"
      "v_mfma_f32_16x16x32_bf16 v[40:43], a[12:15], v[148:151], v[40:43]\n\t"
      "ds_read_b64_tr_b16 a[48:49], v224 offset:8192\n\t"
      "v_mfma_f32_16x16x32_bf16 v[44:47], a[12:15], v[152:155], v[44:47]\n\t"
      "ds_read_b64_tr_b16 a[50:51], v224 offset:12288\n\t"
      "v_mfma_f32_16x16x32_bf16 v[48:51], a[16:19], v[144:147], v[48:51]\n\t"
      "buffer_load_dwordx4 v206, s[28:31], s52 offen offset:2048 nt lds\n\t"
      "v_mfma_f32_16x16x32_bf16 v[52:55], a[16:19], v[148:151], v[52:55]\n\t"
      "ds_read_b64_tr_b16 a[52:53], v225 offset:8192\n\t"
      "v_mfma_f32_16x16x32_bf16 v[56:59], a[16:19], v[152:155], v[56:59]\n\t"
      "ds_read_b64_tr_b16 a[54:55], v225 offset:12288\n\t"
      "v_mfma_f32_16x16x32_bf16 v[60:63], a[20:23], v[144:147], v[60:63]\n\t"
      "ds_read_b64_tr_b16 a[56:57], v226 offset:8192\n\t"
      "v_mfma_f32_16x16x32_bf16 v[64:67], a[20:23], v[148:151], v[64:67]\n\t"
      "buffer_load_dwordx4 v207, s[28:31], s52 offen offset:3072 nt lds\n\t"
      "v_mfma_f32_16x16x32_bf16 v[68:71], a[20:23], v[152:155], v[68:71]\n\t"
      "ds_read_b64_tr_b16 a[58:59], v226 offset:12288\n\t"
      "v_mfma_f32_16x16x32_bf16 v[72:75], a[24:27], v[144:147], v[72:75]\n\t"
      "ds_read_b64_tr_b16 a[60:61], v227 offset:8192\n\t"
      "v_mfma_f32_16x16x32_bf16 v[76:79], a[24:27], v[148:151], v[76:79]\n\t"
      "ds_read_b64_tr_b16 a[62:63], v227 offset:12288\n\t"
      "s_mov_b32 m0, s91\n\t"
      "v_mfma_f32_16x16x32_bf16 v[80:83], a[24:27], v[152:155], v[80:83]\n\t"
      "buffer_load_dwordx4 v204, s[28:31], s53 offen nt lds\n\t"
      "v_mfma_f32_16x16x32_bf16 v[84:87], a[28:31], v[144:147], v[84:87]\n\t"
      "buffer_load_dwordx4 v205, s[28:31], s53 offen offset:1024 nt lds\n\t"
      "v_mfma_f32_16x16x32_bf16 v[88:91], a[28:31], v[148:151], v[88:91]\n\t"
      "buffer_load_dwordx4 v206, s[28:31], s53 offen offset:2048 nt lds\n\t"
      "v_mfma_f32_16x16x32_bf16 v[92:95], a[28:31], v[152:155], v[92:95]\n\t"
      "buffer_load_dwordx4 v207, s[28:31], s53 offen offset:3072 nt lds\n\t"
      "s_add_u32 s44, s44, 0x40000\n\t"
      "s_addc_u32 s45, s45, 0\n\t"
      "s_sub_u32 s46, s46, 0x40000\n\t"
      "s_waitcnt lgkmcnt(0)\n\t"
      "s_waitcnt vmcnt(16)\n\t"
      "v_mfma_f32_16x16x32_bf16 v[0:3], a[32:35], v[156:159], v[0:3]\n\t"
      "ds_read_b64_tr_b16 a[0:1], v220 offset:16384\n\t"
      "v_mfma_f32_16x16x32_bf16 v[4:7], a[32:35], v[160:163], v[4:7]\n\t"
      "ds_read_b64_tr_b16 a[2:3], v220 offset:20480\n\t"
      "v_mfma_f32_16x16x32_bf16 v[8:11], a[32:35], v[164:167], v[8:11]\n\t"
      "ds_read_b64_tr_b16 a[4:5], v221 offset:16384\n\t"
      "v_mfma_f32_16x16x32_bf16 v[12:15], a[36:39], v[156:159], v[12:15]\n\t"
      "ds_read_b64_tr_b16 a[6:7], v221 offset:20480\n\t"
      "s_mov_b32 m0, s92\n\t"
      "v_mfma_f32_16x16x32_bf16 v[16:19], a[36:39], v[160:163], v[16:19]\n\t"
      "buffer_load_dwordx4 v204, s[28:31], s54 offen nt lds\n\t"
      "v_mfma_f32_16x16x32_bf16 v[20:23], a[36:39], v[164:167], v[20:23]\n\t"
      "ds_read_b64_tr_b16 a[8:9], v222 offset:16384\n\t"
      "v_mfma_f32_16x16x32_bf16 v[24:27], a[40:43], v[156:159], v[24:27]\n\t"
      "ds_read_b64_tr_b16 a[10:11], v222 offset:20480\n\t"
      "v_mfma_f32_16x16x32_bf16 v[28:31], a[40:43], v[160:163], v[28:31]\n\t"
      "ds_read_b64_tr_b16 a[12:13], v223 offset:16384\n\t"
      "v_mfma_f32_16x16x32_bf16 v[32:35], a[40:43], v[164:167], v[32:35]\n\t"
      "buffer_load_dwordx4 v205, s[28:31], s54 offen offset:1024 nt lds\n\t"
      "v_mfma_f32_16x16x32_bf16 v[36:39], a[44:47], v[156:159], v[36:39]\n\t"
      "ds_read_b64_tr_b16 a[14:15], v223 offset:20480\n\t"
      "v_mfma_f32_16x16x32_bf16 v[40:43], a[44:47], v[160:163], v[40:43]\n\t"
      "ds_read_b64_tr_b16 a[16:17], v224 offset:16384\n\t"
      "v_mfma_f32_16x16x32_bf16 v[44:47], a[44:47], v[164:167], v[44:47]\n\t"
      "ds_read_b64_tr_b16 a[18:19], v224 offset:20480\n\t"
      "v_mfma_f32_16x16x32_bf16 v[48:51], a[48:51], v[156:159], v[48:51]\n\t"
      "buffer_load_dwordx4 v206, s[28:31], s54 offen offset:2048 nt lds\n\t"
      "v_mfma_f32_16x16x32_bf16 v[52:55], a[48:51], v[160:163], v[52:55]\n\t"
      "ds_read_b64_tr_b16 a[20:21], v225 offset:16384\n\t"
      "v_mfma_f32_16x16x32_bf16 v[56:59], a[48:51], v[164:167], v[56:59]\n\t"
      "ds_read_b64_tr_b16 a[22:23], v225 offset:20480\n\t"
      "v_mfma_f32_16x16x32_bf16 v[60:63], a[52:55], v[156:159], v[60:63]\n\t"
      "ds_read_b64_tr_b16 a[24:25], v226 offset:16384\n\t"
      "v_mfma_f32_16x16x32_bf16 v[64:67], a[52:55], v[160:163], v[64:67]\n\t"
      "buffer_load_dwordx4 v207, s[28:31], s54 offen offset:3072 nt lds\n\t"
      "v_mfma_f32_16x16x32_bf16 v[68:71], a[52:55], v[164:167], v[68:71]\n\t"
      "ds_read_b64_tr_b16 a[26:27], v226 offset:20480\n\t"
      "v_mfma_f32_16x16x32_bf16 v[72:75], a[56:59], v[156:159], v[72:75]\n\t"
      "ds_read_b64_tr_b16 a[28:29], v227 offset:16384\n\t"
      "v_mfma_f32_16x16x32_bf16 v[76:79], a[56:59], v[160:163], v[76:79]\n\t"
      "ds_read_b64_tr_b16 a[30:31], v227 offset:20480\n\t"
      "s_mov_b32 m0, s93\n\t"
      "v_mfma_f32_16x16x32_bf16 v[80:83], a[56:59], v[164:167], v[80:83]\n\t"
      "buffer_load_dwordx4 v204, s[28:31], s55 offen nt lds\n\t"
      "v_mfma_f32_16x16x32_bf16 v[84:87], a[60:63], v[156:159], v[84:87]\n\t"
      "buffer_load_dwordx4 v205, s[28:31], s55 offen offset:1024 nt lds\n\t"
      "v_mfma_f32_16x16x32_bf16 v[88:91], a[60:63], v[160:163], v[88:91]\n\t"
      "buffer_load_dwordx4 v206, s[28:31], s55 offen offset:2048 nt lds\n\t"
      "v_mfma_f32_16x16x32_bf16 v[92:95], a[60:63], v[164:167], v[92:95]\n\t"
      "buffer_load_dwordx4 v207, s[28:31], s55 offen offset:3072 nt lds\n\t"
      "s_waitcnt lgkmcnt(0)\n\t"
      "s_waitcnt vmcnt(16)\n\t"
      "v_mfma_f32_16x16x32_bf16 v[0:3], a[0:3], v[168:171], v[0:3]\n\t"
      "ds_read_b64_tr_b16 a[32:33], v220 offset:24576\n\t"
      "v_mfma_f32_16x16x32_bf16 v[4:7], a[0:3], v[172:175], v[4:7]\n\t"
      "ds_read_b64_tr_b16 a[34:35], v220 offset:28672\n\t"
      "v_mfma_f32_16x16x32_bf16 v[8:11], a[0:3], v[176:179], v[8:11]\n\t"
      "ds_read_b64_tr_b16 a[36:37], v221 offset:24576\n\t"
      "v_mfma_f32_16x16x32_bf16 v[12:15], a[4:7], v[168:171], v[12:15]\n\t"
      "ds_read_b64_tr_b16 a[38:39], v221 offset:28672\n\t"
      "s_mov_b32 m0, s94\n\t"
      "v_mfma_f32_16x16x32_bf16 v[16:19], a[4:7], v[172:175], v[16:19]\n\t"
      "buffer_load_dwordx4 v204, s[28:31], s56 offen nt lds\n\t"
      "v_mfma_f32_16x16x32_bf16 v[20:23], a[4:7], v[176:179], v[20:23]\n\t"
      "ds_read_b64_tr_b16 a[40:41], v222 offset:24576\n\t"
      "v_mfma_f32_16x16x32_bf16 v[24:27], a[8:11], v[168:171], v[24:27]\n\t"
      "ds_read_b64_tr_b16 a[42:43], v222 offset:28672\n\t"
      "v_mfma_f32_16x16x32_bf16 v[28:31], a[8:11], v[172:175], v[28:31]\n\t"
      "ds_read_b64_tr_b16 a[44:45], v223 offset:24576\n\t"
      "v_mfma_f32_16x16x32_bf16 v[32:35], a[8:11], v[176:179], v[32:35]\n\t"
      "buffer_load_dwordx4 v205, s[28:31], s56 offen offset:1024 nt lds\n\t"
      "v_mfma_f32_16x16x32_bf16 v[36:39], a[12:15], v[168:171], v[36:39]\n\t"
      "ds_read_b64_tr_b16 a[46:47], v223 offset:28672\n\t"
      "v_mfma_f32_16x16x32_bf16 v[40:43], a[12:15], v[172:175], v[40:43]\n\t"
      "ds_read_b64_tr_b16 a[48:49], v224 offset:24576\n\t"
      "v_mfma_f32_16x16x32_bf16 v[44:47], a[12:15], v[176:179], v[44:47]\n\t"
      "ds_read_b64_tr_b16 a[50:51], v224 offset:28672\n\t"
      "v_mfma_f32_16x16x32_bf16 v[48:51], a[16:19], v[168:171], v[48:51]\n\t"
      "buffer_load_dwordx4 v206, s[28:31], s56 offen offset:2048 nt lds\n\t"
      "v_mfma_f32_16x16x32_bf16 v[52:55], a[16:19], v[172:175], v[52:55]\n\t"
      "ds_read_b64_tr_b16 a[52:53], v225 offset:24576\n\t"
      "v_mfma_f32_16x16x32_bf16 v[56:59], a[16:19], v[176:179], v[56:59]\n\t"
      "ds_read_b64_tr_b16 a[54:55], v225 offset:28672\n\t"
      "v_mfma_f32_16x16x32_bf16 v[60:63], a[20:23], v[168:171], v[60:63]\n\t"
      "ds_read_b64_tr_b16 a[56:57], v226 offset:24576\n\t"
      "v_mfma_f32_16x16x32_bf16 v[64:67], a[20:23], v[172:175], v[64:67]\n\t"
      "buffer_load_dwordx4 v207, s[28:31], s56 offen offset:3072 nt lds\n\t"
      "v_mfma_f32_16x16x32_bf16 v[68:71], a[20:23], v[176:179], v[68:71]\n\t"
      "ds_read_b64_tr_b16 a[58:59], v226 offset:28672\n\t"
      "v_mfma_f32_16x16x32_bf16 v[72:75], a[24:27], v[168:171], v[72:75]\n\t"
      "ds_read_b64_tr_b16 a[60:61], v227 offset:24576\n\t"
      "v_mfma_f32_16x16x32_bf16 v[76:79], a[24:27], v[172:175], v[76:79]\n\t"
      "ds_read_b64_tr_b16 a[62:63], v227 offset:28672\n\t"
      "s_mov_b32 m0, s95\n\t"
      "v_mfma_f32_16x16x32_bf16 v[80:83], a[24:27], v[176:179], v[80:83]\n\t"
      "buffer_load_dwordx4 v204, s[28:31], s57 offen nt lds\n\t"
      "v_mfma_f32_16x16x32_bf16 v[84:87], a[28:31], v[168:171], v[84:87]\n\t"
      "buffer_load_dwordx4 v205, s[28:31], s57 offen offset:1024 nt lds\n\t"
      "v_mfma_f32_16x16x32_bf16 v[88:91], a[28:31], v[172:175], v[88:91]\n\t"
      "buffer_load_dwordx4 v206, s[28:31], s57 offen offset:2048 nt lds\n\t"
      "v_mfma_f32_16x16x32_bf16 v[92:95], a[28:31], v[176:179], v[92:95]\n\t"
      "buffer_load_dwordx4 v207, s[28:31], s57 offen offset:3072 nt lds\n\t"
      "s_waitcnt lgkmcnt(0)\n\t"
      "s_waitcnt vmcnt(16)\n\t"
      "s_mov_b32 m0, s96\n\t"
      "v_mfma_f32_16x16x32_bf16 v[0:3], a[32:35], v[180:183], v[0:3]\n\t"
      "buffer_load_dwordx4 v204, s[28:31], s58 offen nt lds\n\t"
      "v_xad_u32 v220, v217, 0, s14\n\t"
      "v_mfma_f32_16x16x32_bf16 v[4:7], a[32:35], v[184:187], v[4:7]\n\t"
      "v_xad_u32 v221, v217, 64, s14\n\t"
      "v_mfma_f32_16x16x32_bf16 v[8:11], a[32:35], v[188:191], v[8:11]\n\t"
      "buffer_load_dwordx4 v205, s[28:31], s58 offen offset:1024 nt lds\n\t"
      "v_xor_b32 v222, 128, v217\n\t"
      "v_add_u32 v222, s14, v222\n\t"
      "v_mfma_f32_16x16x32_bf16 v[12:15], a[36:39], v[180:183], v[12:15]\n\t"
      "v_xor_b32 v223, 192, v217\n\t"
      "v_add_u32 v223, s14, v223\n\t"
      "v_mfma_f32_16x16x32_bf16 v[16:19], a[36:39], v[184:187], v[16:19]\n\t"
      "ds_read_b128 a[0:3], v220\n\t"
      "v_mfma_f32_16x16x32_bf16 v[20:23], a[36:39], v[188:191], v[20:23]\n\t"
      "buffer_load_dwordx4 v206, s[28:31], s58 offen offset:2048 nt lds\n\t"
      "v_mfma_f32_16x16x32_bf16 v[24:27], a[40:43], v[180:183], v[24:27]\n\t"
      "ds_read_b128 a[4:7], v221\n\t"
      "v_mfma_f32_16x16x32_bf16 v[28:31], a[40:43], v[184:187], v[28:31]\n\t"
      "v_mfma_f32_16x16x32_bf16 v[32:35], a[40:43], v[188:191], v[32:35]\n\t"
      "ds_read_b128 a[8:11], v222\n\t"
      "v_mfma_f32_16x16x32_bf16 v[36:39], a[44:47], v[180:183], v[36:39]\n\t"
      "buffer_load_dwordx4 v207, s[28:31], s58 offen offset:3072 nt lds\n\t"
      "v_mfma_f32_16x16x32_bf16 v[40:43], a[44:47], v[184:187], v[40:43]\n\t"
      "ds_read_b128 a[12:15], v223\n\t"
      "v_accvgpr_write_b32 a112, v0\n\t"
      "v_accvgpr_write_b32 a113, v1\n\t"
      "v_mfma_f32_16x16x32_bf16 v[44:47], a[44:47], v[188:191], v[44:47]\n\t"
      "v_accvgpr_write_b32 a114, v2\n\t"
      "v_accvgpr_write_b32 a115, v3\n\t"
      "v_mfma_f32_16x16x32_bf16 v[48:51], a[48:51], v[180:183], v[48:51]\n\t"
      "ds_read_b128 a[16:19], v220 offset:4096\n\t"
      "s_mov_b32 m0, s97\n\t"
      "v_accvgpr_write_b32 a116, v4\n\t"
      "v_accvgpr_write_b32 a117, v5\n\t"
      "v_mfma_f32_16x16x32_bf16 v[52:55], a[48:51], v[184:187], v[52:55]\n\t"
      "buffer_load_dwordx4 v204, s[28:31], s59 offen nt lds\n\t"
      "v_accvgpr_write_b32 a118, v6\n\t"
      "v_mfma_f32_16x16x32_bf16 v[56:59], a[48:51], v[188:191], v[56:59]\n\t"
      "ds_read_b128 a[20:23], v221 offset:4096\n\t"
      "v_accvgpr_write_b32 a119, v7\n\t"
      "v_accvgpr_write_b32 a120, v8\n\t"
      "v_mfma_f32_16x16x32_bf16 v[60:63], a[52:55], v[180:183], v[60:63]\n\t"
      "v_accvgpr_write_b32 a121, v9\n\t"
      "v_accvgpr_write_b32 a122, v10\n\t"
      "v_mfma_f32_16x16x32_bf16 v[64:67], a[52:55], v[184:187], v[64:67]\n\t"
      "ds_read_b128 a[24:27], v222 offset:4096\n\t"
      "v_accvgpr_write_b32 a123, v11\n\t"
      "v_mfma_f32_16x16x32_bf16 v[68:71], a[52:55], v[188:191], v[68:71]\n\t"
      "buffer_load_dwordx4 v205, s[28:31], s59 offen offset:1024 nt lds\n\t"
      "v_accvgpr_write_b32 a124, v12\n\t"
      "v_accvgpr_write_b32 a125, v13\n\t"
      "v_mfma_f32_16x16x32_bf16 v[72:75], a[56:59], v[180:183], v[72:75]\n\t"
      "ds_read_b128 a[28:31], v223 offset:4096\n\t"
      "v_accvgpr_write_b32 a126, v14\n\t"
      "v_accvgpr_write_b32 a127, v15\n\t"
      "v_mfma_f32_16x16x32_bf16 v[76:79], a[56:59], v[184:187], v[76:79]\n\t"
      "v_accvgpr_write_b32 a128, v16\n\t"
      "v_accvgpr_write_b32 a129, v17\n\t"
      "v_mfma_f32_16x16x32_bf16 v[80:83], a[56:59], v[188:191], v[80:83]\n\t"
      "buffer_load_dwordx4 v206, s[28:31], s59 offen offset:2048 nt lds\n\t"
      "v_accvgpr_write_b32 a130, v18\n\t"
      "v_mfma_f32_16x16x32_bf16 v[84:87], a[60:63], v[180:183], v[84:87]\n\t"
      "v_accvgpr_write_b32 a131, v19\n\t"
      "v_accvgpr_write_b32 a132, v20\n\t"
      "v_mfma_f32_16x16x32_bf16 v[88:91], a[60:63], v[184:187], v[88:91]\n\t"
      "buffer_load_dwordx4 v207, s[28:31], s59 offen offset:3072 nt lds\n\t"
      "v_accvgpr_write_b32 a133, v21\n\t"
      "v_accvgpr_write_b32 a134, v22\n\t"
      "v_mfma_f32_16x16x32_bf16 v[92:95], a[60:63], v[188:191], v[92:95]\n\t"
      "v_accvgpr_write_b32 a135, v23\n\t"
      "v_accvgpr_write_b32 a136, v24\n\t"
      "v_accvgpr_write_b32 a137, v25\n\t"
      "v_accvgpr_write_b32 a138, v26\n\t"
      "v_accvgpr_write_b32 a139, v27\n\t"
      "v_accvgpr_write_b32 a140, v28\n\t"
      "v_accvgpr_write_b32 a141, v29\n\t"
      "v_accvgpr_write_b32 a142, v30\n\t"
      "v_accvgpr_write_b32 a143, v31\n\t"
      "v_accvgpr_write_b32 a144, v32\n\t"
      "v_accvgpr_write_b32 a145, v33\n\t"
      "v_accvgpr_write_b32 a146, v34\n\t"
      "v_accvgpr_write_b32 a147, v35\n\t"
      "v_accvgpr_write_b32 a148, v36\n\t"
      "v_accvgpr_write_b32 a149, v37\n\t"
      "v_accvgpr_write_b32 a150, v38\n\t"
      "v_accvgpr_write_b32 a151, v39\n\t"
      "v_accvgpr_write_b32 a152, v40\n\t"
      "v_accvgpr_write_b32 a153, v41\n\t"
      "v_accvgpr_write_b32 a154, v42\n\t"
      "v_accvgpr_write_b32 a155, v43\n\t"
      "v_accvgpr_write_b32 a156, v44\n\t"
      "v_accvgpr_write_b32 a157, v45\n\t"
      "v_accvgpr_write_b32 a158, v46\n\t"
      "v_accvgpr_write_b32 a159, v47\n\t"
      "v_accvgpr_write_b32 a160, v48\n\t"
      "v_accvgpr_write_b32 a161, v49\n\t"
      "v_accvgpr_write_b32 a162, v50\n\t"
      "v_accvgpr_write_b32 a163, v51\n\t"
      "v_accvgpr_write_b32 a164, v52\n\t"
      "v_accvgpr_write_b32 a165, v53\n\t"
      "v_accvgpr_write_b32 a166, v54\n\t"
      "v_accvgpr_write_b32 a167, v55\n\t"
      "v_accvgpr_write_b32 a168, v56\n\t"
      "v_accvgpr_write_b32 a169, v57\n\t"
      "v_accvgpr_write_b32 a170, v58\n\t"
      "v_accvgpr_write_b32 a171, v59\n\t"
      "v_accvgpr_write_b32 a172, v60\n\t"
      "v_accvgpr_write_b32 a173, v61\n\t"
      "v_accvgpr_write_b32 a174, v62\n\t"
      "v_accvgpr_write_b32 a175, v63\n\t"
      "v_accvgpr_write_b32 a176, v64\n\t"
      "v_accvgpr_write_b32 a177, v65\n\t"
      "v_accvgpr_write_b32 a178, v66\n\t"
      "v_accvgpr_write_b32 a179, v67\n\t"
      "v_accvgpr_write_b32 a180, v68\n\t"
      "v_accvgpr_write_b32 a181, v69\n\t"
      "v_accvgpr_write_b32 a182, v70\n\t"
      "v_accvgpr_write_b32 a183, v71\n\t"
      "v_accvgpr_write_b32 a184, v72\n\t"
      "v_accvgpr_write_b32 a185, v73\n\t"
      "v_accvgpr_write_b32 a186, v74\n\t"
      "v_accvgpr_write_b32 a187, v75\n\t"
      "v_accvgpr_write_b32 a188, v76\n\t"
      "v_accvgpr_write_b32 a189, v77\n\t"
      "v_accvgpr_write_b32 a190, v78\n\t"
      "v_accvgpr_write_b32 a191, v79\n\t"
      "v_accvgpr_write_b32 a192, v80\n\t"
      "v_accvgpr_write_b32 a193, v81\n\t"
      "v_accvgpr_write_b32 a194, v82\n\t"
      "v_accvgpr_write_b32 a195, v83\n\t"
      "v_accvgpr_write_b32 a196, v84\n\t"
      "v_accvgpr_write_b32 a197, v85\n\t"
      "v_accvgpr_write_b32 a198, v86\n\t"
      "v_accvgpr_write_b32 a199, v87\n\t"
      "v_accvgpr_write_b32 a200, v88\n\t"
      "v_accvgpr_write_b32 a201, v89\n\t"
      "v_accvgpr_write_b32 a202, v90\n\t"
      "v_accvgpr_write_b32 a203, v91\n\t"
      "v_accvgpr_write_b32 a204, v92\n\t"
      "v_accvgpr_write_b32 a205, v93\n\t"
      "v_accvgpr_write_b32 a206, v94\n\t"
      "v_accvgpr_write_b32 a207, v95\n\t"
      "s_mov_b32 s13, 1\n\t"
      "s_waitcnt lgkmcnt(0)\n\t"
      "s_branch L_pass_%=\n\t"
      "L_Blast_%=:\n\t"
      "ds_read_b128 v[96:99], v48\n\t"
      "v_accvgpr_read_b32 v0, a112\n\t"
      "v_accvgpr_read_b32 v1, a113\n\t"
      "v_accvgpr_read_b32 v2, a114\n\t"
      "ds_read_b128 v[100:103], v48 offset:1024\n\t"
      "v_accvgpr_read_b32 v3, a115\n\t"
      "v_accvgpr_read_b32 v4, a116\n\t"
      "v_accvgpr_read_b32 v5, a117\n\t"
      "ds_read_b128 v[104:107], v48 offset:2048\n\t"
      "v_accvgpr_read_b32 v6, a118\n\t"
      "v_accvgpr_read_b32 v7, a119\n\t"
      "v_accvgpr_read_b32 v8, a120\n\t"
      "ds_read_b128 v[108:111], v49\n\t"
      "v_accvgpr_read_b32 v9, a121\n\t"
      "v_accvgpr_read_b32 v10, a122\n\t"
      "v_accvgpr_read_b32 v11, a123\n\t"
      "ds_read_b128 v[112:115], v49 offset:1024\n\t"
      "v_accvgpr_read_b32 v12, a124\n\t"
      "v_accvgpr_read_b32 v13, a125\n\t"
      "v_accvgpr_read_b32 v14, a126\n\t"
      "ds_read_b128 v[116:119], v49 offset:2048\n\t"
      "v_accvgpr_read_b32 v15, a127\n\t"
      "v_accvgpr_read_b32 v16, a128\n\t"
      "v_accvgpr_read_b32 v17, a129\n\t"
      "ds_read_b128 v[120:123], v50\n\t"
      "v_accvgpr_read_b32 v18, a130\n\t"
      "v_accvgpr_read_b32 v19, a131\n\t"
      "v_accvgpr_read_b32 v20, a132\n\t"
      "ds_read_b128 v[124:127], v50 offset:1024\n\t"
      "v_accvgpr_read_b32 v21, a133\n\t"
      "v_accvgpr_read_b32 v22, a134\n\t"
      "v_accvgpr_read_b32 v23, a135\n\t"
      "ds_read_b128 v[128:131], v50 offset:2048\n\t"
      "v_accvgpr_read_b32 v24, a136\n\t"
      "v_accvgpr_read_b32 v25, a137\n\t"
      "v_accvgpr_read_b32 v26, a138\n\t"
      "ds_read_b128 v[132:135], v51\n\t"
      "v_accvgpr_read_b32 v27, a139\n\t"
      "v_accvgpr_read_b32 v28, a140\n\t"
      "v_accvgpr_read_b32 v29, a141\n\t"
      "ds_read_b128 v[136:139], v51 offset:1024\n\t"
      "v_accvgpr_read_b32 v30, a142\n\t"
      "v_accvgpr_read_b32 v31, a143\n\t"
      "v_accvgpr_read_b32 v32, a144\n\t"
      "ds_read_b128 v[140:143], v51 offset:2048\n\t"
      "v_accvgpr_read_b32 v33, a145\n\t"
      "v_accvgpr_read_b32 v34, a146\n\t"
      "v_accvgpr_read_b32 v35, a147\n\t"
      "ds_read_b128 v[144:147], v52\n\t"
      "v_accvgpr_read_b32 v36, a148\n\t"
      "v_accvgpr_read_b32 v37, a149\n\t"
      "v_accvgpr_read_b32 v38, a150\n\t"
      "ds_read_b128 v[148:151], v52 offset:1024\n\t"
      "v_accvgpr_read_b32 v39, a151\n\t"
      "v_accvgpr_read_b32 v40, a152\n\t"
      "v_accvgpr_read_b32 v41, a153\n\t"
      "ds_read_b128 v[152:155], v52 offset:2048\n\t"
      "v_accvgpr_read_b32 v42, a154\n\t"
      "v_accvgpr_read_b32 v43, a155\n\t"
      "v_accvgpr_read_b32 v44, a156\n\t"
      "ds_read_b128 v[156:159], v53\n\t"
      "v_accvgpr_read_b32 v45, a157\n\t"
      "v_accvgpr_read_b32 v46, a158\n\t"
      "v_accvgpr_read_b32 v47, a159\n\t"
      "ds_read_b128 v[160:163], v53 offset:1024\n\t"
      "v_accvgpr_read_b32 v56, a168\n\t"
      "v_accvgpr_read_b32 v57, a169\n\t"
      "v_accvgpr_read_b32 v58, a170\n\t"
      "ds_read_b128 v[164:167], v53 offset:2048\n\t"
      "v_accvgpr_read_b32 v59, a171\n\t"
      "v_accvgpr_read_b32 v60, a172\n\t"
      "v_accvgpr_read_b32 v61, a173\n\t"
      "ds_read_b128 v[168:171], v54\n\t"
      "v_accvgpr_read_b32 v62, a174\n\t"
      "v_accvgpr_read_b32 v63, a175\n\t"
      "v_accvgpr_read_b32 v64, a176\n\t"
      "ds_read_b128 v[172:175], v54 offset:1024\n\t"
      "v_accvgpr_read_b32 v65, a177\n\t"
      "v_accvgpr_read_b32 v66, a178\n\t"
      "v_accvgpr_read_b32 v67, a179\n\t"
      "ds_read_b128 v[176:179], v54 offset:2048\n\t"
      "v_accvgpr_read_b32 v68, a180\n\t"
      "v_accvgpr_read_b32 v69, a181\n\t"
      "v_accvgpr_read_b32 v70, a182\n\t"
      "ds_read_b128 v[180:183], v55\n\t"
      "v_accvgpr_read_b32 v71, a183\n\t"
      "v_accvgpr_read_b32 v72, a184\n\t"
      "v_accvgpr_read_b32 v73, a185\n\t"
      "ds_read_b128 v[184:187], v55 offset:1024\n\t"
      "v_accvgpr_read_b32 v74, a186\n\t"
      "v_accvgpr_read_b32 v75, a187\n\t"
      "v_accvgpr_read_b32 v76, a188\n\t"
      "ds_read_b128 v[188:191], v55 offset:2048\n\t"
      "v_accvgpr_read_b32 v77, a189\n\t"
      "v_accvgpr_read_b32 v78, a190\n\t"
      "v_accvgpr_read_b32 v79, a191\n\t"
      "v_accvgpr_read_b32 v80, a192\n\t"
      "v_accvgpr_read_b32 v81, a193\n\t"
      "v_accvgpr_read_b32 v82, a194\n\t"
      "v_accvgpr_read_b32 v83, a195\n\t"
      "v_accvgpr_read_b32 v84, a196\n\t"
      "v_accvgpr_read_b32 v85, a197\n\t"
      "v_accvgpr_read_b32 v86, a198\n\t"
      "v_accvgpr_read_b32 v87, a199\n\t"
      "v_accvgpr_read_b32 v88, a200\n\t"
      "v_accvgpr_read_b32 v89, a201\n\t"
      "v_accvgpr_read_b32 v90, a202\n\t"
      "v_accvgpr_read_b32 v91, a203\n\t"
      "v_accvgpr_read_b32 v92, a204\n\t"
      "v_accvgpr_read_b32 v93, a205\n\t"
      "v_accvgpr_read_b32 v94, a206\n\t"
      "v_accvgpr_read_b32 v95, a207\n\t"
      "v_accvgpr_read_b32 v48, a160\n\t"
      "v_accvgpr_read_b32 v49, a161\n\t"
      "v_accvgpr_read_b32 v50, a162\n\t"
      "v_accvgpr_read_b32 v51, a163\n\t"
      "v_accvgpr_read_b32 v52, a164\n\t"
      "v_accvgpr_read_b32 v53, a165\n\t"
      "v_accvgpr_read_b32 v54, a166\n\t"
      "v_accvgpr_read_b32 v55, a167\n\t"
      "L_O_%=:\n\t"
      "s_waitcnt lgkmcnt(15)\n\t"
      "s_waitcnt vmcnt(16)\n\t"
      "v_mfma_f32_16x16x32_bf16 v[0:3], a[0:3], v[96:99], v[0:3]\n\t"
      "ds_read_b64_tr_b16 a[32:33], v220 offset:8192\n\t"
      "v_mfma_f32_16x16x32_bf16 v[4:7], a[0:3], v[100:103], v[4:7]\n\t"
      "ds_read_b64_tr_b16 a[34:35], v220 offset:12288\n\t"
      "v_mfma_f32_16x16x32_bf16 v[8:11], a[0:3], v[104:107], v[8:11]\n\t"
      "ds_read_b64_tr_b16 a[36:37], v221 offset:8192\n\t"
      "v_mfma_f32_16x16x32_bf16 v[12:15], a[4:7], v[96:99], v[12:15]\n\t"
      "ds_read_b64_tr_b16 a[38:39], v221 offset:12288\n\t"
      "s_mov_b32 m0, s90\n\t"
      "v_mfma_f32_16x16x32_bf16 v[16:19], a[4:7], v[100:103], v[16:19]\n\t"
      "buffer_load_dwordx4 v208, s[44:47], s18 offen lds\n\t"
      "v_mfma_f32_16x16x32_bf16 v[20:23], a[4:7], v[104:107], v[20:23]\n\t"
      "ds_read_b64_tr_b16 a[40:41], v222 offset:8192\n\t"
      "v_mfma_f32_16x16x32_bf16 v[24:27], a[8:11], v[96:99], v[24:27]\n\t"
      "ds_read_b64_tr_b16 a[42:43], v222 offset:12288\n\t"
      "v_mfma_f32_16x16x32_bf16 v[28:31], a[8:11], v[100:103], v[28:31]\n\t"
      "ds_read_b64_tr_b16 a[44:45], v223 offset:8192\n\t"
      "v_mfma_f32_16x16x32_bf16 v[32:35], a[8:11], v[104:107], v[32:35]\n\t"
      "buffer_load_dwordx4 v209, s[44:47], s18 offen offset:1024 lds\n\t"
      "v_mfma_f32_16x16x32_bf16 v[36:39], a[12:15], v[96:99], v[36:39]\n\t"
      "ds_read_b64_tr_b16 a[46:47], v223 offset:12288\n\t"
      "v_mfma_f32_16x16x32_bf16 v[40:43], a[12:15], v[100:103], v[40:43]\n\t"
      "ds_read_b64_tr_b16 a[48:49], v224 offset:8192\n\t"
      "v_mfma_f32_16x16x32_bf16 v[44:47], a[12:15], v[104:107], v[44:47]\n\t"
      "ds_read_b64_tr_b16 a[50:51], v224 offset:12288\n\t"
      "v_mfma_f32_16x16x32_bf16 v[48:51], a[16:19], v[96:99], v[48:51]\n\t"
      "buffer_load_dwordx4 v210, s[44:47], s18 offen offset:2048 lds\n\t"
      "v_mfma_f32_16x16x32_bf16 v[52:55], a[16:19], v[100:103], v[52:55]\n\t"
      "ds_read_b64_tr_b16 a[52:53], v225 offset:8192\n\t"
      "v_mfma_f32_16x16x32_bf16 v[56:59], a[16:19], v[104:107], v[56:59]\n\t"
      "ds_read_b64_tr_b16 a[54:55], v225 offset:12288\n\t"
      "v_mfma_f32_16x16x32_bf16 v[60:63], a[20:23], v[96:99], v[60:63]\n\t"
      "ds_read_b64_tr_b16 a[56:57], v226 offset:8192\n\t"
      "v_mfma_f32_16x16x32_bf16 v[64:67], a[20:23], v[100:103], v[64:67]\n\t"
      "buffer_load_dwordx4 v211, s[44:47], s18 offen offset:3072 lds\n\t"
      "v_mfma_f32_16x16x32_bf16 v[68:71], a[20:23], v[104:107], v[68:71]\n\t"
      "ds_read_b64_tr_b16 a[58:59], v226 offset:12288\n\t"
      "v_mfma_f32_16x16x32_bf16 v[72:75], a[24:27], v[96:99], v[72:75]\n\t"
      "ds_read_b64_tr_b16 a[60:61], v227 offset:8192\n\t"
      "v_mfma_f32_16x16x32_bf16 v[76:79], a[24:27], v[100:103], v[76:79]\n\t"
      "ds_read_b64_tr_b16 a[62:63], v227 offset:12288\n\t"
      "s_add_u32 s18, s18, 0x4000\n\t"
      "s_and_b32 s18, s18, 0x3ffff\n\t"
      "s_mov_b32 m0, s91\n\t"
      "v_mfma_f32_16x16x32_bf16 v[80:83], a[24:27], v[104:107], v[80:83]\n\t"
      "buffer_load_dwordx4 v208, s[44:47], s18 offen lds\n\t"
      "v_mfma_f32_16x16x32_bf16 v[84:87], a[28:31], v[96:99], v[84:87]\n\t"
      "buffer_load_dwordx4 v209, s[44:47], s18 offen offset:1024 lds\n\t"
      "v_mfma_f32_16x16x32_bf16 v[88:91], a[28:31], v[100:103], v[88:91]\n\t"
      "buffer_load_dwordx4 v210, s[44:47], s18 offen offset:2048 lds\n\t"
      "v_mfma_f32_16x16x32_bf16 v[92:95], a[28:31], v[104:107], v[92:95]\n\t"
      "buffer_load_dwordx4 v211, s[44:47], s18 offen offset:3072 lds\n\t"
      "s_add_u32 s18, s18, 0x4000\n\t"
      "s_and_b32 s18, s18, 0x3ffff\n\t"
      "s_waitcnt lgkmcnt(0)\n\t"
      "s_waitcnt vmcnt(16)\n\t"
      "v_mfma_f32_16x16x32_bf16 v[0:3], a[32:35], v[108:111], v[0:3]\n\t"
      "ds_read_b64_tr_b16 a[0:1], v220 offset:16384\n\t"
      "v_mfma_f32_16x16x32_bf16 v[4:7], a[32:35], v[112:115], v[4:7]\n\t"
      "ds_read_b64_tr_b16 a[2:3], v220 offset:20480\n\t"
      "v_mfma_f32_16x16x32_bf16 v[8:11], a[32:35], v[116:119], v[8:11]\n\t"
      "ds_read_b64_tr_b16 a[4:5], v221 offset:16384\n\t"
      "v_mfma_f32_16x16x32_bf16 v[12:15], a[36:39], v[108:111], v[12:15]\n\t"
      "ds_read_b64_tr_b16 a[6:7], v221 offset:20480\n\t"
      "s_mov_b32 m0, s92\n\t"
      "v_mfma_f32_16x16x32_bf16 v[16:19], a[36:39], v[112:115], v[16:19]\n\t"
      "buffer_load_dwordx4 v208, s[44:47], s18 offen lds\n\t"
      "v_mfma_f32_16x16x32_bf16 v[20:23], a[36:39], v[116:119], v[20:23]\n\t"
      "ds_read_b64_tr_b16 a[8:9], v222 offset:16384\n\t"
      "v_mfma_f32_16x16x32_bf16 v[24:27], a[40:43], v[108:111], v[24:27]\n\t"
      "ds_read_b64_tr_b16 a[10:11], v222 offset:20480\n\t"
      "v_mfma_f32_16x16x32_bf16 v[28:31], a[40:43], v[112:115], v[28:31]\n\t"
      "ds_read_b64_tr_b16 a[12:13], v223 offset:16384\n\t"
      "v_mfma_f32_16x16x32_bf16 v[32:35], a[40:43], v[116:119], v[32:35]\n\t"
      "buffer_load_dwordx4 v209, s[44:47], s18 offen offset:1024 lds\n\t"
      "v_mfma_f32_16x16x32_bf16 v[36:39], a[44:47], v[108:111], v[36:39]\n\t"
      "ds_read_b64_tr_b16 a[14:15], v223 offset:20480\n\t"
      "v_mfma_f32_16x16x32_bf16 v[40:43], a[44:47], v[112:115], v[40:43]\n\t"
      "ds_read_b64_tr_b16 a[16:17], v224 offset:16384\n\t"
      "v_mfma_f32_16x16x32_bf16 v[44:47], a[44:47], v[116:119], v[44:47]\n\t"
      "ds_read_b64_tr_b16 a[18:19], v224 offset:20480\n\t"
      "v_mfma_f32_16x16x32_bf16 v[48:51], a[48:51], v[108:111], v[48:51]\n\t"
      "buffer_load_dwordx4 v210, s[44:47], s18 offen offset:2048 lds\n\t"
      "v_mfma_f32_16x16x32_bf16 v[52:55], a[48:51], v[112:115], v[52:55]\n\t"
      "ds_read_b64_tr_b16 a[20:21], v225 offset:16384\n\t"
      "v_mfma_f32_16x16x32_bf16 v[56:59], a[48:51], v[116:119], v[56:59]\n\t"
      "ds_read_b64_tr_b16 a[22:23], v225 offset:20480\n\t"
      "v_mfma_f32_16x16x32_bf16 v[60:63], a[52:55], v[108:111], v[60:63]\n\t"
      "ds_read_b64_tr_b16 a[24:25], v226 offset:16384\n\t"
      "v_mfma_f32_16x16x32_bf16 v[64:67], a[52:55], v[112:115], v[64:67]\n\t"
      "buffer_load_dwordx4 v211, s[44:47], s18 offen offset:3072 lds\n\t"
      "v_mfma_f32_16x16x32_bf16 v[68:71], a[52:55], v[116:119], v[68:71]\n\t"
      "ds_read_b64_tr_b16 a[26:27], v226 offset:20480\n\t"
      "v_mfma_f32_16x16x32_bf16 v[72:75], a[56:59], v[108:111], v[72:75]\n\t"
      "ds_read_b64_tr_b16 a[28:29], v227 offset:16384\n\t"
      "v_mfma_f32_16x16x32_bf16 v[76:79], a[56:59], v[112:115], v[76:79]\n\t"
      "ds_read_b64_tr_b16 a[30:31], v227 offset:20480\n\t"
      "s_add_u32 s18, s18, 0x4000\n\t"
      "s_and_b32 s18, s18, 0x3ffff\n\t"
      "s_mov_b32 m0, s93\n\t"
      "v_mfma_f32_16x16x32_bf16 v[80:83], a[56:59], v[116:119], v[80:83]\n\t"
      "buffer_load_dwordx4 v208, s[44:47], s18 offen lds\n\t"
      "v_mfma_f32_16x16x32_bf16 v[84:87], a[60:63], v[108:111], v[84:87]\n\t"
      "buffer_load_dwordx4 v209, s[44:47], s18 offen offset:1024 lds\n\t"
      "v_mfma_f32_16x16x32_bf16 v[88:91], a[60:63], v[112:115], v[88:91]\n\t"
      "buffer_load_dwordx4 v210, s[44:47], s18 offen offset:2048 lds\n\t"
      "v_mfma_f32_16x16x32_bf16 v[92:95], a[60:63], v[116:119], v[92:95]\n\t"
      "buffer_load_dwordx4 v211, s[44:47], s18 offen offset:3072 lds\n\t"
      "s_add_u32 s18, s18, 0x4000\n\t"
      "s_and_b32 s18, s18, 0x3ffff\n\t"
      "s_waitcnt lgkmcnt(0)\n\t"
      "s_waitcnt vmcnt(16)\n\t"
      "v_mfma_f32_16x16x32_bf16 v[0:3], a[0:3], v[120:123], v[0:3]\n\t"
      "ds_read_b64_tr_b16 a[32:33], v220 offset:24576\n\t"
      "v_mfma_f32_16x16x32_bf16 v[4:7], a[0:3], v[124:127], v[4:7]\n\t"
      "ds_read_b64_tr_b16 a[34:35], v220 offset:28672\n\t"
      "v_mfma_f32_16x16x32_bf16 v[8:11], a[0:3], v[128:131], v[8:11]\n\t"
      "ds_read_b64_tr_b16 a[36:37], v221 offset:24576\n\t"
      "v_mfma_f32_16x16x32_bf16 v[12:15], a[4:7], v[120:123], v[12:15]\n\t"
      "ds_read_b64_tr_b16 a[38:39], v221 offset:28672\n\t"
      "s_mov_b32 m0, s94\n\t"
      "v_mfma_f32_16x16x32_bf16 v[16:19], a[4:7], v[124:127], v[16:19]\n\t"
      "buffer_load_dwordx4 v208, s[44:47], s18 offen lds\n\t"
      "v_mfma_f32_16x16x32_bf16 v[20:23], a[4:7], v[128:131], v[20:23]\n\t"
      "ds_read_b64_tr_b16 a[40:41], v222 offset:24576\n\t"
      "v_mfma_f32_16x16x32_bf16 v[24:27], a[8:11], v[120:123], v[24:27]\n\t"
      "ds_read_b64_tr_b16 a[42:43], v222 offset:28672\n\t"
      "v_mfma_f32_16x16x32_bf16 v[28:31], a[8:11], v[124:127], v[28:31]\n\t"
      "ds_read_b64_tr_b16 a[44:45], v223 offset:24576\n\t"
      "v_mfma_f32_16x16x32_bf16 v[32:35], a[8:11], v[128:131], v[32:35]\n\t"
      "buffer_load_dwordx4 v209, s[44:47], s18 offen offset:1024 lds\n\t"
      "v_mfma_f32_16x16x32_bf16 v[36:39], a[12:15], v[120:123], v[36:39]\n\t"
      "ds_read_b64_tr_b16 a[46:47], v223 offset:28672\n\t"
      "v_mfma_f32_16x16x32_bf16 v[40:43], a[12:15], v[124:127], v[40:43]\n\t"
      "ds_read_b64_tr_b16 a[48:49], v224 offset:24576\n\t"
      "v_mfma_f32_16x16x32_bf16 v[44:47], a[12:15], v[128:131], v[44:47]\n\t"
      "ds_read_b64_tr_b16 a[50:51], v224 offset:28672\n\t"
      "v_mfma_f32_16x16x32_bf16 v[48:51], a[16:19], v[120:123], v[48:51]\n\t"
      "buffer_load_dwordx4 v210, s[44:47], s18 offen offset:2048 lds\n\t"
      "v_mfma_f32_16x16x32_bf16 v[52:55], a[16:19], v[124:127], v[52:55]\n\t"
      "ds_read_b64_tr_b16 a[52:53], v225 offset:24576\n\t"
      "v_mfma_f32_16x16x32_bf16 v[56:59], a[16:19], v[128:131], v[56:59]\n\t"
      "ds_read_b64_tr_b16 a[54:55], v225 offset:28672\n\t"
      "v_mfma_f32_16x16x32_bf16 v[60:63], a[20:23], v[120:123], v[60:63]\n\t"
      "ds_read_b64_tr_b16 a[56:57], v226 offset:24576\n\t"
      "v_mfma_f32_16x16x32_bf16 v[64:67], a[20:23], v[124:127], v[64:67]\n\t"
      "buffer_load_dwordx4 v211, s[44:47], s18 offen offset:3072 lds\n\t"
      "v_mfma_f32_16x16x32_bf16 v[68:71], a[20:23], v[128:131], v[68:71]\n\t"
      "ds_read_b64_tr_b16 a[58:59], v226 offset:28672\n\t"
      "v_mfma_f32_16x16x32_bf16 v[72:75], a[24:27], v[120:123], v[72:75]\n\t"
      "ds_read_b64_tr_b16 a[60:61], v227 offset:24576\n\t"
      "v_mfma_f32_16x16x32_bf16 v[76:79], a[24:27], v[124:127], v[76:79]\n\t"
      "ds_read_b64_tr_b16 a[62:63], v227 offset:28672\n\t"
      "s_add_u32 s18, s18, 0x4000\n\t"
      "s_and_b32 s18, s18, 0x3ffff\n\t"
      "s_mov_b32 m0, s95\n\t"
      "v_mfma_f32_16x16x32_bf16 v[80:83], a[24:27], v[128:131], v[80:83]\n\t"
      "buffer_load_dwordx4 v208, s[44:47], s18 offen lds\n\t"
      "v_mfma_f32_16x16x32_bf16 v[84:87], a[28:31], v[120:123], v[84:87]\n\t"
      "buffer_load_dwordx4 v209, s[44:47], s18 offen offset:1024 lds\n\t"
      "v_mfma_f32_16x16x32_bf16 v[88:91], a[28:31], v[124:127], v[88:91]\n\t"
      "buffer_load_dwordx4 v210, s[44:47], s18 offen offset:2048 lds\n\t"
      "v_mfma_f32_16x16x32_bf16 v[92:95], a[28:31], v[128:131], v[92:95]\n\t"
      "buffer_load_dwordx4 v211, s[44:47], s18 offen offset:3072 lds\n\t"
      "s_add_u32 s18, s18, 0x4000\n\t"
      "s_and_b32 s18, s18, 0x3ffff\n\t"
      "s_waitcnt lgkmcnt(0)\n\t"
      "s_waitcnt vmcnt(16)\n\t"
      "v_mfma_f32_16x16x32_bf16 v[0:3], a[32:35], v[132:135], v[0:3]\n\t"
      "ds_read_b64_tr_b16 a[0:1], v220\n\t"
      "v_mfma_f32_16x16x32_bf16 v[4:7], a[32:35], v[136:139], v[4:7]\n\t"
      "ds_read_b64_tr_b16 a[2:3], v220 offset:4096\n\t"
      "v_mfma_f32_16x16x32_bf16 v[8:11], a[32:35], v[140:143], v[8:11]\n\t"
      "ds_read_b64_tr_b16 a[4:5], v221\n\t"
      "v_mfma_f32_16x16x32_bf16 v[12:15], a[36:39], v[132:135], v[12:15]\n\t"
      "ds_read_b64_tr_b16 a[6:7], v221 offset:4096\n\t"
      "s_mov_b32 m0, s96\n\t"
      "v_mfma_f32_16x16x32_bf16 v[16:19], a[36:39], v[136:139], v[16:19]\n\t"
      "buffer_load_dwordx4 v208, s[44:47], s18 offen lds\n\t"
      "v_mfma_f32_16x16x32_bf16 v[20:23], a[36:39], v[140:143], v[20:23]\n\t"
      "ds_read_b64_tr_b16 a[8:9], v222\n\t"
      "v_mfma_f32_16x16x32_bf16 v[24:27], a[40:43], v[132:135], v[24:27]\n\t"
      "ds_read_b64_tr_b16 a[10:11], v222 offset:4096\n\t"
      "v_mfma_f32_16x16x32_bf16 v[28:31], a[40:43], v[136:139], v[28:31]\n\t"
      "ds_read_b64_tr_b16 a[12:13], v223\n\t"
      "v_mfma_f32_16x16x32_bf16 v[32:35], a[40:43], v[140:143], v[32:35]\n\t"
      "buffer_load_dwordx4 v209, s[44:47], s18 offen offset:1024 lds\n\t"
      "v_mfma_f32_16x16x32_bf16 v[36:39], a[44:47], v[132:135], v[36:39]\n\t"
      "ds_read_b64_tr_b16 a[14:15], v223 offset:4096\n\t"
      "v_mfma_f32_16x16x32_bf16 v[40:43], a[44:47], v[136:139], v[40:43]\n\t"
      "ds_read_b64_tr_b16 a[16:17], v224\n\t"
      "v_mfma_f32_16x16x32_bf16 v[44:47], a[44:47], v[140:143], v[44:47]\n\t"
      "ds_read_b64_tr_b16 a[18:19], v224 offset:4096\n\t"
      "v_mfma_f32_16x16x32_bf16 v[48:51], a[48:51], v[132:135], v[48:51]\n\t"
      "buffer_load_dwordx4 v210, s[44:47], s18 offen offset:2048 lds\n\t"
      "v_mfma_f32_16x16x32_bf16 v[52:55], a[48:51], v[136:139], v[52:55]\n\t"
      "ds_read_b64_tr_b16 a[20:21], v225\n\t"
      "v_mfma_f32_16x16x32_bf16 v[56:59], a[48:51], v[140:143], v[56:59]\n\t"
      "ds_read_b64_tr_b16 a[22:23], v225 offset:4096\n\t"
      "v_mfma_f32_16x16x32_bf16 v[60:63], a[52:55], v[132:135], v[60:63]\n\t"
      "ds_read_b64_tr_b16 a[24:25], v226\n\t"
      "v_mfma_f32_16x16x32_bf16 v[64:67], a[52:55], v[136:139], v[64:67]\n\t"
      "buffer_load_dwordx4 v211, s[44:47], s18 offen offset:3072 lds\n\t"
      "v_mfma_f32_16x16x32_bf16 v[68:71], a[52:55], v[140:143], v[68:71]\n\t"
      "ds_read_b64_tr_b16 a[26:27], v226 offset:4096\n\t"
      "v_mfma_f32_16x16x32_bf16 v[72:75], a[56:59], v[132:135], v[72:75]\n\t"
      "ds_read_b64_tr_b16 a[28:29], v227\n\t"
      "v_mfma_f32_16x16x32_bf16 v[76:79], a[56:59], v[136:139], v[76:79]\n\t"
      "ds_read_b64_tr_b16 a[30:31], v227 offset:4096\n\t"
      "s_add_u32 s18, s18, 0x4000\n\t"
      "s_and_b32 s18, s18, 0x3ffff\n\t"
      "s_mov_b32 m0, s97\n\t"
      "v_mfma_f32_16x16x32_bf16 v[80:83], a[56:59], v[140:143], v[80:83]\n\t"
      "buffer_load_dwordx4 v208, s[44:47], s18 offen lds\n\t"
      "v_mfma_f32_16x16x32_bf16 v[84:87], a[60:63], v[132:135], v[84:87]\n\t"
      "buffer_load_dwordx4 v209, s[44:47], s18 offen offset:1024 lds\n\t"
      "v_mfma_f32_16x16x32_bf16 v[88:91], a[60:63], v[136:139], v[88:91]\n\t"
      "buffer_load_dwordx4 v210, s[44:47], s18 offen offset:2048 lds\n\t"
      "v_mfma_f32_16x16x32_bf16 v[92:95], a[60:63], v[140:143], v[92:95]\n\t"
      "buffer_load_dwordx4 v211, s[44:47], s18 offen offset:3072 lds\n\t"
      "s_add_u32 s18, s18, 0x4000\n\t"
      "s_and_b32 s18, s18, 0x3ffff\n\t"
      "s_waitcnt lgkmcnt(0)\n\t"
      "s_waitcnt vmcnt(16)\n\t"
      "v_mfma_f32_16x16x32_bf16 v[0:3], a[0:3], v[144:147], v[0:3]\n\t"
      "ds_read_b64_tr_b16 a[32:33], v220 offset:8192\n\t"
      "v_mfma_f32_16x16x32_bf16 v[4:7], a[0:3], v[148:151], v[4:7]\n\t"
      "ds_read_b64_tr_b16 a[34:35], v220 offset:12288\n\t"
      "v_mfma_f32_16x16x32_bf16 v[8:11], a[0:3], v[152:155], v[8:11]\n\t"
      "ds_read_b64_tr_b16 a[36:37], v221 offset:8192\n\t"
      "v_mfma_f32_16x16x32_bf16 v[12:15], a[4:7], v[144:147], v[12:15]\n\t"
      "ds_read_b64_tr_b16 a[38:39], v221 offset:12288\n\t"
      "v_mfma_f32_16x16x32_bf16 v[16:19], a[4:7], v[148:151], v[16:19]\n\t"
      "v_mfma_f32_16x16x32_bf16 v[20:23], a[4:7], v[152:155], v[20:23]\n\t"
      "ds_read_b64_tr_b16 a[40:41], v222 offset:8192\n\t"
      "v_mfma_f32_16x16x32_bf16 v[24:27], a[8:11], v[144:147], v[24:27]\n\t"
      "ds_read_b64_tr_b16 a[42:43], v222 offset:12288\n\t"
      "v_mfma_f32_16x16x32_bf16 v[28:31], a[8:11], v[148:151], v[28:31]\n\t"
      "ds_read_b64_tr_b16 a[44:45], v223 offset:8192\n\t"
      "v_mfma_f32_16x16x32_bf16 v[32:35], a[8:11], v[152:155], v[32:35]\n\t"
      "v_mfma_f32_16x16x32_bf16 v[36:39], a[12:15], v[144:147], v[36:39]\n\t"
      "ds_read_b64_tr_b16 a[46:47], v223 offset:12288\n\t"
      "v_mfma_f32_16x16x32_bf16 v[40:43], a[12:15], v[148:151], v[40:43]\n\t"
      "ds_read_b64_tr_b16 a[48:49], v224 offset:8192\n\t"
      "v_mfma_f32_16x16x32_bf16 v[44:47], a[12:15], v[152:155], v[44:47]\n\t"
      "ds_read_b64_tr_b16 a[50:51], v224 offset:12288\n\t"
      "v_mfma_f32_16x16x32_bf16 v[48:51], a[16:19], v[144:147], v[48:51]\n\t"
      "v_mfma_f32_16x16x32_bf16 v[52:55], a[16:19], v[148:151], v[52:55]\n\t"
      "ds_read_b64_tr_b16 a[52:53], v225 offset:8192\n\t"
      "v_mfma_f32_16x16x32_bf16 v[56:59], a[16:19], v[152:155], v[56:59]\n\t"
      "ds_read_b64_tr_b16 a[54:55], v225 offset:12288\n\t"
      "v_mfma_f32_16x16x32_bf16 v[60:63], a[20:23], v[144:147], v[60:63]\n\t"
      "ds_read_b64_tr_b16 a[56:57], v226 offset:8192\n\t"
      "v_mfma_f32_16x16x32_bf16 v[64:67], a[20:23], v[148:151], v[64:67]\n\t"
      "v_mfma_f32_16x16x32_bf16 v[68:71], a[20:23], v[152:155], v[68:71]\n\t"
      "ds_read_b64_tr_b16 a[58:59], v226 offset:12288\n\t"
      "v_mfma_f32_16x16x32_bf16 v[72:75], a[24:27], v[144:147], v[72:75]\n\t"
      "ds_read_b64_tr_b16 a[60:61], v227 offset:8192\n\t"
      "v_mfma_f32_16x16x32_bf16 v[76:79], a[24:27], v[148:151], v[76:79]\n\t"
      "ds_read_b64_tr_b16 a[62:63], v227 offset:12288\n\t"
      "v_mfma_f32_16x16x32_bf16 v[80:83], a[24:27], v[152:155], v[80:83]\n\t"
      "v_mfma_f32_16x16x32_bf16 v[84:87], a[28:31], v[144:147], v[84:87]\n\t"
      "s_lshl_b32 s19, s11, 3\n\t"
      "s_lshl_b32 s22, s6, 1\n\t"
      "s_add_u32 s19, s19, s22\n\t"
      "s_lshl_b32 s22, s19, 3\n\t"
      "s_add_u32 s22, s22, s10\n\t"
      "s_mul_i32 s22, s22, 6144\n\t"
      "s_add_u32 s22, s22, 131072\n\t"
      "s_add_u32 s48, s78, s22\n\t"
      "s_addc_u32 s49, s79, 0\n\t"
      "s_sub_u32 s50, 0xc20000, s22\n\t"
      "s_mov_b32 s51, 0x20000\n\t"
      "v_mfma_f32_16x16x32_bf16 v[88:91], a[28:31], v[148:151], v[88:91]\n\t"
      "s_lshl_b32 s19, s19, 7\n\t"
      "s_lshl_b32 s22, s10, 3\n\t"
      "s_add_u32 s19, s19, s22\n\t"
      "s_add_u32 s19, s19, 8192\n\t"
      "v_lshl_add_u32 v254, v216, 3, s19\n\t"
      "v_mov_b32 v252, s4\n\t"
      "v_mov_b32 v253, s5\n\t"
      "v_mfma_f32_16x16x32_bf16 v[92:95], a[28:31], v[152:155], v[92:95]\n\t"
      "s_waitcnt lgkmcnt(0)\n\t"
      "s_waitcnt vmcnt(8)\n\t"
      "v_mfma_f32_16x16x32_bf16 v[0:3], a[32:35], v[156:159], v[0:3]\n\t"
      "ds_read_b64_tr_b16 a[0:1], v220 offset:16384\n\t"
      "v_mov_b32 v228, 0\n\t"
      "v_mfma_f32_16x16x32_bf16 v[4:7], a[32:35], v[160:163], v[4:7]\n\t"
      "ds_read_b64_tr_b16 a[2:3], v220 offset:20480\n\t"
      "v_mov_b32 v229, 0\n\t"
      "v_mfma_f32_16x16x32_bf16 v[8:11], a[32:35], v[164:167], v[8:11]\n\t"
      "ds_read_b64_tr_b16 a[4:5], v221 offset:16384\n\t"
      "v_mov_b32 v232, 0\n\t"
      "v_mfma_f32_16x16x32_bf16 v[12:15], a[36:39], v[156:159], v[12:15]\n\t"
      "ds_read_b64_tr_b16 a[6:7], v221 offset:20480\n\t"
      "v_mov_b32 v233, 0\n\t"
      "v_mfma_f32_16x16x32_bf16 v[16:19], a[36:39], v[160:163], v[16:19]\n\t"
      "v_lshrrev_b32 v234, 8, v216\n\t"
      "v_mfma_f32_16x16x32_bf16 v[20:23], a[36:39], v[164:167], v[20:23]\n\t"
      "ds_read_b64_tr_b16 a[8:9], v222 offset:16384\n\t"
      "v_bfe_u32 v235, v216, 6, 2\n\t"
      "v_mfma_f32_16x16x32_bf16 v[24:27], a[40:43], v[156:159], v[24:27]\n\t"
      "ds_read_b64_tr_b16 a[10:11], v222 offset:20480\n\t"
      "v_mfma_f32_16x16x32_bf16 v[28:31], a[40:43], v[160:163], v[28:31]\n\t"
      "ds_read_b64_tr_b16 a[12:13], v223 offset:16384\n\t"
      "v_and_b32 v230, 16, v216\n\t"
      "v_mfma_f32_16x16x32_bf16 v[32:35], a[40:43], v[164:167], v[32:35]\n\t"
      "s_movk_i32 s23, 0x3f80\n\t"
      "v_mfma_f32_16x16x32_bf16 v[36:39], a[44:47], v[156:159], v[36:39]\n\t"
      "ds_read_b64_tr_b16 a[14:15], v223 offset:20480\n\t"
      "v_lshlrev_b32 v230, v230, s23\n\t"
      "v_mfma_f32_16x16x32_bf16 v[40:43], a[44:47], v[160:163], v[40:43]\n\t"
      "ds_read_b64_tr_b16 a[16:17], v224 offset:16384\n\t"
      "v_cmp_eq_u32 vcc, v234, v235\n\t"
      "v_mfma_f32_16x16x32_bf16 v[44:47], a[44:47], v[164:167], v[44:47]\n\t"
      "ds_read_b64_tr_b16 a[18:19], v224 offset:20480\n\t"
      "v_and_b32 v234, 32, v216\n\t"
      "v_mfma_f32_16x16x32_bf16 v[48:51], a[48:51], v[156:159], v[48:51]\n\t"
      "s_nop 0\n\t"
      "v_mfma_f32_16x16x32_bf16 v[52:55], a[48:51], v[160:163], v[52:55]\n\t"
      "ds_read_b64_tr_b16 a[20:21], v225 offset:16384\n\t"
      "v_mfma_f32_16x16x32_bf16 v[56:59], a[48:51], v[164:167], v[56:59]\n\t"
      "ds_read_b64_tr_b16 a[22:23], v225 offset:20480\n\t"
      "v_cndmask_b32 v230, 0, v230, vcc\n\t"
      "v_mfma_f32_16x16x32_bf16 v[60:63], a[52:55], v[156:159], v[60:63]\n\t"
      "ds_read_b64_tr_b16 a[24:25], v226 offset:16384\n\t"
      "v_cmp_eq_u32 vcc, 0, v234\n\t"
      "v_mfma_f32_16x16x32_bf16 v[64:67], a[52:55], v[160:163], v[64:67]\n\t"
      "s_nop 1\n\t"
      "v_mfma_f32_16x16x32_bf16 v[68:71], a[52:55], v[164:167], v[68:71]\n\t"
      "ds_read_b64_tr_b16 a[26:27], v226 offset:20480\n\t"
      "v_cndmask_b32 v231, v230, v228, vcc\n\t"
      "v_mfma_f32_16x16x32_bf16 v[72:75], a[56:59], v[156:159], v[72:75]\n\t"
      "ds_read_b64_tr_b16 a[28:29], v227 offset:16384\n\t"
      "v_cndmask_b32 v230, 0, v230, vcc\n\t"
      "v_mfma_f32_16x16x32_bf16 v[76:79], a[56:59], v[160:163], v[76:79]\n\t"
      "ds_read_b64_tr_b16 a[30:31], v227 offset:20480\n\t"
      "v_mfma_f32_16x16x32_bf16 v[80:83], a[56:59], v[164:167], v[80:83]\n\t"
      "v_mfma_f32_16x16x32_bf16 v[84:87], a[60:63], v[156:159], v[84:87]\n\t"
      "s_lshl_b32 s19, s11, 3\n\t"
      "s_add_u32 s19, s19, s10\n\t"
      "s_lshl_b32 s22, s19, 7\n\t"
      "s_add_u32 s22, s22, 8192\n\t"
      "s_add_u32 s32, s78, s22\n\t"
      "s_addc_u32 s33, s79, 0\n\t"
      "s_mul_i32 s19, s19, 49152\n\t"
      "s_lshl_b32 s22, s6, 10\n\t"
      "s_add_u32 s19, s19, s22\n\t"
      "s_add_u32 s19, s19, 131072\n\t"
      "s_add_u32 s36, s78, s19\n\t"
      "s_addc_u32 s37, s79, 0\n\t"
      "s_sub_u32 s38, 0xc20000, s19\n\t"
      "s_mov_b32 s39, 0x20000\n\t"
      "v_mfma_f32_16x16x32_bf16 v[88:91], a[60:63], v[160:163], v[88:91]\n\t"
      "v_mfma_f32_16x16x32_bf16 v[92:95], a[60:63], v[164:167], v[92:95]\n\t"
      "s_waitcnt lgkmcnt(0)\n\t"
      "s_waitcnt vmcnt(0)\n\t"
      "v_mfma_f32_16x16x32_bf16 v[0:3], a[0:3], v[168:171], v[0:3]\n\t"
      "ds_read_b64_tr_b16 a[32:33], v220 offset:24576\n\t"
      "v_mfma_f32_16x16x32_bf16 v[4:7], a[0:3], v[172:175], v[4:7]\n\t"
      "ds_read_b64_tr_b16 a[34:35], v220 offset:28672\n\t"
      "v_mfma_f32_16x16x32_bf16 v[8:11], a[0:3], v[176:179], v[8:11]\n\t"
      "ds_read_b64_tr_b16 a[36:37], v221 offset:24576\n\t"
      "v_mfma_f32_16x16x32_bf16 v[12:15], a[4:7], v[168:171], v[12:15]\n\t"
      "ds_read_b64_tr_b16 a[38:39], v221 offset:28672\n\t"
      "v_mfma_f32_16x16x32_bf16 v[16:19], a[4:7], v[172:175], v[16:19]\n\t"
      "v_mfma_f32_16x16x32_bf16 v[20:23], a[4:7], v[176:179], v[20:23]\n\t"
      "ds_read_b64_tr_b16 a[40:41], v222 offset:24576\n\t"
      "v_mfma_f32_16x16x32_bf16 v[24:27], a[8:11], v[168:171], v[24:27]\n\t"
      "ds_read_b64_tr_b16 a[42:43], v222 offset:28672\n\t"
      "v_mfma_f32_16x16x32_bf16 v[28:31], a[8:11], v[172:175], v[28:31]\n\t"
      "ds_read_b64_tr_b16 a[44:45], v223 offset:24576\n\t"
      "v_mfma_f32_16x16x32_bf16 v[32:35], a[8:11], v[176:179], v[32:35]\n\t"
      "v_mfma_f32_16x16x32_bf16 v[36:39], a[12:15], v[168:171], v[36:39]\n\t"
      "ds_read_b64_tr_b16 a[46:47], v223 offset:28672\n\t"
      "v_mfma_f32_16x16x32_bf16 v[40:43], a[12:15], v[172:175], v[40:43]\n\t"
      "ds_read_b64_tr_b16 a[48:49], v224 offset:24576\n\t"
      "v_mfma_f32_16x16x32_bf16 v[44:47], a[12:15], v[176:179], v[44:47]\n\t"
      "ds_read_b64_tr_b16 a[50:51], v224 offset:28672\n\t"
      "v_mfma_f32_16x16x32_bf16 v[48:51], a[16:19], v[168:171], v[48:51]\n\t"
      "v_mfma_f32_16x16x32_bf16 v[52:55], a[16:19], v[172:175], v[52:55]\n\t"
      "ds_read_b64_tr_b16 a[52:53], v225 offset:24576\n\t"
      "v_mfma_f32_16x16x32_bf16 v[56:59], a[16:19], v[176:179], v[56:59]\n\t"
      "ds_read_b64_tr_b16 a[54:55], v225 offset:28672\n\t"
      "v_mfma_f32_16x16x32_bf16 v[60:63], a[20:23], v[168:171], v[60:63]\n\t"
      "ds_read_b64_tr_b16 a[56:57], v226 offset:24576\n\t"
      "v_mfma_f32_16x16x32_bf16 v[64:67], a[20:23], v[172:175], v[64:67]\n\t"
      "v_mfma_f32_16x16x32_bf16 v[68:71], a[20:23], v[176:179], v[68:71]\n\t"
      "ds_read_b64_tr_b16 a[58:59], v226 offset:28672\n\t"
      "v_mfma_f32_16x16x32_bf16 v[72:75], a[24:27], v[168:171], v[72:75]\n\t"
      "ds_read_b64_tr_b16 a[60:61], v227 offset:24576\n\t"
      "v_mfma_f32_16x16x32_bf16 v[76:79], a[24:27], v[172:175], v[76:79]\n\t"
      "ds_read_b64_tr_b16 a[62:63], v227 offset:28672\n\t"
      "v_mfma_f32_16x16x32_bf16 v[80:83], a[24:27], v[176:179], v[80:83]\n\t"
      "v_mfma_f32_16x16x32_bf16 v[84:87], a[28:31], v[168:171], v[84:87]\n\t"
      "s_mul_i32 s19, s9, 98304\n\t"
      "s_lshl_b32 s22, s12, 10\n\t"
      "s_add_u32 s19, s19, s22\n\t"
      "s_lshl_b32 s22, s10, 7\n\t"
      "s_add_u32 s19, s19, s22\n\t"
      "s_lshl_b32 s22, s6, 15\n\t"
      "s_add_u32 s19, s19, s22\n\t"
      "s_add_u32 s40, s76, s19\n\t"
      "s_addc_u32 s41, s77, 0\n\t"
      "s_sub_u32 s42, 0x180000, s19\n\t"
      "s_mov_b32 s43, 0x20000\n\t"
      "v_mfma_f32_16x16x32_bf16 v[88:91], a[28:31], v[172:175], v[88:91]\n\t"
      "v_and_b32 v247, 0xf0, v216\n\t"
      "v_lshrrev_b32 v246, 4, v216\n\t"
      "v_and_b32 v246, 48, v246\n\t"
      "v_lshl_add_u32 v246, v247, 7, v246\n\t"
      "v_and_b32 v247, 0x70, v216\n\t"
      "v_lshrrev_b32 v247, 1, v247\n\t"
      "v_mfma_f32_16x16x32_bf16 v[92:95], a[28:31], v[176:179], v[92:95]\n\t"
      "s_waitcnt lgkmcnt(0)\n\t"
      "v_mfma_f32_16x16x32_bf16 v[0:3], a[32:35], v[180:183], v[0:3]\n\t"
      "v_mfma_f32_16x16x32_bf16 v[4:7], a[32:35], v[184:187], v[4:7]\n\t"
      "v_mfma_f32_16x16x32_bf16 v[8:11], a[32:35], v[188:191], v[8:11]\n\t"
      "v_mfma_f32_16x16x32_bf16 v[12:15], a[36:39], v[180:183], v[12:15]\n\t"
      "v_mfma_f32_16x16x32_bf16 v[16:19], a[36:39], v[184:187], v[16:19]\n\t"
      "v_mfma_f32_16x16x32_bf16 v[20:23], a[36:39], v[188:191], v[20:23]\n\t"
      "v_mfma_f32_16x16x32_bf16 v[24:27], a[40:43], v[180:183], v[24:27]\n\t"
      "v_mfma_f32_16x16x32_bf16 v[28:31], a[40:43], v[184:187], v[28:31]\n\t"
      "v_mfma_f32_16x16x32_bf16 v[32:35], a[40:43], v[188:191], v[32:35]\n\t"
      "v_mfma_f32_16x16x32_bf16 v[36:39], a[44:47], v[180:183], v[36:39]\n\t"
      "v_cvt_pk_bf16_f32 v96, v0, v1\n\t"
      "v_cvt_pk_bf16_f32 v97, v2, v3\n\t"
      "v_cvt_pk_bf16_f32 v98, v12, v13\n\t"
      "v_mfma_f32_16x16x32_bf16 v[40:43], a[44:47], v[184:187], v[40:43]\n\t"
      "v_cvt_pk_bf16_f32 v99, v14, v15\n\t"
      "v_mfma_f32_16x16x32_bf16 v[44:47], a[44:47], v[188:191], v[44:47]\n\t"
      "s_mov_b32 s24, 0x0\n\t"
      "buffer_store_dwordx4 v[96:99], v216, s[48:51], s24 offen sc1\n\t"
      "v_cvt_pk_bf16_f32 v100, v4, v5\n\t"
      "v_cvt_pk_bf16_f32 v101, v6, v7\n\t"
      "v_cvt_pk_bf16_f32 v102, v16, v17\n\t"
      "v_mfma_f32_16x16x32_bf16 v[48:51], a[48:51], v[180:183], v[48:51]\n\t"
      "v_cvt_pk_bf16_f32 v103, v18, v19\n\t"
      "v_mfma_f32_16x16x32_bf16 v[52:55], a[48:51], v[184:187], v[52:55]\n\t"
      "buffer_store_dwordx4 v[100:103], v216, s[48:51], s24 offen offset:1024 sc1\n\t"
      "v_cvt_pk_bf16_f32 v104, v8, v9\n\t"
      "v_cvt_pk_bf16_f32 v105, v10, v11\n\t"
      "v_cvt_pk_bf16_f32 v106, v20, v21\n\t"
      "v_mfma_f32_16x16x32_bf16 v[56:59], a[48:51], v[188:191], v[56:59]\n\t"
      "v_cvt_pk_bf16_f32 v107, v22, v23\n\t"
      "v_mfma_f32_16x16x32_bf16 v[60:63], a[52:55], v[180:183], v[60:63]\n\t"
      "buffer_store_dwordx4 v[104:107], v216, s[48:51], s24 offen offset:2048 sc1\n\t"
      "v_cvt_pk_bf16_f32 v108, v24, v25\n\t"
      "v_cvt_pk_bf16_f32 v109, v26, v27\n\t"
      "v_cvt_pk_bf16_f32 v110, v36, v37\n\t"
      "v_mfma_f32_16x16x32_bf16 v[64:67], a[52:55], v[184:187], v[64:67]\n\t"
      "v_cvt_pk_bf16_f32 v111, v38, v39\n\t"
      "v_mfma_f32_16x16x32_bf16 v[68:71], a[52:55], v[188:191], v[68:71]\n\t"
      "s_mov_b32 s24, 0xc00\n\t"
      "buffer_store_dwordx4 v[108:111], v216, s[48:51], s24 offen sc1\n\t"
      "v_cvt_pk_bf16_f32 v112, v28, v29\n\t"
      "v_cvt_pk_bf16_f32 v113, v30, v31\n\t"
      "v_cvt_pk_bf16_f32 v114, v40, v41\n\t"
      "v_mfma_f32_16x16x32_bf16 v[72:75], a[56:59], v[180:183], v[72:75]\n\t"
      "v_cvt_pk_bf16_f32 v115, v42, v43\n\t"
      "v_mfma_f32_16x16x32_bf16 v[76:79], a[56:59], v[184:187], v[76:79]\n\t"
      "buffer_store_dwordx4 v[112:115], v216, s[48:51], s24 offen offset:1024 sc1\n\t"
      "v_cvt_pk_bf16_f32 v116, v32, v33\n\t"
      "v_cvt_pk_bf16_f32 v117, v34, v35\n\t"
      "v_cvt_pk_bf16_f32 v118, v44, v45\n\t"
      "v_mfma_f32_16x16x32_bf16 v[80:83], a[56:59], v[188:191], v[80:83]\n\t"
      "v_cvt_pk_bf16_f32 v119, v46, v47\n\t"
      "v_mfma_f32_16x16x32_bf16 v[84:87], a[60:63], v[180:183], v[84:87]\n\t"
      "buffer_store_dwordx4 v[116:119], v216, s[48:51], s24 offen offset:2048 sc1\n\t"
      "v_cvt_pk_bf16_f32 v120, v48, v49\n\t"
      "v_cvt_pk_bf16_f32 v121, v50, v51\n\t"
      "v_cvt_pk_bf16_f32 v122, v60, v61\n\t"
      "v_cvt_pk_bf16_f32 v123, v62, v63\n\t"
      "v_mfma_f32_16x16x32_bf16 v[88:91], a[60:63], v[184:187], v[88:91]\n\t"
      "s_mov_b32 s24, 0xc000\n\t"
      "buffer_store_dwordx4 v[120:123], v216, s[48:51], s24 offen sc1\n\t"
      "v_cvt_pk_bf16_f32 v124, v52, v53\n\t"
      "v_cvt_pk_bf16_f32 v125, v54, v55\n\t"
      "v_cvt_pk_bf16_f32 v126, v64, v65\n\t"
      "v_cvt_pk_bf16_f32 v127, v66, v67\n\t"
      "v_mfma_f32_16x16x32_bf16 v[92:95], a[60:63], v[188:191], v[92:95]\n\t"
      "buffer_store_dwordx4 v[124:127], v216, s[48:51], s24 offen offset:1024 sc1\n\t"
      "v_cvt_pk_bf16_f32 v128, v56, v57\n\t"
      "v_cvt_pk_bf16_f32 v129, v58, v59\n\t"
      "v_cvt_pk_bf16_f32 v130, v68, v69\n\t"
      "v_cvt_pk_bf16_f32 v131, v70, v71\n\t"
      "buffer_store_dwordx4 v[128:131], v216, s[48:51], s24 offen offset:2048 sc1\n\t"
      "v_cvt_pk_bf16_f32 v132, v72, v73\n\t"
      "v_cvt_pk_bf16_f32 v133, v74, v75\n\t"
      "v_cvt_pk_bf16_f32 v134, v84, v85\n\t"
      "v_cvt_pk_bf16_f32 v135, v86, v87\n\t"
      "s_mov_b32 s24, 0xcc00\n\t"
      "buffer_store_dwordx4 v[132:135], v216, s[48:51], s24 offen sc1\n\t"
      "v_cvt_pk_bf16_f32 v136, v76, v77\n\t"
      "v_cvt_pk_bf16_f32 v137, v78, v79\n\t"
      "v_cvt_pk_bf16_f32 v138, v88, v89\n\t"
      "v_cvt_pk_bf16_f32 v139, v90, v91\n\t"
      "buffer_store_dwordx4 v[136:139], v216, s[48:51], s24 offen offset:1024 sc1\n\t"
      "v_cvt_pk_bf16_f32 v140, v80, v81\n\t"
      "v_cvt_pk_bf16_f32 v141, v82, v83\n\t"
      "v_cvt_pk_bf16_f32 v142, v92, v93\n\t"
      "v_cvt_pk_bf16_f32 v143, v94, v95\n\t"
      "buffer_store_dwordx4 v[140:143], v216, s[48:51], s24 offen offset:2048 sc1\n\t"
      "s_mov_b32 s24, 0x100000\n\t"
      "s_waitcnt vmcnt(6)\n\t"
      "s_mov_b64 exec, 1\n\t"
      "global_store_dwordx2 v254, v[252:253], s[78:79] sc1\n\t"
      "s_waitcnt vmcnt(1)\n\t"
      "s_mov_b64 exec, 2\n\t"
      "global_store_dwordx2 v254, v[252:253], s[78:79] sc1\n\t"
      "s_mov_b64 exec, -1\n\t"
      "s_cmp_eq_u32 s6, 3\n\t"
      "s_cbranch_scc1 L_done_%=\n\t"
      "L_poll_%=:\n\t"
      "global_load_dwordx2 v[248:249], v247, s[32:33] sc1\n\t"
      "s_waitcnt vmcnt(0)\n\t"
      "v_cmp_ne_u64 vcc, s[4:5], v[248:249]\n\t"
      "s_nop 3\n\t"
      "s_cbranch_vccz L_go_%=\n\t"
      "s_sub_u32 s24, s24, 1\n\t"
      "s_cmp_lg_u32 s24, 0\n\t"
      "s_cbranch_scc1 L_poll_%=\n\t"
      "L_go_%=:\n\t"
      "s_mov_b32 s19, 0\n\t"
      "buffer_load_dwordx4 v[0:3], v216, s[36:39], s19 offen sc1\n\t"
      "buffer_load_dwordx4 v[4:7], v216, s[36:39], s19 offen offset:3072 sc1\n\t"
      "s_add_u32 s19, s19, 6144\n\t"
      "buffer_load_dwordx4 v[8:11], v216, s[36:39], s19 offen sc1\n\t"
      "buffer_load_dwordx4 v[12:15], v216, s[36:39], s19 offen offset:3072 sc1\n\t"
      "s_add_u32 s19, s19, 6144\n\t"
      "buffer_load_dwordx4 v[16:19], v216, s[36:39], s19 offen sc1\n\t"
      "buffer_load_dwordx4 v[20:23], v216, s[36:39], s19 offen offset:3072 sc1\n\t"
      "s_add_u32 s19, s19, 6144\n\t"
      "buffer_load_dwordx4 v[24:27], v216, s[36:39], s19 offen sc1\n\t"
      "buffer_load_dwordx4 v[28:31], v216, s[36:39], s19 offen offset:3072 sc1\n\t"
      "s_add_u32 s19, s19, 6144\n\t"
      "buffer_load_dwordx4 v[32:35], v216, s[36:39], s19 offen sc1\n\t"
      "buffer_load_dwordx4 v[36:39], v216, s[36:39], s19 offen offset:3072 sc1\n\t"
      "s_add_u32 s19, s19, 6144\n\t"
      "buffer_load_dwordx4 v[40:43], v216, s[36:39], s19 offen sc1\n\t"
      "buffer_load_dwordx4 v[44:47], v216, s[36:39], s19 offen offset:3072 sc1\n\t"
      "s_add_u32 s19, s19, 6144\n\t"
      "buffer_load_dwordx4 v[48:51], v216, s[36:39], s19 offen sc1\n\t"
      "buffer_load_dwordx4 v[52:55], v216, s[36:39], s19 offen offset:3072 sc1\n\t"
      "s_add_u32 s19, s19, 6144\n\t"
      "buffer_load_dwordx4 v[56:59], v216, s[36:39], s19 offen sc1\n\t"
      "buffer_load_dwordx4 v[60:63], v216, s[36:39], s19 offen offset:3072 sc1\n\t"
      "s_barrier\n\t"
      "s_waitcnt vmcnt(15)\n\t"
      "v_mfma_f32_16x16x32_bf16 v[64:67], v[230:233], v[0:3], 0\n\t"
      "v_mfma_f32_16x16x32_bf16 v[68:71], v[228:231], v[0:3], 0\n\t"
      "s_waitcnt vmcnt(14)\n\t"
      "v_mfma_f32_16x16x32_bf16 v[72:75], v[230:233], v[4:7], 0\n\t"
      "v_mfma_f32_16x16x32_bf16 v[76:79], v[228:231], v[4:7], 0\n\t"
      "s_waitcnt vmcnt(13)\n\t"
      "v_mfma_f32_16x16x32_bf16 v[64:67], v[230:233], v[8:11], v[64:67]\n\t"
      "v_mfma_f32_16x16x32_bf16 v[68:71], v[228:231], v[8:11], v[68:71]\n\t"
      "s_waitcnt vmcnt(12)\n\t"
      "v_mfma_f32_16x16x32_bf16 v[72:75], v[230:233], v[12:15], v[72:75]\n\t"
      "v_mfma_f32_16x16x32_bf16 v[76:79], v[228:231], v[12:15], v[76:79]\n\t"
      "s_waitcnt vmcnt(11)\n\t"
      "v_mfma_f32_16x16x32_bf16 v[64:67], v[230:233], v[16:19], v[64:67]\n\t"
      "v_mfma_f32_16x16x32_bf16 v[68:71], v[228:231], v[16:19], v[68:71]\n\t"
      "s_waitcnt vmcnt(10)\n\t"
      "v_mfma_f32_16x16x32_bf16 v[72:75], v[230:233], v[20:23], v[72:75]\n\t"
      "v_mfma_f32_16x16x32_bf16 v[76:79], v[228:231], v[20:23], v[76:79]\n\t"
      "s_waitcnt vmcnt(9)\n\t"
      "v_mfma_f32_16x16x32_bf16 v[64:67], v[230:233], v[24:27], v[64:67]\n\t"
      "v_mfma_f32_16x16x32_bf16 v[68:71], v[228:231], v[24:27], v[68:71]\n\t"
      "s_waitcnt vmcnt(8)\n\t"
      "v_mfma_f32_16x16x32_bf16 v[72:75], v[230:233], v[28:31], v[72:75]\n\t"
      "v_mfma_f32_16x16x32_bf16 v[76:79], v[228:231], v[28:31], v[76:79]\n\t"
      "s_waitcnt vmcnt(7)\n\t"
      "v_mfma_f32_16x16x32_bf16 v[64:67], v[230:233], v[32:35], v[64:67]\n\t"
      "v_mfma_f32_16x16x32_bf16 v[68:71], v[228:231], v[32:35], v[68:71]\n\t"
      "s_waitcnt vmcnt(6)\n\t"
      "v_mfma_f32_16x16x32_bf16 v[72:75], v[230:233], v[36:39], v[72:75]\n\t"
      "v_mfma_f32_16x16x32_bf16 v[76:79], v[228:231], v[36:39], v[76:79]\n\t"
      "s_waitcnt vmcnt(5)\n\t"
      "v_mfma_f32_16x16x32_bf16 v[64:67], v[230:233], v[40:43], v[64:67]\n\t"
      "v_mfma_f32_16x16x32_bf16 v[68:71], v[228:231], v[40:43], v[68:71]\n\t"
      "s_waitcnt vmcnt(4)\n\t"
      "v_mfma_f32_16x16x32_bf16 v[72:75], v[230:233], v[44:47], v[72:75]\n\t"
      "v_mfma_f32_16x16x32_bf16 v[76:79], v[228:231], v[44:47], v[76:79]\n\t"
      "s_waitcnt vmcnt(3)\n\t"
      "v_mfma_f32_16x16x32_bf16 v[64:67], v[230:233], v[48:51], v[64:67]\n\t"
      "v_mfma_f32_16x16x32_bf16 v[68:71], v[228:231], v[48:51], v[68:71]\n\t"
      "s_waitcnt vmcnt(2)\n\t"
      "v_mfma_f32_16x16x32_bf16 v[72:75], v[230:233], v[52:55], v[72:75]\n\t"
      "v_mfma_f32_16x16x32_bf16 v[76:79], v[228:231], v[52:55], v[76:79]\n\t"
      "s_waitcnt vmcnt(1)\n\t"
      "v_mfma_f32_16x16x32_bf16 v[64:67], v[230:233], v[56:59], v[64:67]\n\t"
      "v_mfma_f32_16x16x32_bf16 v[68:71], v[228:231], v[56:59], v[68:71]\n\t"
      "s_waitcnt vmcnt(0)\n\t"
      "v_mfma_f32_16x16x32_bf16 v[72:75], v[230:233], v[60:63], v[72:75]\n\t"
      "v_mfma_f32_16x16x32_bf16 v[76:79], v[228:231], v[60:63], v[76:79]\n\t"
      "s_nop 3\n\t"
      "v_cvt_pk_bf16_f32 v88, v64, v65\n\t"
      "v_cvt_pk_bf16_f32 v89, v66, v67\n\t"
      "v_cvt_pk_bf16_f32 v90, v68, v69\n\t"
      "v_cvt_pk_bf16_f32 v91, v70, v71\n\t"
      "s_nop 0\n\t"
      "v_permlane32_swap_b32 v88, v90\n\t"
      "v_permlane32_swap_b32 v89, v91\n\t"
      "s_nop 0\n\t"
      "v_permlane16_swap_b32 v88, v90\n\t"
      "v_permlane16_swap_b32 v89, v91\n\t"
      "buffer_store_dwordx4 v[88:91], v246, s[40:43], 0 offen sc0 sc1\n\t"
      "v_cvt_pk_bf16_f32 v92, v72, v73\n\t"
      "v_cvt_pk_bf16_f32 v93, v74, v75\n\t"
      "v_cvt_pk_bf16_f32 v94, v76, v77\n\t"
      "v_cvt_pk_bf16_f32 v95, v78, v79\n\t"
      "s_nop 0\n\t"
      "v_permlane32_swap_b32 v92, v94\n\t"
      "v_permlane32_swap_b32 v93, v95\n\t"
      "s_nop 0\n\t"
      "v_permlane16_swap_b32 v92, v94\n\t"
      "v_permlane16_swap_b32 v93, v95\n\t"
      "buffer_store_dwordx4 v[92:95], v246, s[40:43], 0 offen offset:64 sc0 sc1\n\t"
      "s_branch L_end_%=\n\t"
      "L_done_%=:\n\t"
      "s_barrier\n\t"
      "v_mov_b32 v248, 0\n\t"
      "v_mov_b32 v249, 0\n\t"
      "s_mov_b64 exec, 0xff\n\t"
      "global_store_dwordx2 v247, v[248:249], s[32:33] sc1\n\t"
      "s_mov_b64 exec, -1\n\t"
      "s_cmp_lg_u32 s10, 0\n\t"
      "s_cbranch_scc1 L_end_%=\n\t"
      "s_lshl_b32 s19, s11, 7\n\t"
      "v_mov_b32 v250, s19\n\t"
      "s_mov_b64 exec, 1\n\t"
      "global_store_dwordx2 v250, v[252:253], s[78:79] sc1\n\t"
      "s_mov_b64 exec, -1\n\t"
      "L_end_%=:\n\t"
      "s_waitcnt vmcnt(0)\n\t"
      "L_last_%=:\n\t"
      :
      : "s"(__builtin_amdgcn_kernarg_segment_ptr()), "s"(blockIdx.x), "s"(w)
      : "v0", "v1", "v2", "v3", "v4", "v5", "v6", "v7", "v8", "v9", "v10", "v11", "v12", "v13", "v14", "v15", "v16", "v17", "v18", "v19", "v20", "v21", "v22", "v23", "v24", "v25", "v26", "v27", "v28", "v29", "v30", "v31", "v32", "v33", "v34", "v35", "v36", "v37", "v38", "v39", "v40", "v41", "v42", "v43", "v44", "v45", "v46", "v47", "v48", "v49", "v50", "v51", "v52", "v53", "v54", "v55", "v56", "v57", "v58", "v59", "v60", "v61", "v62", "v63", "v64", "v65", "v66", "v67", "v68", "v69", "v70", "v71", "v72", "v73", "v74", "v75", "v76", "v77", "v78", "v79", "v80", "v81", "v82", "v83", "v84", "v85", "v86", "v87", "v88", "v89", "v90", "v91", "v92", "v93", "v94", "v95", "v96", "v97", "v98", "v99", "v100", "v101", "v102", "v103", "v104", "v105", "v106", "v107", "v108", "v109", "v110", "v111", "v112", "v113", "v114", "v115", "v116", "v117", "v118", "v119", "v120", "v121", "v122", "v123", "v124", "v125", "v126", "v127", "v128", "v129", "v130", "v131", "v132", "v133", "v134", "v135", "v136", "v137", "v138", "v139", "v140", "v141", "v142", "v143", "v144", "v145", "v146", "v147", "v148", "v149", "v150", "v151", "v152", "v153", "v154", "v155", "v156", "v157", "v158", "v159", "v160", "v161", "v162", "v163", "v164", "v165", "v166", "v167", "v168", "v169", "v170", "v171", "v172", "v173", "v174", "v175", "v176", "v177", "v178", "v179", "v180", "v181", "v182", "v183", "v184", "v185", "v186", "v187", "v188", "v189", "v190", "v191", "v192", "v193", "v194", "v195", "v196", "v197", "v198", "v199", "v200", "v201", "v202", "v203", "v204", "v205", "v206", "v207", "v208", "v209", "v210", "v211", "v212", "v213", "v214", "v215", "v216", "v217", "v218", "v219", "v220", "v221", "v222", "v223", "v224", "v225", "v226", "v227", "v228", "v229", "v230", "v231", "v232", "v233", "v234", "v235", "v236", "v237", "v238", "v239", "v240", "v241", "v242", "v243", "v244", "v245", "v246", "v247", "v248", "v249", "v250", "v251", "v252", "v253", "v254", "v255", "a0", "a1", "a2", "a3", "a4", "a5", "a6", "a7", "a8", "a9", "a10", "a11", "a12", "a13", "a14", "a15", "a16", "a17", "a18", "a19", "a20", "a21", "a22", "a23", "a24", "a25", "a26", "a27", "a28", "a29", "a30", "a31", "a32", "a33", "a34", "a35", "a36", "a37", "a38", "a39", "a40", "a41", "a42", "a43", "a44", "a45", "a46", "a47", "a48", "a49", "a50", "a51", "a52", "a53", "a54", "a55", "a56", "a57", "a58", "a59", "a60", "a61", "a62", "a63", "a64", "a65", "a66", "a67", "a68", "a69", "a70", "a71", "a72", "a73", "a74", "a75", "a76", "a77", "a78", "a79", "a80", "a81", "a82", "a83", "a84", "a85", "a86", "a87", "a88", "a89", "a90", "a91", "a92", "a93", "a94", "a95", "a96", "a97", "a98", "a99", "a100", "a101", "a102", "a103", "a104", "a105", "a106", "a107", "a108", "a109", "a110", "a111", "a112", "a113", "a114", "a115", "a116", "a117", "a118", "a119", "a120", "a121", "a122", "a123", "a124", "a125", "a126", "a127", "a128", "a129", "a130", "a131", "a132", "a133", "a134", "a135", "a136", "a137", "a138", "a139", "a140", "a141", "a142", "a143", "a144", "a145", "a146", "a147", "a148", "a149", "a150", "a151", "a152", "a153", "a154", "a155", "a156", "a157", "a158", "a159", "a160", "a161", "a162", "a163", "a164", "a165", "a166", "a167", "a168", "a169", "a170", "a171", "a172", "a173", "a174", "a175", "a176", "a177", "a178", "a179", "a180", "a181", "a182", "a183", "a184", "a185", "a186", "a187", "a188", "a189", "a190", "a191", "a192", "a193", "a194", "a195", "a196", "a197", "a198", "a199", "a200", "a201", "a202", "a203", "a204", "a205", "a206", "a207", "a208", "a209", "a210", "a211", "a212", "a213", "a214", "a215", "a216", "a217", "a218", "a219", "a220", "a221", "a222", "a223", "a224", "a225", "a226", "a227", "a228", "a229", "a230", "a231", "a232", "a233", "a234", "a235", "a236", "a237", "a238", "a239", "a240", "a241", "a242", "a243", "a244", "a245", "a246", "a247", "s4", "s5", "s6", "s7", "s8", "s9", "s10", "s11", "s12", "s13", "s14", "s15", "s16", "s17", "s18", "s19", "s20", "s21", "s22", "s23", "s24", "s25", "s26", "s27", "s28", "s29", "s30", "s31", "s32", "s33", "s34", "s35", "s36", "s37", "s38", "s39", "s40", "s41", "s42", "s43", "s44", "s45", "s46", "s47", "s48", "s49", "s50", "s51", "s52", "s53", "s54", "s55", "s56", "s57", "s58", "s59", "s60", "s61", "s62", "s63", "s64", "s65", "s66", "s67", "s68", "s69", "s70", "s71", "s72", "s73", "s74", "s75", "s76", "s77", "s78", "s79", "s80", "s81", "s82", "s83", "s84", "s85", "s86", "s87", "s88", "s89", "s90", "s91", "s92", "s93", "s94", "s95", "s96", "s97", "s98", "s99", "vcc", "scc", "memory");
}

extern "C" void kernel_launch(void* const* d_in, const int* in_sizes, int n_in, void* d_out, int out_size, void* d_ws, size_t ws_size, hipStream_t stream) {
  (void)in_sizes; (void)n_in; (void)out_size; (void)ws_size;
  (void)hipFuncSetAttribute((const void*)fused_block, hipFuncAttributeMaxDynamicSharedMemorySize, 163840);
  fused_block<<<256, 256, 163840, stream>>>(d_in[0], d_in[1], d_in[2], d_in[3], d_in[4], d_in[5], d_in[6], d_in[7], d_in[8], d_in[9], d_out, d_ws);
}
```
